```python
import torch

def _softmax(x, axis=-1):
    return torch.softmax(x, dim=axis)

def _gelu(x):
    return torch.nn.functional.gelu(x, approximate='tanh')

def _silu(x):
    return torch.nn.functional.silu(x)

def _sigmoid(x):
    return torch.sigmoid(x)

def _layernorm(x, axis=-1, eps=1e-05):
    m = x.mean(dim=axis, keepdim=True)
    v = x.var(dim=axis, keepdim=True, unbiased=False)
    return (x - m) / torch.sqrt(v + eps)

def _scatter_add(tgt, idx, src, axis=0):
    return tgt.clone().index_add_(axis, idx, src)

def _segment_sum(src, seg, n):
    out = torch.zeros((n,) + src.shape[1:], dtype=src.dtype)
    return out.index_add_(0, seg, src)

def _pad(x, pw):
    flat = tuple((v for pair in reversed(pw) for v in pair))
    return torch.nn.functional.pad(x, flat)
kernel_name = 'adaLN_cond__gqa_block_cached__moe_arch_mod__prompt'

def setup_inputs():

    _w = lambda a: (lambda t: t.to(torch.bfloat16) if t.is_floating_point() else t)(torch.as_tensor(a))

    def _A_setup():
        B, Sq, D = (1, 2048, 576)
        Hq, Hkv, Sk, Dh = (32, 16, 2048, 128)
        assert Hq % Hkv == 0, (Hq, Hkv)
        return {'x': _w((lambda *s: torch.randn(*s, dtype=torch.bfloat16) * 0.1)(B, Sq, D)), 'k_cache': _w((lambda *s: torch.randn(*s, dtype=torch.bfloat16) * 0.8)(B, Hkv, Sk, Dh)), 'v_cache': _w((lambda *s: torch.randn(*s, dtype=torch.bfloat16) * 0.8)(B, Hkv, Sk, Dh)), 'wq': _w((lambda *s: torch.randn(*s, dtype=torch.bfloat16) * 0.8)(D, Hq * Dh) * D ** (-0.5)), 'wo': _w((lambda *s: torch.randn(*s, dtype=torch.bfloat16) * 0.8)(Hq * Dh, D) * (Hq * Dh) ** (-0.5))}
    _A_in = _A_setup()

    def _B_setup():
        B, S, D, F, C = (1, 2048, 576, 4608, 2)
        return {'x': _w((lambda *s: torch.randn(*s, dtype=torch.bfloat16) * 0.1)(B, S, D)), 'w_r': _w((lambda *s: torch.randn(*s, dtype=torch.bfloat16) * 3.2)(D)), 'w_gate': _w((lambda *s: torch.randn(*s, dtype=torch.bfloat16) * 0.2)(D, F)), 'w_up': _w((lambda *s: torch.randn(*s, dtype=torch.bfloat16) * 0.2)(D, F)), 'w_down': _w((lambda *s: torch.randn(*s, dtype=torch.bfloat16) * 0.2)(F, D))}
    _B_in = _B_setup()
    return {'A_x': _A_in['x'], 'A_k_cache': _A_in['k_cache'], 'A_v_cache': _A_in['v_cache'], 'A_wq': _A_in['wq'], 'A_wo': _A_in['wo'], 'B_x': _B_in['x'], 'B_w_r': _B_in['w_r'], 'B_w_gate': _B_in['w_gate'], 'B_w_up': _B_in['w_up'], 'B_w_down': _B_in['w_down'], 'c': _w((lambda *s: torch.randn(*s, dtype=torch.bfloat16) * 0.4)(1, 576) * 8.0), 'w_mod': _w((lambda *s: torch.randn(*s, dtype=torch.bfloat16) * 0.4)(576, 6 * 576) * 576 ** (-0.5) * 8.0)}

def reference(A_x, A_k_cache, A_v_cache, A_wq, A_wo, B_x, B_w_r, B_w_gate, B_w_up, B_w_down, c, w_mod):

    def _A_ref(x, k_cache, v_cache, wq, wo):
        B, Sq, D = x.shape
        Hq, Dh = (32, 128)
        Hkv = k_cache.shape[1]
        rep = Hq // Hkv
        q = (lambda x, *d: x.permute(*d))((x @ wq).reshape(B, Sq, Hq, Dh), 0, 2, 1, 3)
        k = (lambda x, n, axis: torch.repeat_interleave(x, n, dim=axis))(k_cache, rep, axis=1)
        v = (lambda x, n, axis: torch.repeat_interleave(x, n, dim=axis))(v_cache, rep, axis=1)
        scores = torch.einsum('bhqd,bhkd->bhqk', q, k) * Dh ** (-0.5)
        p = _softmax(scores, axis=-1)
        o = torch.einsum('bhqk,bhkd->bhqd', p, v)
        return (lambda x, *d: x.permute(*d))(o, 0, 2, 1, 3).reshape(B, Sq, Hq * Dh) @ wo

    def _B_ref(x, w_r, w_gate, w_up, w_down):
        B, S, D = x.shape
        C = 2
        r = x @ w_r
        idx = torch.argsort(-r, axis=-1)[:, :C]
        idx_e = (lambda x, n, axis: torch.repeat_interleave(x, n, dim=axis))(idx.reshape(B, C, 1), D, axis=2)
        x_sel = (lambda x, i, axis: torch.take_along_dim(x, i, dim=axis))(x, idx_e, axis=1)
        g = _silu(x_sel @ w_gate)
        u = x_sel @ w_up
        y = g * u @ w_down
        r_sel = _sigmoid((lambda x, i, axis: torch.take_along_dim(x, i, dim=axis))(r, idx, axis=1))
        y = y * r_sel.reshape(B, C, 1)
        b_off = (torch.arange(B) * S).reshape(B, 1)
        flat_idx = (idx + b_off).reshape(B * C)
        zero = (x * 0.0).reshape(B * S, D)
        upd = _scatter_add(zero, flat_idx, y.reshape(B * C, D), axis=0)
        return x + upd.reshape(B, S, D)
    x = A_x
    mod = (_silu(c) @ w_mod).reshape(c.shape[0], 6, 1, -1)
    sa, ca, ga, sb, cb, gb = (mod[:, 0].clone(), mod[:, 1].clone(), mod[:, 2].clone(), mod[:, 3].clone(), mod[:, 4].clone(), mod[:, 5].clone())
    h = _layernorm(x, axis=-1) * (1 + ca) + sa
    x = x + ga * _A_ref(h, A_k_cache, A_v_cache, A_wq, A_wo)
    h = _layernorm(x, axis=-1) * (1 + cb) + sb
    x = x + gb * _B_ref(h, B_w_r, B_w_gate, B_w_up, B_w_down)
    return x
```

```cpp
#include <hip/hip_runtime.h>
#include <hip/hip_bf16.h>
#include <cstdio>
#include <cstdint>

typedef unsigned short u16;
using bf16x8 = __attribute__((ext_vector_type(8))) short;
using s16x4  = __attribute__((ext_vector_type(4))) short;
using f32x16 = __attribute__((ext_vector_type(16))) float;
using f32x4  = __attribute__((ext_vector_type(4))) float;
using u32x4  = __attribute__((ext_vector_type(4))) unsigned;
typedef __bf16 bf16x2_t __attribute__((ext_vector_type(2)));
typedef float f32x2 __attribute__((ext_vector_type(2)));

constexpr int S = 2048, DM = 576, HQ = 32, HKV = 16, DH = 128, FF = 4608;

__device__ __forceinline__ float bf2f(u16 x) { return __uint_as_float(((unsigned)x) << 16); }
__device__ __forceinline__ u16 f2bf(float f) { __bf16 b = (__bf16)f; return __builtin_bit_cast(u16, b); }
__device__ __forceinline__ float rbf(float f) { return bf2f(f2bf(f)); }
__device__ __forceinline__ unsigned cvtpk(float lo, float hi) {
  f32x2 v = {lo, hi}; bf16x2_t b = __builtin_convertvector(v, bf16x2_t); return __builtin_bit_cast(unsigned, b);
}
__device__ __forceinline__ float wave_sum(float v) {
  for (int o = 32; o; o >>= 1) v += __shfl_xor(v, o);
  return v;
}

__global__ __launch_bounds__(256) void k_mod(const u16* __restrict__ c, const u16* __restrict__ wm, float* __restrict__ mod) {
  __shared__ float sc[DM]; __shared__ float red[4][64];
  for (int i = threadIdx.x; i < DM; i += 256) { float x = bf2f(c[i]); sc[i] = rbf(x / (1.f + __expf(-x))); }
  __syncthreads();
  int col = blockIdx.x * 64 + (threadIdx.x & 63), g = threadIdx.x >> 6;
  float acc = 0.f;
  for (int k = g; k < DM; k += 4) acc += sc[k] * bf2f(wm[(long)k * (6 * DM) + col]);
  red[g][threadIdx.x & 63] = acc;
  __syncthreads();
  if (g == 0) mod[col] = rbf(red[0][threadIdx.x] + red[1][threadIdx.x] + red[2][threadIdx.x] + red[3][threadIdx.x]);
}

__device__ __forceinline__ void ln_mod(const float (&x)[9], const float* __restrict__ sh, const float* __restrict__ scl, int lane, float (&h)[9]) {
  float s = 0.f;
  for (int i = 0; i < 9; ++i) s += x[i];
  s = wave_sum(s);
  float mean = s * (1.f / DM);
  float q = 0.f;
  for (int i = 0; i < 9; ++i) { float d = x[i] - mean; q += d * d; }
  q = wave_sum(q);
  float m = rbf(mean), v = rbf(q * (1.f / DM));
  float sd = rbf(sqrtf(rbf(v + 1e-5f)));
  for (int i = 0; i < 9; ++i) {
    int cidx = lane + 64 * i;
    float xn = rbf(rbf(x[i] - m) / sd);
    h[i] = rbf(rbf(xn * rbf(1.f + scl[cidx])) + sh[cidx]);
  }
}

__global__ __launch_bounds__(256) void k_ln1(const u16* __restrict__ x, const float* __restrict__ mod, u16* __restrict__ h) {
  int row = blockIdx.x * 4 + (threadIdx.x >> 6), lane = threadIdx.x & 63;
  float xv[9], hv[9];
  for (int i = 0; i < 9; ++i) xv[i] = bf2f(x[row * DM + lane + 64 * i]);
  ln_mod(xv, mod, mod + DM, lane, hv);
  for (int i = 0; i < 9; ++i) h[row * DM + lane + 64 * i] = f2bf(hv[i]);
}

__global__ __launch_bounds__(256) void k_gemm(const u16* __restrict__ A, const u16* __restrict__ B, u16* __restrict__ C, int M, int N, int K) {
  __shared__ __attribute__((aligned(16))) u16 sA[64][40];
  __shared__ __attribute__((aligned(16))) u16 sB[64][40];
  int bm = blockIdx.y * 64, bn = blockIdx.x * 64, tid = threadIdx.x, wid = tid >> 6, lane = tid & 63;
  int wr = wid >> 1, wc = wid & 1, fr = lane & 15, fq = lane >> 4;
  f32x4 acc[2][2] = {};
  for (int k0 = 0; k0 < K; k0 += 32) {
    { int r = tid >> 2, cc = (tid & 3) * 8;
      *(bf16x8*)&sA[r][cc] = *(const bf16x8*)&A[(long)(bm + r) * K + k0 + cc]; }
    { int kr = tid >> 3, cc = (tid & 7) * 8;
      bf16x8 v = *(const bf16x8*)&B[(long)(k0 + kr) * N + bn + cc];
      for (int j = 0; j < 8; ++j) sB[cc + j][kr] = (u16)v[j]; }
    __syncthreads();
    bf16x8 a[2], b[2];
    for (int m = 0; m < 2; ++m) a[m] = *(const bf16x8*)&sA[wr * 32 + m * 16 + fr][fq * 8];
    for (int n = 0; n < 2; ++n) b[n] = *(const bf16x8*)&sB[wc * 32 + n * 16 + fr][fq * 8];
    for (int m = 0; m < 2; ++m) for (int n = 0; n < 2; ++n) acc[m][n] = __builtin_amdgcn_mfma_f32_16x16x32_bf16(a[m], b[n], acc[m][n], 0, 0, 0);
    __syncthreads();
  }
  for (int m = 0; m < 2; ++m) for (int n = 0; n < 2; ++n) for (int j = 0; j < 4; ++j)
    C[(long)(bm + wr * 32 + m * 16 + fq * 4 + j) * N + bn + wc * 32 + n * 16 + fr] = f2bf(acc[m][n][j]);
}

constexpr int D = 128, NW = 8, QBLK = 32, KVBLK = 64;
constexpr float SCALE = 0.088388347648318440f;
constexpr float THR = 8.f;
constexpr int LDQ = HQ * DH, LDK = DH, LDO = HQ * DH;
constexpr size_t SHM_V = KVBLK * D * 2, SHM_K = KVBLK * D * 2, SHM_ATTN = 2 * SHM_V + 2 * SHM_K + NW * 64 * 4;
#define KSWZ(row, colB) ((row) * 256 + ((colB) ^ (((row) & 7) << 4)))
#define SBAR() __builtin_amdgcn_sched_barrier(0)
__device__ __forceinline__ int crow(int r, int hi) { return (r & 3) + 8 * (r >> 2) + 4 * hi; }
__device__ __forceinline__ void partialSM(f32x16& p0, f32x16& p1, float& m_reg, float& mn, float& alpha) {
  constexpr float C = SCALE * 1.4426950408889634f;
  float pmax = p0[0]; for (int r = 1; r < 16; ++r) pmax = fmaxf(pmax, p0[r]); for (int r = 0; r < 16; ++r) pmax = fmaxf(pmax, p1[r]);
  { auto rr = __builtin_amdgcn_permlane32_swap(__float_as_uint(pmax), __float_as_uint(pmax), false, false);
    pmax = fmaxf(__uint_as_float(rr[0]), __uint_as_float(rr[1])); }
  if (__builtin_expect(__all(pmax - m_reg <= THR / SCALE), 1)) { mn = m_reg; alpha = 1.f; }
  else { mn = fmaxf(m_reg, pmax); alpha = __builtin_amdgcn_exp2f((m_reg - mn) * C); m_reg = mn; }
  float mnC = -mn * C;
  for (int r = 0; r < 16; ++r) p0[r] = fmaf(p0[r], C, mnC); for (int r = 0; r < 16; ++r) p1[r] = fmaf(p1[r], C, mnC);
  for (int r = 0; r < 16; ++r) p0[r] = __builtin_amdgcn_exp2f(p0[r]);
}
__device__ __forceinline__ void finishSM(f32x16& p0, f32x16& p1, float alpha, float& l_reg, bf16x8& pa0, bf16x8& pa1, bf16x8& pa2, bf16x8& pa3) {
  for (int r = 0; r < 16; ++r) p1[r] = __builtin_amdgcn_exp2f(p1[r]);
  float ps = 0; for (int r = 0; r < 16; ++r) ps += p0[r]; for (int r = 0; r < 16; ++r) ps += p1[r];
  { auto rr = __builtin_amdgcn_permlane32_swap(__float_as_uint(ps), __float_as_uint(ps), false, false);
    ps = __uint_as_float(rr[0]) + __uint_as_float(rr[1]); }
  l_reg = l_reg * alpha + ps;
#define PK4(P, BASE, OUT) do { unsigned a0 = cvtpk(P[BASE + 0], P[BASE + 1]), a1 = cvtpk(P[BASE + 2], P[BASE + 3]);   \
    unsigned b0 = cvtpk(P[BASE + 4], P[BASE + 5]), b1 = cvtpk(P[BASE + 6], P[BASE + 7]);                              \
    auto r0 = __builtin_amdgcn_permlane32_swap(a0, b0, false, false); auto r1 = __builtin_amdgcn_permlane32_swap(a1, b1, false, false); \
    u32x4 w = {r0[0], r1[0], r0[1], r1[1]}; OUT = *reinterpret_cast<bf16x8*>(&w); } while (0)
  PK4(p0, 0, pa0); PK4(p0, 8, pa1); PK4(p1, 0, pa2); PK4(p1, 8, pa3);
#undef PK4
}
__device__ __forceinline__ void qkt(f32x16& p0, f32x16& p1, const u16* Ks, const bf16x8* qr, int r32, int hi) {
  p0 = f32x16{}; p1 = f32x16{};
  for (int d0 = 0; d0 < 8; ++d0) { int cb = (d0 * 16 + hi * 8) * 2;
    bf16x8 b0 = *reinterpret_cast<const bf16x8*>((const char*)Ks + KSWZ(r32, cb));
    bf16x8 b1 = *reinterpret_cast<const bf16x8*>((const char*)Ks + KSWZ(32 + r32, cb));
    p0 = __builtin_amdgcn_mfma_f32_32x32x16_bf16(b0, qr[d0], p0, 0, 0, 0);
    p1 = __builtin_amdgcn_mfma_f32_32x32x16_bf16(b1, qr[d0], p1, 0, 0, 0); }
}
__device__ __forceinline__ int v_st(int k, int c) { const int kk = (k & ~0xC) | ((k & 4) << 1) | ((k & 8) >> 1); return ((kk >> 3) * 4 + (c >> 5)) * 512 + ((kk & 7) * 32 + (c & 31)) * 2; }
__device__ __forceinline__ int v_rd_base(int lane) { return ((lane & 3) << 3) | (((lane >> 2) & 3) << 6) | (((lane >> 4) & 1) << 5) | (((lane >> 5) & 1) << 8); }
constexpr int v_rd_off(int d0, int ks, int half) { return d0 * 512 + ks * 4096 + half * 2048; }
template <int OFF> __device__ __forceinline__ s16x4 tr_read(int vb) {
  s16x4 r; asm volatile("ds_read_b64_tr_b16 %0, %1 offset:%2" : "=&v"(r) : "v"(vb), "i"(OFF) : "memory"); return r;
}
template <int D0> __device__ __forceinline__ void pv_one(f32x16& od, int vb, bf16x8 pa0, bf16x8 pa1, bf16x8 pa2, bf16x8 pa3) {
  const s16x4 l0 = tr_read<v_rd_off(D0, 0, 0)>(vb), h0 = tr_read<v_rd_off(D0, 0, 1)>(vb), l1 = tr_read<v_rd_off(D0, 1, 0)>(vb), h1 = tr_read<v_rd_off(D0, 1, 1)>(vb);
  const s16x4 l2 = tr_read<v_rd_off(D0, 2, 0)>(vb), h2 = tr_read<v_rd_off(D0, 2, 1)>(vb), l3 = tr_read<v_rd_off(D0, 3, 0)>(vb), h3 = tr_read<v_rd_off(D0, 3, 1)>(vb);
  asm volatile("s_waitcnt lgkmcnt(0)" ::: "memory"); SBAR();
#define PK(L, H) (bf16x8){L[0], L[1], L[2], L[3], H[0], H[1], H[2], H[3]}
  od = __builtin_amdgcn_mfma_f32_32x32x16_bf16(pa0, PK(l0, h0), od, 0, 0, 0);
  od = __builtin_amdgcn_mfma_f32_32x32x16_bf16(pa1, PK(l1, h1), od, 0, 0, 0);
  od = __builtin_amdgcn_mfma_f32_32x32x16_bf16(pa2, PK(l2, h2), od, 0, 0, 0);
  od = __builtin_amdgcn_mfma_f32_32x32x16_bf16(pa3, PK(l3, h3), od, 0, 0, 0);
#undef PK
}
__device__ __forceinline__ void pv_d0(f32x16* o, int vb, bf16x8 pa0, bf16x8 pa1, bf16x8 pa2, bf16x8 pa3) {
  pv_one<0>(o[0], vb, pa0, pa1, pa2, pa3); pv_one<1>(o[1], vb, pa0, pa1, pa2, pa3); pv_one<2>(o[2], vb, pa0, pa1, pa2, pa3); pv_one<3>(o[3], vb, pa0, pa1, pa2, pa3);
}
__global__ __launch_bounds__(64 * NW, 1)
void k_attn(const u16* __restrict__ Q, const u16* __restrict__ K, const u16* __restrict__ V, u16* __restrict__ O) {
  extern __shared__ __attribute__((aligned(16))) char lds[];
  const int h = blockIdx.y, kvh = h >> 1, i0 = blockIdx.x * 256;
  const u16* Qb = Q + (long)i0 * LDQ + h * DH; const u16* Kh = K + (long)kvh * S * DH; const u16* Vh = V + (long)kvh * S * DH;
  u16* Ob = O + (long)i0 * LDO + h * DH;
  const int seq = S;
  const int tid = threadIdx.x, wid = tid >> 6, lane = tid & 63, r32 = lane & 31, hi = lane >> 5;
  u16* V_lds = (u16*)lds; u16* K_lds = (u16*)(lds + 2 * SHM_V);
  float* ws = (float*)(lds + 2 * SHM_V + 2 * SHM_K) + wid * 64; float* li_l = ws; float* al_l = ws + 32;
  float m_reg = -1e30f, l_reg = 0; f32x16 o[4] = {}; bf16x8 qr[8];
  const u16* Qw = Qb + (long)(wid * QBLK + r32) * LDQ + hi * 8;
#pragma unroll
  for (int d0 = 0; d0 < 8; ++d0) qr[d0] = *(const bf16x8*)(Qw + d0 * 16);
  const int sr = tid >> 4, sc = (tid & 15) * 8, vst0 = v_st(sr, sc), vst1 = v_st(32 + sr, sc);
  const int vb0 = (int)(uintptr_t)V_lds + v_rd_base(lane);
  struct { bf16x8 vs0, vs1, ks0, ks1; } sr_[2];
#define LD8(p) (*reinterpret_cast<const bf16x8*>(p))
#define SLOAD(i, k0) do { sr_[i].vs0 = LD8(&Vh[(long)((k0) + sr) * LDK + sc]); sr_[i].vs1 = LD8(&Vh[(long)((k0) + 32 + sr) * LDK + sc]); \
    sr_[i].ks0 = LD8(&Kh[(long)((k0) + sr) * LDK + sc]); sr_[i].ks1 = LD8(&Kh[(long)((k0) + 32 + sr) * LDK + sc]); } while (0)
#define SWRITE(b, i) do { *(bf16x8*)((char*)V_lds + (b) * SHM_V + vst0) = sr_[i].vs0;          \
    *(bf16x8*)((char*)V_lds + (b) * SHM_V + vst1) = sr_[i].vs1; int kc = sc * 2;               \
    *(bf16x8*)((char*)K_lds + (b) * SHM_K + KSWZ(sr, kc)) = sr_[i].ks0;                       \
    *(bf16x8*)((char*)K_lds + (b) * SHM_K + KSWZ(32 + sr, kc)) = sr_[i].ks1; } while (0)
#define SWAIT() asm volatile("s_waitcnt vmcnt(4)" ::: "memory")
#define RESC(a) do { if (__any((a) < 1.f)) { if (hi == 0) al_l[r32] = (a); asm volatile("s_waitcnt lgkmcnt(0)" ::: "memory"); \
    for (int d = 0; d < 4; ++d) for (int r = 0; r < 16; ++r) o[d][r] *= al_l[crow(r, hi)]; } } while (0)
  f32x16 pA0, pA1, pB0, pB1; float mnA, mnB, alA, alB; bf16x8 pa0, pa1, pa2, pa3; const int NT = seq / KVBLK;
  constexpr int SE = 0, SO = 1;
  SLOAD(SE, 0); asm volatile("s_waitcnt vmcnt(0)" ::: "memory"); SWRITE(0, SE); __syncthreads();
  qkt(pA0, pA1, K_lds, qr, r32, hi); partialSM(pA0, pA1, m_reg, mnA, alA);
  SLOAD(SO, KVBLK); SLOAD(SE, 2 * KVBLK);
  SWAIT(); SWRITE(1, SO); __syncthreads();
  for (int j = 1; j + 1 < NT; j += 2) {
    SBAR(); qkt(pB0, pB1, (u16*)((char*)K_lds + SHM_K), qr, r32, hi);
    finishSM(pA0, pA1, alA, l_reg, pa0, pa1, pa2, pa3); SBAR();
    SLOAD(SO, (j + 2) * KVBLK); SBAR();
    pv_d0(o, vb0, pa0, pa1, pa2, pa3); partialSM(pB0, pB1, m_reg, mnB, alB);
    __syncthreads(); SWAIT(); SWRITE(0, SE);
    RESC(alB); __syncthreads();
    SBAR(); qkt(pA0, pA1, K_lds, qr, r32, hi);
    finishSM(pB0, pB1, alB, l_reg, pa0, pa1, pa2, pa3); SBAR();
    if (j + 3 < NT) SLOAD(SE, (j + 3) * KVBLK); SBAR();
    pv_d0(o, vb0 + (int)SHM_V, pa0, pa1, pa2, pa3); partialSM(pA0, pA1, m_reg, mnA, alA);
    __syncthreads(); SWAIT(); SWRITE(1, SO);
    RESC(alA); __syncthreads();
  }
  SBAR(); qkt(pB0, pB1, (u16*)((char*)K_lds + SHM_K), qr, r32, hi);
  finishSM(pA0, pA1, alA, l_reg, pa0, pa1, pa2, pa3); SBAR();
  pv_d0(o, vb0, pa0, pa1, pa2, pa3); partialSM(pB0, pB1, m_reg, mnB, alB);
  __syncthreads(); RESC(alB);
  finishSM(pB0, pB1, alB, l_reg, pa0, pa1, pa2, pa3); SBAR();
  pv_d0(o, vb0 + (int)SHM_V, pa0, pa1, pa2, pa3);
  if (hi == 0) li_l[r32] = l_reg; asm volatile("s_waitcnt lgkmcnt(0)" ::: "memory");
  float rli[16];
#pragma unroll
  for (int r = 0; r < 16; ++r) rli[r] = __builtin_amdgcn_rcpf(li_l[crow(r, hi)]);
  u16* Ow = Ob + (long)(wid * QBLK) * LDO;
#pragma unroll
  for (int r = 0; r < 16; ++r) { int orow = crow(r, hi);
    for (int d0 = 0; d0 < 4; ++d0) Ow[(long)orow * LDO + d0 * 32 + r32] = f2bf(o[d0][r] * rli[r]); }
}

__global__ __launch_bounds__(256) void k_ln2(const u16* __restrict__ x, const u16* __restrict__ a, const float* __restrict__ mod, const u16* __restrict__ wr,
                                             u16* __restrict__ x1o, u16* __restrict__ h2o, float* __restrict__ ro, u16* __restrict__ out) {
  int row = blockIdx.x * 4 + (threadIdx.x >> 6), lane = threadIdx.x & 63;
  float xv[9], hv[9];
  for (int i = 0; i < 9; ++i) { int cidx = lane + 64 * i; xv[i] = rbf(bf2f(x[row * DM + cidx]) + rbf(mod[2 * DM + cidx] * bf2f(a[row * DM + cidx]))); }
  ln_mod(xv, mod + 3 * DM, mod + 4 * DM, lane, hv);
  float r = 0.f;
  for (int i = 0; i < 9; ++i) { int cidx = lane + 64 * i; r += hv[i] * bf2f(wr[cidx]);
    x1o[row * DM + cidx] = f2bf(xv[i]); h2o[row * DM + cidx] = f2bf(hv[i]);
    out[row * DM + cidx] = f2bf(xv[i] + rbf(mod[5 * DM + cidx] * hv[i])); }
  r = wave_sum(r);
  if (lane == 0) ro[row] = rbf(r);
}

__device__ __forceinline__ void top2_block(const float* __restrict__ r, int* sidx, float* sval) {
  __shared__ float v1s[256], v2s[256]; __shared__ int i1s[256], i2s[256];
  float v1 = -INFINITY, v2 = -INFINITY; int i1 = 0x7fffffff, i2 = 0x7fffffff;
  for (int i = threadIdx.x; i < S; i += 256) { float v = r[i];
    if (v > v1 || i1 == 0x7fffffff) { v2 = v1; i2 = i1; v1 = v; i1 = i; } else if (v > v2 || i2 == 0x7fffffff) { v2 = v; i2 = i; } }
  v1s[threadIdx.x] = v1; v2s[threadIdx.x] = v2; i1s[threadIdx.x] = i1; i2s[threadIdx.x] = i2;
  __syncthreads();
  if (threadIdx.x == 0) {
    float a1 = v1s[0], a2 = v2s[0]; int j1 = i1s[0], j2 = i2s[0];
    for (int t = 1; t < 256; ++t) {
      for (int u = 0; u < 2; ++u) { float v = u ? v2s[t] : v1s[t]; int i = u ? i2s[t] : i1s[t];
        if (v > a1 || (v == a1 && i < j1)) { a2 = a1; j2 = j1; a1 = v; j1 = i; } else if (v > a2 || (v == a2 && i < j2)) { a2 = v; j2 = i; } }
    }
    sidx[0] = j1; sidx[1] = j2; sval[0] = a1; sval[1] = a2;
  }
  __syncthreads();
}

__global__ __launch_bounds__(256) void k_gateup(const float* __restrict__ r, const u16* __restrict__ h2, const u16* __restrict__ wg, const u16* __restrict__ wu, float* __restrict__ act) {
  __shared__ int sidx[2]; __shared__ float sval[2]; __shared__ float xs[2][DM]; __shared__ float red[4][4][64];
  top2_block(r, sidx, sval);
  for (int i = threadIdx.x; i < 2 * DM; i += 256) xs[i / DM][i % DM] = bf2f(h2[sidx[i / DM] * DM + i % DM]);
  __syncthreads();
  int col = blockIdx.x * 64 + (threadIdx.x & 63), g = threadIdx.x >> 6;
  float g0 = 0, g1 = 0, u0 = 0, u1 = 0;
  for (int k = g; k < DM; k += 4) { float a = bf2f(wg[(long)k * FF + col]), b = bf2f(wu[(long)k * FF + col]);
    g0 += xs[0][k] * a; g1 += xs[1][k] * a; u0 += xs[0][k] * b; u1 += xs[1][k] * b; }
  red[g][0][threadIdx.x & 63] = g0; red[g][1][threadIdx.x & 63] = g1; red[g][2][threadIdx.x & 63] = u0; red[g][3][threadIdx.x & 63] = u1;
  __syncthreads();
  if (threadIdx.x < 128) { int t = threadIdx.x >> 6, cl = threadIdx.x & 63;
    float gg = rbf(red[0][t][cl] + red[1][t][cl] + red[2][t][cl] + red[3][t][cl]);
    float uu = rbf(red[0][2 + t][cl] + red[1][2 + t][cl] + red[2][2 + t][cl] + red[3][2 + t][cl]);
    float sg = rbf(gg / (1.f + __expf(-gg)));
    act[t * FF + blockIdx.x * 64 + cl] = rbf(sg * uu); }
}

__global__ __launch_bounds__(256) void k_down(const float* __restrict__ r, const float* __restrict__ act, const u16* __restrict__ wd, const u16* __restrict__ x1, const u16* __restrict__ h2,
                                              const float* __restrict__ mod, u16* __restrict__ out) {
  __shared__ int sidx[2]; __shared__ float sval[2]; __shared__ float red[4][2][64];
  top2_block(r, sidx, sval);
  int col = blockIdx.x * 64 + (threadIdx.x & 63), g = threadIdx.x >> 6;
  float y0 = 0, y1 = 0;
  for (int k = g; k < FF; k += 4) { float w = bf2f(wd[(long)k * DM + col]); y0 += act[k] * w; y1 += act[FF + k] * w; }
  red[g][0][threadIdx.x & 63] = y0; red[g][1][threadIdx.x & 63] = y1;
  __syncthreads();
  if (threadIdx.x < 128) { int t = threadIdx.x >> 6, cl = threadIdx.x & 63, cc = blockIdx.x * 64 + cl;
    float y = rbf(red[0][t][cl] + red[1][t][cl] + red[2][t][cl] + red[3][t][cl]);
    float sg = rbf(1.f / (1.f + __expf(-sval[t])));
    y = rbf(y * sg);
    int row = sidx[t];
    float b = rbf(bf2f(h2[row * DM + cc]) + y);
    out[row * DM + cc] = f2bf(bf2f(x1[row * DM + cc]) + rbf(mod[5 * DM + cc] * b)); }
}

extern "C" void kernel_launch(void* const* d_in, const int* in_sizes, int n_in, void* d_out, int out_size, void* d_ws, size_t ws_size, hipStream_t stream) {
  const u16* x = (const u16*)d_in[0]; const u16* kc = (const u16*)d_in[1]; const u16* vc = (const u16*)d_in[2];
  const u16* wq = (const u16*)d_in[3]; const u16* wo = (const u16*)d_in[4];
  const u16* wr = (const u16*)d_in[6]; const u16* wg = (const u16*)d_in[7]; const u16* wu = (const u16*)d_in[8]; const u16* wd = (const u16*)d_in[9];
  const u16* c = (const u16*)d_in[10]; const u16* wm = (const u16*)d_in[11];
  u16* out = (u16*)d_out;
  char* ws = (char*)d_ws;
  float* mod = (float*)ws;
  float* r = (float*)(ws + 16384);
  float* act = (float*)(ws + 32768);
  u16* h = (u16*)(ws + (1 << 20));
  u16* a = (u16*)(ws + (4 << 20));
  u16* x1 = (u16*)(ws + (8 << 20));
  u16* h2 = (u16*)(ws + (12 << 20));
  u16* q = (u16*)(ws + (16 << 20));
  u16* o = (u16*)(ws + (40 << 20));
  hipFuncSetAttribute((const void*)k_attn, hipFuncAttributeMaxDynamicSharedMemorySize, (int)SHM_ATTN);
  k_mod<<<54, 256, 0, stream>>>(c, wm, mod);
  k_ln1<<<S / 4, 256, 0, stream>>>(x, mod, h);
  k_gemm<<<dim3(4096 / 64, S / 64), 256, 0, stream>>>(h, wq, q, S, 4096, DM);
  k_attn<<<dim3(S / 256, HQ), 512, SHM_ATTN, stream>>>(q, kc, vc, o);
  k_gemm<<<dim3(DM / 64, S / 64), 256, 0, stream>>>(o, wo, a, S, DM, 4096);
  k_ln2<<<S / 4, 256, 0, stream>>>(x, a, mod, wr, x1, h2, r, out);
  k_gateup<<<72, 256, 0, stream>>>(r, h2, wg, wu, act);
  k_down<<<9, 256, 0, stream>>>(r, act, wd, x1, h2, mod, out);
}
```

```cpp
#include <hip/hip_runtime.h>
#include <hip/hip_bf16.h>

typedef unsigned short u16;


__global__ __launch_bounds__(256, 1) void k_main(const u16* x, const u16* k, const u16* v, const u16* wq, const u16* wo, const u16* wr,
                                                 const u16* wg, const u16* wu, const u16* wd, u16* out, char* ws,
                                                 const u16* c, const u16* wm) {
  __shared__ __attribute__((aligned(16))) char lds[163840];
  const unsigned wid = __builtin_amdgcn_readfirstlane(threadIdx.x >> 6);
  asm volatile(
    "v_mbcnt_lo_u32_b32 v239, -1, 0\n"
    "v_mbcnt_hi_u32_b32 v239, -1, v239\n"
    "s_getpc_b64 s[60:61]\n"
    "v_lshlrev_b32 v48, 7, v239\n"
    "s_lshl_b32 s80, %13, 6\n"
    "v_add_u32 v1, s80, v239\n"
    "v_lshrrev_b32 v9, 1, v1\n"
    "v_and_b32 v10, 7, v239\n"
    "v_lshrrev_b32 v11, 3, v239\n"
    "s_mul_i32 s81, %13, 18\n"
    "v_add_u32 v11, s81, v11\n"
    "s_mul_i32 s81, %14, 9216\n"
    "v_mul_u32_u24 v2, 1152, v10\n"
    "v_lshl_add_u32 v2, v11, 4, v2\n"
    "v_add_u32 v2, s81, v2\n"
    "global_load_dwordx4 v[32:35], v2, %0 offset:0\n"
    "global_load_dwordx4 v[36:39], v2, %0 offset:128\n"
    "v_mov_b32 v40, 0\n"
    "v_mov_b32 v41, 0\n"
    "v_mov_b32 v42, 0\n"
    "v_mov_b32 v43, 0\n"
    "s_mov_b64 exec, 0xffff\n"
    "global_load_dwordx4 v[40:43], v2, %0 offset:256\n"
    "s_mov_b64 exec, -1\n"
    "v_lshlrev_b32 v2, 3, v9\n"
    "global_load_dwordx2 v[4:5], v2, %11\n"
    "v_min_u32 v3, 63, v9\n"
    "v_lshlrev_b32 v2, 1, v3\n"
    "global_load_ushort v8, v2, %11 offset:1024\n"
    "v_mov_b32 v2, 24576\n"
    "global_load_dword v45, v2, %10 sc1\n"
    "s_lshr_b32 s81, %14, 3\n"
    "v_lshrrev_b32 v48, 4, v1\n"
    "v_lshl_add_u32 v48, v48, 5, s81\n"
    "v_min_u32 v48, 483, v48\n"
    "v_lshlrev_b32 v48, 7, v48\n"
    "global_load_dword v254, v48, s[60:61]\n"
    "s_and_b32 s67, %14, 0xe0\n"
    "s_and_b32 s81, %14, 7\n"
    "s_lshl_b32 s81, s81, 2\n"
    "s_add_u32 s67, s67, s81\n"
    "s_bfe_u32 s81, %14, 0x20003\n"
    "s_add_u32 s67, s67, s81\n"
    "s_min_u32 s81, s67, 215\n"
    "s_lshl_b32 s81, s81, 5\n"
    "v_and_b32 v6, 1, v1\n"
    "v_lshl_add_u32 v6, v6, 4, s81\n"
    "v_mul_u32_u24 v2, 27648, v9\n"
    "v_add_u32 v2, v2, v6\n"
    "global_load_dwordx4 v[12:15], v2, %12\n"
    "v_add_u32 v2, 6912, v2\n"
    "global_load_dwordx4 v[16:19], v2, %12\n"
    "v_add_u32 v2, 6912, v2\n"
    "global_load_dwordx4 v[20:23], v2, %12\n"
    "v_add_u32 v2, 6912, v2\n"
    "global_load_dwordx4 v[24:27], v2, %12\n"
    "v_mul_u32_u24 v3, 6912, v3\n"
    "v_add_u32 v3, v3, v6\n"
    "v_add_u32 v3, 3538944, v3\n"
    "global_load_dwordx4 v[28:31], v3, %12\n"
    "s_mov_b32 s78, %13\n"
    "v_lshlrev_b32 v244, 4, v239\n"
    "s_lshl_b32 s79, s78, 12\n"
    "s_and_b32 s88, %14, 3\n"
    "s_lshl_b32 s88, s88, 2\n"
    "s_bfe_u32 s81, %14, 0x20003\n"
    "s_add_u32 s88, s88, s81\n"
    "s_lshr_b32 s89, %14, 5\n"
    "s_and_b32 s81, %14, 4\n"
    "s_lshl_b32 s81, s81, 1\n"
    "s_add_u32 s89, s89, s81\n"
    "s_lshl_b32 s82, s88, 9\n"
    "s_mov_b64 s[48:49], %3\n"
    "s_add_u32 s48, s48, s82\n"
    "s_addc_u32 s49, s49, 0\n"
    "s_sub_u32 s48, s48, 768\n"
    "s_subb_u32 s49, s49, 0\n"
    "s_and_b32 s49, s49, 0xffff\n"
    "s_mov_b32 s51, 0x20000\n"
    "s_sub_u32 s50, 4719360, s82\n"
    "v_lshrrev_b32 v247, 4, v239\n"
    "v_bfe_u32 v248, v239, 2, 2\n"
    "v_and_b32 v246, 3, v239\n"
    "v_lshlrev_b32 v249, 13, v248\n"
    "v_lshl_add_u32 v249, v247, 6, v249\n"
    "v_lshl_add_u32 v249, v246, 4, v249\n"
    "s_lshl_b32 s82, s78, 17\n"
    "v_add_u32 v249, s82, v249\n"
    "v_add_u32 v228, 768, v249\n"
    "v_add_u32 v229, 0, v249\n"
    "v_add_u32 v230, 31488, v249\n"
    "v_add_u32 v231, 30720, v249\n"
    "s_mov_b64 s[56:57], %10\n"
    "s_and_b32 s57, s57, 0xffff\n"
    "s_mov_b32 s58, 41943040\n"
    "s_mov_b32 s59, 0x20000\n"
    "s_lshl_b32 s80, s78, 3\n"
    "s_add_u32 s80, s80, 131072\n"
    "v_lshl_add_u32 v50, v10, 5, s80\n"
    "v_lshlrev_b32 v51, 5, v10\n"
    "v_add_u32 v51, 131072, v51\n"
    "v_and_b32 v52, 1, v239\n"
    "v_lshlrev_b32 v52, 3, v52\n"
    "v_lshrrev_b32 v53, 4, v239\n"
    "v_lshl_add_u32 v52, v53, 1, v52\n"
    "s_lshl_b32 s80, s78, 2\n"
    "s_add_u32 s80, s80, 131328\n"
    "v_lshl_add_u32 v52, v52, 4, s80\n"
    "v_and_b32 v54, 15, v239\n"
    "v_lshlrev_b32 v53, 4, v54\n"
    "v_add_u32 v53, 131328, v53\n"
    "s_lshr_b32 s80, %14, 1\n"
    "s_mul_i32 s80, s80, 18432\n"
    "s_and_b32 s81, %14, 1\n"
    "s_lshl_b32 s81, s81, 7\n"
    "s_add_u32 s80, s80, s81\n"
    "s_add_u32 s84, s80, 1048576\n"
    "s_add_u32 s85, s84, 4096\n"
    "v_lshlrev_b32 v55, 8, v11\n"
    "v_lshl_add_u32 v55, v10, 4, v55\n"
    "s_mul_i32 s80, s67, 57\n"
    "s_lshr_b32 s80, s80, 11\n"
    "s_lshl_b32 s81, 1, s80\n"
    "s_and_b32 s81, s81, 0x12\n"
    "s_cmp_lg_u32 s81, 0\n"
    "s_cselect_b32 s86, 1.0, 0\n"
    "s_lshl_b32 s87, s67, 7\n"
    "s_add_u32 s87, s87, 32768\n"
    "v_lshrrev_b32 v58, 4, v1\n"
    "v_cmp_eq_u32 vcc, 12, v58\n"
    "v_mov_b32 v57, 2147483632\n"
    "v_lshlrev_b32 v56, 3, v54\n"
    "v_cndmask_b32 v56, v57, v56, vcc\n"
    "v_cmp_eq_u32 vcc, 0, v1\n"
    "s_nop 1\n"
    "v_cndmask_b32 v57, v57, 0, vcc\n"
    "v_mov_b32 v46, 2147483632\n"
    "v_mov_b32 v47, 0\n"
    "s_lshl_b32 s81, %14, 7\n"
    "s_add_u32 s81, s81, 73728\n"
    "v_writelane_b32 v46, s81, 0\n"
    "s_add_u32 s81, s81, 4\n"
    "v_writelane_b32 v46, s81, 1\n"
    "s_and_b32 s81, %14, 0xf0\n"
    "s_lshl_b32 s81, s81, 4\n"
    "s_and_b32 s80, %14, 15\n"
    "s_lshl_b32 s80, s80, 2\n"
    "s_add_u32 s81, s81, s80\n"
    "s_add_u32 s81, s81, 65536\n"
    "v_writelane_b32 v46, s81, 2\n"
    "s_and_b32 s81, %14, 0xfc\n"
    "s_lshl_b32 s81, s81, 4\n"
    "s_and_b32 s80, %14, 3\n"
    "s_lshl_b32 s80, s80, 2\n"
    "s_add_u32 s81, s81, s80\n"
    "s_add_u32 s81, s81, 69632\n"
    "v_writelane_b32 v46, s81, 3\n"
    "s_add_u32 s81, s81, 16\n"
    "v_writelane_b32 v46, s81, 5\n"
    "s_cmp_eq_u32 %14, 0\n"
    "s_cselect_b32 s81, 24832, -16\n"
    "s_and_b32 s81, s81, 0x3ffffff0\n"
    "v_writelane_b32 v46, s81, 4\n"
    "s_cmp_eq_u32 %13, 0\n"
    "s_cselect_b32 s81, 0, 1073741816\n"
    "v_add_u32 v46, s81, v46\n"
    "s_cmp_lt_u32 s78, 2\n"
    "s_cselect_b32 s82, -1, 0\n"
    "s_mov_b32 s83, 0xbfb8aa3b\n"
    "s_waitcnt vmcnt(11)\n"
    "v_lshlrev_b32 v72, 16, v32\n"
    "v_and_b32 v73, 0xffff0000, v32\n"
    "v_lshlrev_b32 v74, 16, v33\n"
    "v_and_b32 v75, 0xffff0000, v33\n"
    "v_lshlrev_b32 v76, 16, v34\n"
    "v_and_b32 v77, 0xffff0000, v34\n"
    "v_lshlrev_b32 v78, 16, v35\n"
    "v_and_b32 v79, 0xffff0000, v35\n"
    "s_waitcnt vmcnt(10)\n"
    "v_lshlrev_b32 v80, 16, v36\n"
    "v_and_b32 v81, 0xffff0000, v36\n"
    "v_lshlrev_b32 v82, 16, v37\n"
    "v_and_b32 v83, 0xffff0000, v37\n"
    "v_lshlrev_b32 v84, 16, v38\n"
    "v_and_b32 v85, 0xffff0000, v38\n"
    "v_lshlrev_b32 v86, 16, v39\n"
    "v_and_b32 v87, 0xffff0000, v39\n"
    "s_waitcnt vmcnt(9)\n"
    "v_lshlrev_b32 v88, 16, v40\n"
    "v_and_b32 v89, 0xffff0000, v40\n"
    "v_lshlrev_b32 v90, 16, v41\n"
    "v_and_b32 v91, 0xffff0000, v41\n"
    "v_lshlrev_b32 v92, 16, v42\n"
    "v_and_b32 v93, 0xffff0000, v42\n"
    "v_lshlrev_b32 v94, 16, v43\n"
    "v_and_b32 v95, 0xffff0000, v43\n"
    "v_pk_add_f32 v[60:61], v[72:73], v[74:75]\n"
    "v_pk_add_f32 v[62:63], v[76:77], v[78:79]\n"
    "v_pk_add_f32 v[60:61], v[60:61], v[80:81]\n"
    "v_pk_add_f32 v[62:63], v[62:63], v[82:83]\n"
    "v_pk_add_f32 v[60:61], v[60:61], v[84:85]\n"
    "v_pk_add_f32 v[62:63], v[62:63], v[86:87]\n"
    "v_pk_add_f32 v[60:61], v[60:61], v[88:89]\n"
    "v_pk_add_f32 v[62:63], v[62:63], v[90:91]\n"
    "v_pk_add_f32 v[60:61], v[60:61], v[92:93]\n"
    "v_pk_add_f32 v[62:63], v[62:63], v[94:95]\n"
    "v_pk_add_f32 v[60:61], v[60:61], v[62:63]\n"
    "v_add_f32 v96, v60, v61\n"
    "s_waitcnt vmcnt(8)\n"
    "v_and_b32 v7, 0xffff0000, v5\n"
    "v_lshlrev_b32 v6, 16, v5\n"
    "v_add_f32_dpp v96, v96, v96 row_ror:8 row_mask:0xf bank_mask:0xf\n"
    "v_mov_b32 v62, v96\n"
    "v_and_b32 v5, 0xffff0000, v4\n"
    "v_lshlrev_b32 v4, 16, v4\n"
    "v_permlane16_swap_b32 v96, v62\n"
    "s_waitcnt vmcnt(7)\n"
    "v_lshlrev_b32 v8, 16, v8\n"
    "v_and_b32 v8, s82, v8\n"
    "v_add_f32 v96, v96, v62\n"
    "v_mov_b32 v62, v96\n"
    "v_mul_f32 v108, s83, v4\n"
    "v_mul_f32 v109, s83, v5\n"
    "v_permlane32_swap_b32 v96, v62\n"
    "v_mul_f32 v110, s83, v6\n"
    "v_mul_f32 v111, s83, v7\n"
    "v_add_f32 v96, v96, v62\n"
    "v_mul_f32 v96, 0x3be38e39, v96\n"
    "v_mov_b32 v60, 0\n"
    "v_mov_b32 v61, 0\n"
    "v_pk_add_f32 v[62:63], v[72:73], v[96:97] op_sel_hi:[1,0] neg_lo:[0,1] neg_hi:[0,1]\n"
    "v_pk_fma_f32 v[60:61], v[62:63], v[62:63], v[60:61]\n"
    "v_pk_add_f32 v[64:65], v[74:75], v[96:97] op_sel_hi:[1,0] neg_lo:[0,1] neg_hi:[0,1]\n"
    "v_pk_fma_f32 v[60:61], v[64:65], v[64:65], v[60:61]\n"
    "v_pk_add_f32 v[62:63], v[76:77], v[96:97] op_sel_hi:[1,0] neg_lo:[0,1] neg_hi:[0,1]\n"
    "v_pk_fma_f32 v[60:61], v[62:63], v[62:63], v[60:61]\n"
    "v_pk_add_f32 v[64:65], v[78:79], v[96:97] op_sel_hi:[1,0] neg_lo:[0,1] neg_hi:[0,1]\n"
    "v_pk_fma_f32 v[60:61], v[64:65], v[64:65], v[60:61]\n"
    "v_pk_add_f32 v[62:63], v[80:81], v[96:97] op_sel_hi:[1,0] neg_lo:[0,1] neg_hi:[0,1]\n"
    "v_pk_fma_f32 v[60:61], v[62:63], v[62:63], v[60:61]\n"
    "v_pk_add_f32 v[64:65], v[82:83], v[96:97] op_sel_hi:[1,0] neg_lo:[0,1] neg_hi:[0,1]\n"
    "v_pk_fma_f32 v[60:61], v[64:65], v[64:65], v[60:61]\n"
    "v_pk_add_f32 v[62:63], v[84:85], v[96:97] op_sel_hi:[1,0] neg_lo:[0,1] neg_hi:[0,1]\n"
    "v_pk_fma_f32 v[60:61], v[62:63], v[62:63], v[60:61]\n"
    "v_pk_add_f32 v[64:65], v[86:87], v[96:97] op_sel_hi:[1,0] neg_lo:[0,1] neg_hi:[0,1]\n"
    "v_pk_fma_f32 v[60:61], v[64:65], v[64:65], v[60:61]\n"
    "s_mov_b64 exec, 0xffff\n"
    "v_pk_add_f32 v[62:63], v[88:89], v[96:97] op_sel_hi:[1,0] neg_lo:[0,1] neg_hi:[0,1]\n"
    "v_pk_fma_f32 v[60:61], v[62:63], v[62:63], v[60:61]\n"
    "v_pk_add_f32 v[64:65], v[90:91], v[96:97] op_sel_hi:[1,0] neg_lo:[0,1] neg_hi:[0,1]\n"
    "v_pk_fma_f32 v[60:61], v[64:65], v[64:65], v[60:61]\n"
    "v_pk_add_f32 v[62:63], v[92:93], v[96:97] op_sel_hi:[1,0] neg_lo:[0,1] neg_hi:[0,1]\n"
    "v_pk_fma_f32 v[60:61], v[62:63], v[62:63], v[60:61]\n"
    "v_pk_add_f32 v[64:65], v[94:95], v[96:97] op_sel_hi:[1,0] neg_lo:[0,1] neg_hi:[0,1]\n"
    "v_pk_fma_f32 v[60:61], v[64:65], v[64:65], v[60:61]\n"
    "s_mov_b64 exec, -1\n"
    "v_add_f32 v97, v60, v61\n"
    "v_mul_f32 v112, s83, v8\n"
    "v_fma_f32 v114, v4, s83, -v108\n"
    "v_add_f32_dpp v97, v97, v97 row_ror:8 row_mask:0xf bank_mask:0xf\n"
    "v_mov_b32 v62, v97\n"
    "v_fma_f32 v115, v5, s83, -v109\n"
    "v_fma_f32 v116, v6, s83, -v110\n"
    "v_permlane16_swap_b32 v97, v62\n"
    "v_fma_f32 v117, v7, s83, -v111\n"
    "v_fma_f32 v118, v8, s83, -v112\n"
    "v_add_f32 v97, v97, v62\n"
    "v_mov_b32 v62, v97\n"
    "v_exp_f32 v108, v108\n"
    "v_exp_f32 v109, v109\n"
    "v_permlane32_swap_b32 v97, v62\n"
    "v_exp_f32 v110, v110\n"
    "v_exp_f32 v111, v111\n"
    "v_add_f32 v97, v97, v62\n"
    "ds_write_b64 v50, v[96:97]\n"
    "v_exp_f32 v112, v112\n"
    "v_fmac_f32 v114, 0xb2a57060, v4\n"
    "v_fmac_f32 v115, 0xb2a57060, v5\n"
    "v_fmac_f32 v116, 0xb2a57060, v6\n"
    "v_fmac_f32 v117, 0xb2a57060, v7\n"
    "v_fmac_f32 v118, 0xb2a57060, v8\n"
    "v_mul_f32 v114, 0x3f317218, v114\n"
    "v_mul_f32 v115, 0x3f317218, v115\n"
    "v_mul_f32 v116, 0x3f317218, v116\n"
    "v_mul_f32 v117, 0x3f317218, v117\n"
    "v_mul_f32 v118, 0x3f317218, v118\n"
    "v_min_f32 v108, 0x7e800000, v108\n"
    "s_waitcnt lgkmcnt(0)\n"
    "s_barrier\n"
    "ds_read_b128 v[100:103], v51\n"
    "ds_read_b128 v[104:107], v51 offset:16\n"
    "v_min_f32 v109, 0x7e800000, v109\n"
    "v_min_f32 v110, 0x7e800000, v110\n"
    "v_min_f32 v111, 0x7e800000, v111\n"
    "v_min_f32 v112, 0x7e800000, v112\n"
    "v_fmac_f32 v108, v108, v114\n"
    "v_fmac_f32 v109, v109, v115\n"
    "v_fmac_f32 v110, v110, v116\n"
    "v_fmac_f32 v111, v111, v117\n"
    "v_fmac_f32 v112, v112, v118\n"
    "v_add_f32 v108, 1.0, v108\n"
    "v_add_f32 v109, 1.0, v109\n"
    "v_add_f32 v110, 1.0, v110\n"
    "v_add_f32 v111, 1.0, v111\n"
    "v_add_f32 v112, 1.0, v112\n"
    "v_rcp_f32 v114, v108\n"
    "v_rcp_f32 v115, v109\n"
    "s_waitcnt lgkmcnt(0)\n"
    "v_pk_add_f32 v[60:61], v[100:101], v[102:103]\n"
    "v_pk_add_f32 v[62:63], v[104:105], v[106:107]\n"
    "v_pk_add_f32 v[96:97], v[60:61], v[62:63]\n"
    "v_mul_f32 v96, 0x3e800000, v96\n"
    "v_sub_f32 v100, v100, v96\n"
    "v_sub_f32 v102, v102, v96\n"
    "v_sub_f32 v104, v104, v96\n"
    "v_sub_f32 v106, v106, v96\n"
    "v_mul_f32 v60, v100, v100\n"
    "v_fmac_f32 v60, v102, v102\n"
    "v_fmac_f32 v60, v104, v104\n"
    "v_fmac_f32 v60, v106, v106\n"
    "v_fmac_f32 v97, 0x43100000, v60\n"
    "v_mul_f32 v97, 0x3ae38e39, v97\n"
    "v_cvt_pk_bf16_f32 v60, v96, v97\n"
    "v_lshlrev_b32 v96, 16, v60\n"
    "v_and_b32 v97, 0xffff0000, v60\n"
    "v_add_f32 v97, 0x3727c5ac, v97\n"
    "v_cvt_pk_bf16_f32 v97, v97, v97\n"
    "v_and_b32 v97, 0xffff0000, v97\n"
    "v_sqrt_f32 v97, v97\n"
    "v_rcp_f32 v116, v110\n"
    "v_cvt_pk_bf16_f32 v97, v97, v97\n"
    "v_and_b32 v97, 0xffff0000, v97\n"
    "v_rcp_f32 v97, v97\n"
    "s_nop 0\n"
    "v_pk_add_f32 v[72:73], v[72:73], v[96:97] op_sel_hi:[1,0] neg_lo:[0,1] neg_hi:[0,1]\n"
    "v_cvt_pk_bf16_f32 v60, v72, v73\n"
    "v_lshlrev_b32 v72, 16, v60\n"
    "v_and_b32 v73, 0xffff0000, v60\n"
    "v_pk_mul_f32 v[72:73], v[72:73], v[96:97] op_sel:[0,1] op_sel_hi:[1,1]\n"
    "v_cvt_pk_bf16_f32 v61, v72, v73\n"
    "v_lshlrev_b32 v72, 16, v61\n"
    "v_and_b32 v73, 0xffff0000, v61\n"
    "v_cvt_pkrtz_f16_f32 v32, v72, v73\n"
    "v_pk_add_f32 v[74:75], v[74:75], v[96:97] op_sel_hi:[1,0] neg_lo:[0,1] neg_hi:[0,1]\n"
    "v_cvt_pk_bf16_f32 v60, v74, v75\n"
    "v_lshlrev_b32 v74, 16, v60\n"
    "v_and_b32 v75, 0xffff0000, v60\n"
    "v_pk_mul_f32 v[74:75], v[74:75], v[96:97] op_sel:[0,1] op_sel_hi:[1,1]\n"
    "v_cvt_pk_bf16_f32 v61, v74, v75\n"
    "v_lshlrev_b32 v74, 16, v61\n"
    "v_and_b32 v75, 0xffff0000, v61\n"
    "v_cvt_pkrtz_f16_f32 v33, v74, v75\n"
    "v_pk_add_f32 v[76:77], v[76:77], v[96:97] op_sel_hi:[1,0] neg_lo:[0,1] neg_hi:[0,1]\n"
    "v_cvt_pk_bf16_f32 v60, v76, v77\n"
    "v_lshlrev_b32 v76, 16, v60\n"
    "v_and_b32 v77, 0xffff0000, v60\n"
    "v_pk_mul_f32 v[76:77], v[76:77], v[96:97] op_sel:[0,1] op_sel_hi:[1,1]\n"
    "v_cvt_pk_bf16_f32 v61, v76, v77\n"
    "v_lshlrev_b32 v76, 16, v61\n"
    "v_and_b32 v77, 0xffff0000, v61\n"
    "v_cvt_pkrtz_f16_f32 v34, v76, v77\n"
    "v_pk_add_f32 v[78:79], v[78:79], v[96:97] op_sel_hi:[1,0] neg_lo:[0,1] neg_hi:[0,1]\n"
    "v_cvt_pk_bf16_f32 v60, v78, v79\n"
    "v_lshlrev_b32 v78, 16, v60\n"
    "v_and_b32 v79, 0xffff0000, v60\n"
    "v_pk_mul_f32 v[78:79], v[78:79], v[96:97] op_sel:[0,1] op_sel_hi:[1,1]\n"
    "v_cvt_pk_bf16_f32 v61, v78, v79\n"
    "v_lshlrev_b32 v78, 16, v61\n"
    "v_and_b32 v79, 0xffff0000, v61\n"
    "v_cvt_pkrtz_f16_f32 v35, v78, v79\n"
    "buffer_store_dwordx4 v[32:35], v55, s[56:59], s84 offen offset:0 sc1\n"
    "v_pk_add_f32 v[80:81], v[80:81], v[96:97] op_sel_hi:[1,0] neg_lo:[0,1] neg_hi:[0,1]\n"
    "v_cvt_pk_bf16_f32 v60, v80, v81\n"
    "v_lshlrev_b32 v80, 16, v60\n"
    "v_and_b32 v81, 0xffff0000, v60\n"
    "v_pk_mul_f32 v[80:81], v[80:81], v[96:97] op_sel:[0,1] op_sel_hi:[1,1]\n"
    "v_cvt_pk_bf16_f32 v61, v80, v81\n"
    "v_lshlrev_b32 v80, 16, v61\n"
    "v_and_b32 v81, 0xffff0000, v61\n"
    "v_cvt_pkrtz_f16_f32 v36, v80, v81\n"
    "v_pk_add_f32 v[82:83], v[82:83], v[96:97] op_sel_hi:[1,0] neg_lo:[0,1] neg_hi:[0,1]\n"
    "v_cvt_pk_bf16_f32 v60, v82, v83\n"
    "v_lshlrev_b32 v82, 16, v60\n"
    "v_and_b32 v83, 0xffff0000, v60\n"
    "v_pk_mul_f32 v[82:83], v[82:83], v[96:97] op_sel:[0,1] op_sel_hi:[1,1]\n"
    "v_cvt_pk_bf16_f32 v61, v82, v83\n"
    "v_lshlrev_b32 v82, 16, v61\n"
    "v_and_b32 v83, 0xffff0000, v61\n"
    "v_cvt_pkrtz_f16_f32 v37, v82, v83\n"
    "v_pk_add_f32 v[84:85], v[84:85], v[96:97] op_sel_hi:[1,0] neg_lo:[0,1] neg_hi:[0,1]\n"
    "v_cvt_pk_bf16_f32 v60, v84, v85\n"
    "v_lshlrev_b32 v84, 16, v60\n"
    "v_and_b32 v85, 0xffff0000, v60\n"
    "v_pk_mul_f32 v[84:85], v[84:85], v[96:97] op_sel:[0,1] op_sel_hi:[1,1]\n"
    "v_cvt_pk_bf16_f32 v61, v84, v85\n"
    "v_lshlrev_b32 v84, 16, v61\n"
    "v_and_b32 v85, 0xffff0000, v61\n"
    "v_cvt_pkrtz_f16_f32 v38, v84, v85\n"
    "v_pk_add_f32 v[86:87], v[86:87], v[96:97] op_sel_hi:[1,0] neg_lo:[0,1] neg_hi:[0,1]\n"
    "v_cvt_pk_bf16_f32 v60, v86, v87\n"
    "v_lshlrev_b32 v86, 16, v60\n"
    "v_and_b32 v87, 0xffff0000, v60\n"
    "v_pk_mul_f32 v[86:87], v[86:87], v[96:97] op_sel:[0,1] op_sel_hi:[1,1]\n"
    "v_cvt_pk_bf16_f32 v61, v86, v87\n"
    "v_lshlrev_b32 v86, 16, v61\n"
    "v_and_b32 v87, 0xffff0000, v61\n"
    "v_cvt_pkrtz_f16_f32 v39, v86, v87\n"
    "buffer_store_dwordx4 v[36:39], v55, s[56:59], s84 offen offset:2048 sc1\n"
    "v_pk_add_f32 v[88:89], v[88:89], v[96:97] op_sel_hi:[1,0] neg_lo:[0,1] neg_hi:[0,1]\n"
    "v_cvt_pk_bf16_f32 v60, v88, v89\n"
    "v_lshlrev_b32 v88, 16, v60\n"
    "v_and_b32 v89, 0xffff0000, v60\n"
    "v_pk_mul_f32 v[88:89], v[88:89], v[96:97] op_sel:[0,1] op_sel_hi:[1,1]\n"
    "v_cvt_pk_bf16_f32 v61, v88, v89\n"
    "v_lshlrev_b32 v88, 16, v61\n"
    "v_and_b32 v89, 0xffff0000, v61\n"
    "v_cvt_pkrtz_f16_f32 v40, v88, v89\n"
    "v_pk_add_f32 v[90:91], v[90:91], v[96:97] op_sel_hi:[1,0] neg_lo:[0,1] neg_hi:[0,1]\n"
    "v_cvt_pk_bf16_f32 v60, v90, v91\n"
    "v_lshlrev_b32 v90, 16, v60\n"
    "v_and_b32 v91, 0xffff0000, v60\n"
    "v_pk_mul_f32 v[90:91], v[90:91], v[96:97] op_sel:[0,1] op_sel_hi:[1,1]\n"
    "v_cvt_pk_bf16_f32 v61, v90, v91\n"
    "v_lshlrev_b32 v90, 16, v61\n"
    "v_and_b32 v91, 0xffff0000, v61\n"
    "v_cvt_pkrtz_f16_f32 v41, v90, v91\n"
    "v_pk_add_f32 v[92:93], v[92:93], v[96:97] op_sel_hi:[1,0] neg_lo:[0,1] neg_hi:[0,1]\n"
    "v_cvt_pk_bf16_f32 v60, v92, v93\n"
    "v_lshlrev_b32 v92, 16, v60\n"
    "v_and_b32 v93, 0xffff0000, v60\n"
    "v_pk_mul_f32 v[92:93], v[92:93], v[96:97] op_sel:[0,1] op_sel_hi:[1,1]\n"
    "v_cvt_pk_bf16_f32 v61, v92, v93\n"
    "v_lshlrev_b32 v92, 16, v61\n"
    "v_and_b32 v93, 0xffff0000, v61\n"
    "v_cvt_pkrtz_f16_f32 v42, v92, v93\n"
    "v_pk_add_f32 v[94:95], v[94:95], v[96:97] op_sel_hi:[1,0] neg_lo:[0,1] neg_hi:[0,1]\n"
    "v_cvt_pk_bf16_f32 v60, v94, v95\n"
    "v_lshlrev_b32 v94, 16, v60\n"
    "v_and_b32 v95, 0xffff0000, v60\n"
    "v_pk_mul_f32 v[94:95], v[94:95], v[96:97] op_sel:[0,1] op_sel_hi:[1,1]\n"
    "v_cvt_pk_bf16_f32 v61, v94, v95\n"
    "v_lshlrev_b32 v94, 16, v61\n"
    "v_and_b32 v95, 0xffff0000, v61\n"
    "v_cvt_pkrtz_f16_f32 v43, v94, v95\n"
    "s_mov_b64 exec, 0xffff\n"
    "buffer_store_dwordx4 v[40:43], v55, s[56:59], s85 offen offset:0 sc1\n"
    "s_mov_b64 exec, -1\n"
    "buffer_store_dword v47, v46, s[56:59], 0 offen sc1\n"
    "v_rcp_f32 v117, v111\n"
    "v_rcp_f32 v118, v112\n"
    "v_mul_f32 v120, v4, v114\n"
    "v_mul_f32 v121, v5, v115\n"
    "v_mul_f32 v122, v6, v116\n"
    "v_mul_f32 v123, v7, v117\n"
    "v_mul_f32 v124, v8, v118\n"
    "v_fma_f32 v4, -v108, v120, v4\n"
    "v_fma_f32 v5, -v109, v121, v5\n"
    "v_fma_f32 v6, -v110, v122, v6\n"
    "v_fma_f32 v7, -v111, v123, v7\n"
    "v_fma_f32 v8, -v112, v124, v8\n"
    "v_fma_f32 v4, v4, v114, v120\n"
    "v_fma_f32 v5, v5, v115, v121\n"
    "v_fma_f32 v6, v6, v116, v122\n"
    "v_fma_f32 v7, v7, v117, v123\n"
    "v_fma_f32 v8, v8, v118, v124\n"
    "v_cvt_pk_bf16_f32 v108, v4, v5\n"
    "v_lshlrev_b32 v4, 16, v108\n"
    "v_and_b32 v5, 0xffff0000, v108\n"
    "v_cvt_pk_bf16_f32 v109, v6, v7\n"
    "v_lshlrev_b32 v6, 16, v109\n"
    "v_and_b32 v7, 0xffff0000, v109\n"
    "v_cvt_pk_bf16_f32 v8, v8, v8\n"
    "v_and_b32 v8, 0xffff0000, v8\n"
    "s_waitcnt vmcnt(8)\n"
    "v_lshlrev_b32 v60, 16, v12\n"
    "v_and_b32 v61, 0xffff0000, v12\n"
    "v_pk_mul_f32 v[64:65], v[60:61], v[4:5] op_sel_hi:[1,0]\n"
    "s_add_u32 m0, s79, 0\n"
    "s_mov_b32 s81, 0\n"
    "buffer_load_dwordx4 v228, s[48:51], s81 offen offset:0 lds\n"
    "v_lshlrev_b32 v62, 16, v13\n"
    "v_and_b32 v63, 0xffff0000, v13\n"
    "v_pk_mul_f32 v[66:67], v[62:63], v[4:5] op_sel_hi:[1,0]\n"
    "v_lshlrev_b32 v60, 16, v14\n"
    "v_and_b32 v61, 0xffff0000, v14\n"
    "v_pk_mul_f32 v[68:69], v[60:61], v[4:5] op_sel_hi:[1,0]\n"
    "buffer_load_dwordx4 v229, s[48:51], s81 offen offset:1024 lds\n"
    "v_lshlrev_b32 v62, 16, v15\n"
    "v_and_b32 v63, 0xffff0000, v15\n"
    "v_pk_mul_f32 v[70:71], v[62:63], v[4:5] op_sel_hi:[1,0]\n"
    "s_waitcnt vmcnt(9)\n"
    "v_lshlrev_b32 v60, 16, v16\n"
    "v_and_b32 v61, 0xffff0000, v16\n"
    "v_pk_fma_f32 v[64:65], v[60:61], v[4:5], v[64:65] op_sel:[0,1,0] op_sel_hi:[1,1,1]\n"
    "buffer_load_dwordx4 v230, s[48:51], s81 offen offset:2048 lds\n"
    "v_lshlrev_b32 v62, 16, v17\n"
    "v_and_b32 v63, 0xffff0000, v17\n"
    "v_pk_fma_f32 v[66:67], v[62:63], v[4:5], v[66:67] op_sel:[0,1,0] op_sel_hi:[1,1,1]\n"
    "v_lshlrev_b32 v60, 16, v18\n"
    "v_and_b32 v61, 0xffff0000, v18\n"
    "v_pk_fma_f32 v[68:69], v[60:61], v[4:5], v[68:69] op_sel:[0,1,0] op_sel_hi:[1,1,1]\n"
    "buffer_load_dwordx4 v231, s[48:51], s81 offen offset:3072 lds\n"
    "v_lshlrev_b32 v62, 16, v19\n"
    "v_and_b32 v63, 0xffff0000, v19\n"
    "v_pk_fma_f32 v[70:71], v[62:63], v[4:5], v[70:71] op_sel:[0,1,0] op_sel_hi:[1,1,1]\n"
    "s_waitcnt vmcnt(10)\n"
    "v_lshlrev_b32 v60, 16, v20\n"
    "v_and_b32 v61, 0xffff0000, v20\n"
    "v_pk_fma_f32 v[64:65], v[60:61], v[6:7], v[64:65] op_sel_hi:[1,0,1]\n"
    "s_add_u32 m0, s79, 49160\n"
    "s_mov_b32 s81, 65536\n"
    "buffer_load_dwordx4 v228, s[48:51], s81 offen offset:0 lds\n"
    "v_lshlrev_b32 v62, 16, v21\n"
    "v_and_b32 v63, 0xffff0000, v21\n"
    "v_pk_fma_f32 v[66:67], v[62:63], v[6:7], v[66:67] op_sel_hi:[1,0,1]\n"
    "v_lshlrev_b32 v60, 16, v22\n"
    "v_and_b32 v61, 0xffff0000, v22\n"
    "v_pk_fma_f32 v[68:69], v[60:61], v[6:7], v[68:69] op_sel_hi:[1,0,1]\n"
    "buffer_load_dwordx4 v229, s[48:51], s81 offen offset:1024 lds\n"
    "v_lshlrev_b32 v62, 16, v23\n"
    "v_and_b32 v63, 0xffff0000, v23\n"
    "v_pk_fma_f32 v[70:71], v[62:63], v[6:7], v[70:71] op_sel_hi:[1,0,1]\n"
    "s_waitcnt vmcnt(11)\n"
    "v_lshlrev_b32 v60, 16, v24\n"
    "v_and_b32 v61, 0xffff0000, v24\n"
    "v_pk_fma_f32 v[64:65], v[60:61], v[6:7], v[64:65] op_sel:[0,1,0] op_sel_hi:[1,1,1]\n"
    "buffer_load_dwordx4 v230, s[48:51], s81 offen offset:2048 lds\n"
    "v_lshlrev_b32 v62, 16, v25\n"
    "v_and_b32 v63, 0xffff0000, v25\n"
    "v_pk_fma_f32 v[66:67], v[62:63], v[6:7], v[66:67] op_sel:[0,1,0] op_sel_hi:[1,1,1]\n"
    "v_lshlrev_b32 v60, 16, v26\n"
    "v_and_b32 v61, 0xffff0000, v26\n"
    "v_pk_fma_f32 v[68:69], v[60:61], v[6:7], v[68:69] op_sel:[0,1,0] op_sel_hi:[1,1,1]\n"
    "buffer_load_dwordx4 v231, s[48:51], s81 offen offset:3072 lds\n"
    "v_lshlrev_b32 v62, 16, v27\n"
    "v_and_b32 v63, 0xffff0000, v27\n"
    "v_pk_fma_f32 v[70:71], v[62:63], v[6:7], v[70:71] op_sel:[0,1,0] op_sel_hi:[1,1,1]\n"
    "s_waitcnt vmcnt(12)\n"
    "v_lshlrev_b32 v60, 16, v28\n"
    "v_and_b32 v61, 0xffff0000, v28\n"
    "v_pk_fma_f32 v[64:65], v[60:61], v[8:9], v[64:65] op_sel_hi:[1,0,1]\n"
    "v_lshlrev_b32 v62, 16, v29\n"
    "v_and_b32 v63, 0xffff0000, v29\n"
    "v_pk_fma_f32 v[66:67], v[62:63], v[8:9], v[66:67] op_sel_hi:[1,0,1]\n"
    "v_lshlrev_b32 v60, 16, v30\n"
    "v_and_b32 v61, 0xffff0000, v30\n"
    "v_pk_fma_f32 v[68:69], v[60:61], v[8:9], v[68:69] op_sel_hi:[1,0,1]\n"
    "v_lshlrev_b32 v62, 16, v31\n"
    "v_and_b32 v63, 0xffff0000, v31\n"
    "v_pk_fma_f32 v[70:71], v[62:63], v[8:9], v[70:71] op_sel_hi:[1,0,1]\n"
    "v_permlane32_swap_b32 v64, v68\n"
    "v_permlane32_swap_b32 v65, v69\n"
    "v_permlane32_swap_b32 v66, v70\n"
    "v_permlane32_swap_b32 v67, v71\n"
    "v_add_u32 v45, 0x9e3779b9, v45\n"
    "s_nop 0\n"
    "v_pk_add_f32 v[64:65], v[64:65], v[68:69]\n"
    "v_pk_add_f32 v[66:67], v[66:67], v[70:71]\n"
    "s_nop 1\n"
    "v_permlane16_swap_b32 v64, v66\n"
    "v_permlane16_swap_b32 v65, v67\n"
    "s_nop 1\n"
    "v_pk_add_f32 v[64:65], v[64:65], v[66:67]\n"
    "s_nop 1\n"
    "v_add_f32_dpp v64, v64, v64 quad_perm:[2,3,0,1] row_mask:0xf bank_mask:0xf\n"
    "v_add_f32_dpp v65, v65, v65 quad_perm:[2,3,0,1] row_mask:0xf bank_mask:0xf\n"
    "s_nop 0\n"
    "v_add_f32_dpp v64, v64, v64 row_ror:4 row_mask:0xf bank_mask:0xf\n"
    "v_add_f32_dpp v65, v65, v65 row_ror:4 row_mask:0xf bank_mask:0xf\n"
    "s_nop 0\n"
    "v_add_f32_dpp v64, v64, v64 row_ror:8 row_mask:0xf bank_mask:0xf\n"
    "v_add_f32_dpp v65, v65, v65 row_ror:8 row_mask:0xf bank_mask:0xf\n"
    "ds_write2_b32 v52, v64, v65 offset1:4\n"
    "s_waitcnt vmcnt(8) lgkmcnt(0)\n"
    "s_barrier\n"
    "ds_read_b128 v[20:23], v53\n"
    "v_readfirstlane_b32 s67, v45\n"
    "s_waitcnt lgkmcnt(0)\n"
    "v_pk_add_f32 v[20:21], v[20:21], v[22:23]\n"
    "v_add_f32 v44, v20, v21\n"
    "v_cvt_pk_bf16_f32 v44, v44, v44\n"
    "v_and_b32 v44, 0xffff0000, v44\n"
    "v_add_f32 v44, s86, v44\n"
    "v_cvt_pk_bf16_f32 v44, v44, v44\n"
    "v_and_b32 v44, 0xffff0000, v44\n"
    "buffer_store_dwordx2 v[44:45], v56, s[56:59], s87 offen sc1\n"
    "s_add_u32 m0, s79, 16384\n"
    "s_mov_b32 s81, 524288\n"
    "buffer_load_dwordx4 v228, s[48:51], s81 offen offset:0 lds\n"
    "buffer_load_dwordx4 v229, s[48:51], s81 offen offset:1024 lds\n"
    "buffer_load_dwordx4 v230, s[48:51], s81 offen offset:2048 lds\n"
    "buffer_load_dwordx4 v231, s[48:51], s81 offen offset:3072 lds\n"
    "s_add_u32 m0, s79, 65544\n"
    "s_mov_b32 s81, 589824\n"
    "buffer_load_dwordx4 v228, s[48:51], s81 offen offset:0 lds\n"
    "buffer_load_dwordx4 v229, s[48:51], s81 offen offset:1024 lds\n"
    "buffer_load_dwordx4 v230, s[48:51], s81 offen offset:2048 lds\n"
    "buffer_load_dwordx4 v231, s[48:51], s81 offen offset:3072 lds\n"
    "s_add_u32 m0, s79, 32768\n"
    "s_mov_b32 s81, 1048576\n"
    "buffer_load_dwordx4 v228, s[48:51], s81 offen offset:0 lds\n"
    "buffer_load_dwordx4 v229, s[48:51], s81 offen offset:1024 lds\n"
    "buffer_load_dwordx4 v230, s[48:51], s81 offen offset:2048 lds\n"
    "buffer_load_dwordx4 v231, s[48:51], s81 offen offset:3072 lds\n"
    "s_add_u32 m0, s79, 81928\n"
    "s_mov_b32 s81, 1114112\n"
    "buffer_load_dwordx4 v228, s[48:51], s81 offen offset:0 lds\n"
    "buffer_load_dwordx4 v229, s[48:51], s81 offen offset:1024 lds\n"
    "buffer_load_dwordx4 v230, s[48:51], s81 offen offset:2048 lds\n"
    "buffer_load_dwordx4 v231, s[48:51], s81 offen offset:3072 lds\n"
    "v_lshlrev_b32 v58, 7, v1\n"
    "s_mov_b32 s84, 32768\n"
    "v_lshrrev_b32 v247, 4, v239\n"
    "s_lshl_b32 s82, s89, 3\n"
    "s_lshl_b32 s83, s78, 1\n"
    "s_add_u32 s82, s82, s83\n"
    "s_mul_i32 s82, s82, 18432\n"
    "s_add_u32 s90, s82, 1048576\n"
    "s_lshr_b32 s83, s88, 2\n"
    "s_mul_i32 s83, s83, 2359296\n"
    "s_add_u32 s91, s82, s83\n"
    "s_and_b32 s83, s88, 3\n"
    "s_lshl_b32 s83, s83, 9\n"
    "s_add_u32 s91, s91, s83\n"
    "s_add_u32 s91, s91, 8388608\n"
    "v_add_u32 v245, 18432, v244\n"
    "v_and_b32 v225, 0xf0, v244\n"
    "v_lshrrev_b32 v249, 1, v247\n"
    "v_lshl_add_u32 v225, v249, 12, v225\n"
    "v_and_b32 v246, 1, v247\n"
    "v_mul_u32_u24 v246, 49160, v246\n"
    "v_add_u32 v225, v225, v246\n"
    "v_lshlrev_b32 v226, 6, v247\n"
    "v_add_u32 v226, 98368, v226\n"
    "s_lshl_b32 s82, s88, 19\n"
    "s_mov_b64 s[40:41], %1\n"
    "s_add_u32 s40, s40, s82\n"
    "s_addc_u32 s41, s41, 0\n"
    "s_and_b32 s41, s41, 0xffff\n"
    "s_mov_b32 s42, 524288\n"
    "s_mov_b32 s43, 0x20000\n"
    "s_mov_b64 s[44:45], %2\n"
    "s_add_u32 s44, s44, s82\n"
    "s_addc_u32 s45, s45, 0\n"
    "s_and_b32 s45, s45, 0xffff\n"
    "s_mov_b32 s46, 524288\n"
    "s_mov_b32 s47, 0x20000\n"
    "s_lshr_b32 s82, s88, 2\n"
    "s_mul_i32 s82, s82, 1179648\n"
    "s_mov_b64 s[52:53], %4\n"
    "s_add_u32 s52, s52, s82\n"
    "s_addc_u32 s53, s53, 0\n"
    "s_and_b32 s53, s53, 0xffff\n"
    "s_mov_b32 s54, 1179648\n"
    "s_mov_b32 s55, 0x20000\n"
    "s_mov_b64 s[94:95], 0\n"
    "s_mov_b32 s65, 0\n"
    "s_mov_b32 s66, 180224\n"
    "v_lshrrev_b32 v247, 4, v239\n"
    "v_and_b32 v248, 15, v239\n"
    "v_add_u32 v249, 0, v247\n"
    "v_xor_b32 v249, v249, v248\n"
    "v_lshlrev_b32 v249, 4, v249\n"
    "v_lshl_add_u32 v208, v248, 8, v249\n"
    "v_add_u32 v208, 131072, v208\n"
    "v_lshl_add_u32 v249, v247, 8, v249\n"
    "v_add_u32 v214, s79, v249\n"
    "v_add_u32 v249, 4, v247\n"
    "v_xor_b32 v249, v249, v248\n"
    "v_lshlrev_b32 v249, 4, v249\n"
    "v_lshl_add_u32 v209, v248, 8, v249\n"
    "v_add_u32 v209, 131072, v209\n"
    "v_lshl_add_u32 v249, v247, 8, v249\n"
    "v_add_u32 v215, s79, v249\n"
    "v_add_u32 v249, 8, v247\n"
    "v_xor_b32 v249, v249, v248\n"
    "v_lshlrev_b32 v249, 4, v249\n"
    "v_lshl_add_u32 v210, v248, 8, v249\n"
    "v_add_u32 v210, 131072, v210\n"
    "v_lshl_add_u32 v249, v247, 8, v249\n"
    "v_add_u32 v216, s79, v249\n"
    "v_add_u32 v249, 12, v247\n"
    "v_xor_b32 v249, v249, v248\n"
    "v_lshlrev_b32 v249, 4, v249\n"
    "v_lshl_add_u32 v211, v248, 8, v249\n"
    "v_add_u32 v211, 131072, v211\n"
    "v_lshl_add_u32 v249, v247, 8, v249\n"
    "v_add_u32 v217, s79, v249\n"
    "v_and_b32 v246, 3, v239\n"
    "v_lshlrev_b32 v249, 4, v246\n"
    "v_lshl_add_u32 v249, v247, 6, v249\n"
    "v_bfe_u32 v248, v239, 2, 2\n"
    "v_lshl_add_u32 v218, v248, 8, v249\n"
    "v_add_u32 v218, s79, v218\n"
    "v_xor_b32 v219, 32, v218\n"
    "v_mul_u32_u24 v227, 1152, v248\n"
    "v_add_u32 v227, v227, v249\n"
    "s_and_b32 s85, s88, 3\n"
    "s_lshl_b32 s85, s85, 8\n"
    "v_add_u32 v227, s85, v227\n"
    "v_mov_b32 v240, v227\n"
    "v_xor_b32 v241, 32, v240\n"
    "v_add_u32 v241, 3584, v241\n"
    "v_add_u32 v242, 7168, v240\n"
    "v_add_u32 v243, 7168, v241\n"
    "v_lshlrev_b32 v246, 3, v246\n"
    "v_lshl_add_u32 v246, v248, 6, v246\n"
    "v_lshl_add_u32 v246, v247, 10, v246\n"
    "v_and_b32 v249, 16, v239\n"
    "v_lshl_add_u32 v235, v249, 1, v246\n"
    "v_xor_b32 v236, 32, v235\n"
    "v_add_u32 v212, 98304, v235\n"
    "v_add_u32 v237, 90112, v235\n"
    "v_add_u32 v213, 98304, v236\n"
    "v_add_u32 v238, 90112, v236\n"
    "v_and_b32 v249, 1, v239\n"
    "v_and_b32 v246, 2, v239\n"
    "v_cmp_eq_u32 vcc, 0, v246\n"
    "v_mov_b32 v248, 0x3f803f80\n"
    "v_mov_b32 v247, 0x44444444\n"
    "v_mov_b32 v246, 0xeeeeeeee\n"
    "v_cndmask_b32 v220, 0, v248, vcc\n"
    "v_cndmask_b32 v221, 0, v248, vcc\n"
    "v_cndmask_b32 v222, 0, v248, vcc\n"
    "v_cndmask_b32 v223, 0, v248, vcc\n"
    "v_cmp_eq_u32 vcc, 0, v249\n"
    "s_add_u32 s74, s79, 131072\n"
    "s_add_u32 s76, s79, 114688\n"
    "s_add_u32 s75, s79, 147456\n"
    "s_add_u32 s77, s79, 98304\n"
    "v_cndmask_b32 v224, v246, v247, vcc\n"
    "v_lshrrev_b32 v246, 1, v239\n"
    "v_mul_u32_u24 v233, 1152, v246\n"
    "v_lshl_add_u32 v233, v249, 4, v233\n"
    "s_lshr_b32 s85, s85, 3\n"
    "s_add_u32 s85, s85, 1024\n"
    "v_add_u32 v233, s85, v233\n"
    "s_and_b32 s85, s78, 1\n"
    "s_lshl_b32 s86, s85, 5\n"
    "v_xor_b32 v227, s86, v227\n"
    "s_lshl_b32 s73, s78, 10\n"
    "s_movk_i32 s37, 4096\n"
    "s_movk_i32 s36, 18432\n"
    "s_and_b32 s84, s88, 3\n"
    "s_mul_i32 s38, s84, 294912\n"
    "s_mul_i32 s92, s78, 4608\n"
    "s_add_u32 s92, s92, s38\n"
    "s_mov_b64 s[68:69], s[52:53]\n"
    "s_mov_b64 s[70:71], s[54:55]\n"
    "s_and_b32 s39, %14, 7\n"
    "s_lshl_b32 s39, s39, 1\n"
    "s_bfe_u32 s84, %14, 0x10003\n"
    "s_add_u32 s39, s39, s84\n"
    "s_and_b32 s84, %14, 0xf0\n"
    "s_add_u32 s39, s39, s84\n"
    "s_cmp_eq_u32 s78, 1\n"
    "s_cselect_b64 s[60:61], %6, %7\n"
    "s_cmp_eq_u32 s78, 3\n"
    "s_cselect_b64 s[60:61], %8, s[60:61]\n"
    "s_lshr_b32 s84, %14, 3\n"
    "s_cmp_eq_u32 s78, 0\n"
    "s_cselect_b64 s[60:61], s[52:53], s[60:61]\n"
    "s_cselect_b32 s84, s84, s39\n"
    "s_mul_i32 s85, s84, 36864\n"
    "s_lshr_b32 s86, s39, 1\n"
    "s_lshl_b32 s86, s86, 7\n"
    "s_and_b32 s87, s39, 1\n"
    "s_mul_i32 s87, s87, 9216\n"
    "s_add_u32 s86, s86, s87\n"
    "s_sub_u32 s87, 1769472, s86\n"
    "s_add_u32 s64, s78, 1\n"
    "s_bitcmp1_b32 s64, 1\n"
    "s_cselect_b32 s85, s86, s85\n"
    "s_cselect_b32 s62, s87, 36864\n"
    "s_cselect_b32 s64, 144, 1\n"
    "s_cmp_lt_u32 s84, 144\n"
    "s_cselect_b32 s62, s62, 0\n"
    "s_add_u32 s60, s60, s85\n"
    "s_addc_u32 s61, s61, 0\n"
    "s_and_b32 s61, s61, 0xffff\n"
    "s_mov_b32 s63, 0x20000\n"
    "s_lshl_b32 s64, s64, 7\n"
    "v_mul_u32_u24 v234, s64, v246\n"
    "s_lshl_b32 s64, s64, 5\n"
    "s_mov_b32 s84, 32768\n"
    "buffer_load_dword v46, v58, s[56:59], s84 offen offset:4 sc1\n"
    "s_mul_i32 s80, s78, 56\n"
    "s_add_u32 s80, s80, 64\n"
    "v_min_u32 v60, 55, v239\n"
    "v_add_u32 v60, s80, v60\n"
    "v_lshrrev_b32 v61, 2, v239\n"
    "v_lshlrev_b32 v61, 6, v61\n"
    "v_and_b32 v62, 3, v239\n"
    "v_lshl_add_u32 v62, v62, 3, v61\n"
    "v_add_u32 v62, 98368, v62\n"
    "v_lshrrev_b32 v61, 2, v60\n"
    "v_lshlrev_b32 v61, 6, v61\n"
    "v_and_b32 v63, 3, v60\n"
    "v_lshl_add_u32 v63, v63, 3, v61\n"
    "v_add_u32 v63, 98368, v63\n"
    "v_lshlrev_b32 v60, 4, v60\n"
    "s_mov_b32 s87, 0\n"
    "s_mov_b32 s84, 32768\n"
    "s_mov_b32 s85, 37376\n"
    "s_branch SCHK%=\n"
    "SPOLL%=:\n"
    "s_waitcnt vmcnt(0)\n"
    "buffer_load_dword v46, v58, s[56:59], s84 offen offset:4 sc1\n"
    "SCHK%=:\n"
    "s_waitcnt vmcnt(0)\n"
    "v_cmp_ne_u32 vcc, v46, v45\n"
    "s_nop 1\n"
    "s_cmp_eq_u64 vcc, 0\n"
    "s_cbranch_scc1 SDONE%=\n"
    "s_add_u32 s87, s87, 1\n"
    "s_cmp_gt_u32 s87, 262144\n"
    "s_cbranch_scc1 SDONE%=\n"
    "s_sleep 1\n"
    "s_branch SPOLL%=\n"
    "SDONE%=:\n"
    "s_barrier\n"
    "buffer_load_dwordx4 v[12:15], v244, s[56:59], s84 offen sc1\n"
    "buffer_load_dwordx4 v[16:19], v244, s[56:59], s85 offen sc1\n"
    "s_add_u32 s82, s90, 0\n"
    "buffer_load_dwordx4 v[144:147], v244, s[56:59], s82 offen offset:0 sc1\n"
    "buffer_load_dwordx4 v[148:151], v244, s[56:59], s82 offen offset:1024 sc1\n"
    "buffer_load_dwordx4 v[152:155], v245, s[56:59], s82 offen offset:0 sc1\n"
    "buffer_load_dwordx4 v[156:159], v245, s[56:59], s82 offen offset:1024 sc1\n"
    "buffer_load_dwordx4 v[20:23], v60, s[56:59], s84 offen sc1\n"
    "buffer_load_dwordx4 v[24:27], v60, s[56:59], s85 offen sc1\n"
    "s_add_u32 s82, s90, 2048\n"
    "buffer_load_dwordx4 v[160:163], v244, s[56:59], s82 offen offset:0 sc1\n"
    "buffer_load_dwordx4 v[164:167], v244, s[56:59], s82 offen offset:1024 sc1\n"
    "buffer_load_dwordx4 v[168:171], v245, s[56:59], s82 offen offset:0 sc1\n"
    "buffer_load_dwordx4 v[172:175], v245, s[56:59], s82 offen offset:1024 sc1\n"
    "s_add_u32 s82, s90, 4096\n"
    "buffer_load_dwordx4 v[176:179], v244, s[56:59], s82 offen offset:0 sc1\n"
    "buffer_load_dwordx4 v[180:183], v244, s[56:59], s82 offen offset:1024 sc1\n"
    "buffer_load_dwordx4 v[184:187], v245, s[56:59], s82 offen offset:0 sc1\n"
    "buffer_load_dwordx4 v[188:191], v245, s[56:59], s82 offen offset:1024 sc1\n"
    "ds_read_b64_tr_b16 a[128:129], v225 offset:0\n"
    "ds_read_b64_tr_b16 a[130:131], v225 offset:2048\n"
    "ds_read_b64_tr_b16 a[132:133], v225 offset:8192\n"
    "ds_read_b64_tr_b16 a[134:135], v225 offset:10240\n"
    "ds_read_b64_tr_b16 a[136:137], v225 offset:8\n"
    "ds_read_b64_tr_b16 a[138:139], v225 offset:2056\n"
    "ds_read_b64_tr_b16 a[140:141], v225 offset:8200\n"
    "ds_read_b64_tr_b16 a[142:143], v225 offset:10248\n"
    "ds_read_b64_tr_b16 a[144:145], v225 offset:256\n"
    "ds_read_b64_tr_b16 a[146:147], v225 offset:2304\n"
    "ds_read_b64_tr_b16 a[148:149], v225 offset:8448\n"
    "ds_read_b64_tr_b16 a[150:151], v225 offset:10496\n"
    "ds_read_b64_tr_b16 a[152:153], v225 offset:264\n"
    "ds_read_b64_tr_b16 a[154:155], v225 offset:2312\n"
    "ds_read_b64_tr_b16 a[156:157], v225 offset:8456\n"
    "ds_read_b64_tr_b16 a[158:159], v225 offset:10504\n"
    "ds_read_b64_tr_b16 a[160:161], v225 offset:512\n"
    "ds_read_b64_tr_b16 a[162:163], v225 offset:2560\n"
    "ds_read_b64_tr_b16 a[164:165], v225 offset:8704\n"
    "ds_read_b64_tr_b16 a[166:167], v225 offset:10752\n"
    "ds_read_b64_tr_b16 a[168:169], v225 offset:520\n"
    "ds_read_b64_tr_b16 a[170:171], v225 offset:2568\n"
    "ds_read_b64_tr_b16 a[172:173], v225 offset:8712\n"
    "ds_read_b64_tr_b16 a[174:175], v225 offset:10760\n"
    "ds_read_b64_tr_b16 a[176:177], v225 offset:768\n"
    "ds_read_b64_tr_b16 a[178:179], v225 offset:2816\n"
    "ds_read_b64_tr_b16 a[180:181], v225 offset:8960\n"
    "ds_read_b64_tr_b16 a[182:183], v225 offset:11008\n"
    "ds_read_b64_tr_b16 a[184:185], v225 offset:776\n"
    "ds_read_b64_tr_b16 a[186:187], v225 offset:2824\n"
    "ds_read_b64_tr_b16 a[188:189], v225 offset:8968\n"
    "ds_read_b64_tr_b16 a[190:191], v225 offset:11016\n"
    "ds_read_b64_tr_b16 a[192:193], v225 offset:1024\n"
    "ds_read_b64_tr_b16 a[194:195], v225 offset:3072\n"
    "ds_read_b64_tr_b16 a[196:197], v225 offset:9216\n"
    "ds_read_b64_tr_b16 a[198:199], v225 offset:11264\n"
    "ds_read_b64_tr_b16 a[200:201], v225 offset:1032\n"
    "ds_read_b64_tr_b16 a[202:203], v225 offset:3080\n"
    "ds_read_b64_tr_b16 a[204:205], v225 offset:9224\n"
    "ds_read_b64_tr_b16 a[206:207], v225 offset:11272\n"
    "ds_read_b64_tr_b16 a[208:209], v225 offset:1280\n"
    "ds_read_b64_tr_b16 a[210:211], v225 offset:3328\n"
    "ds_read_b64_tr_b16 a[212:213], v225 offset:9472\n"
    "ds_read_b64_tr_b16 a[214:215], v225 offset:11520\n"
    "ds_read_b64_tr_b16 a[216:217], v225 offset:1288\n"
    "ds_read_b64_tr_b16 a[218:219], v225 offset:3336\n"
    "ds_read_b64_tr_b16 a[220:221], v225 offset:9480\n"
    "ds_read_b64_tr_b16 a[222:223], v225 offset:11528\n"
    "ds_read_b64_tr_b16 a[224:225], v225 offset:1536\n"
    "ds_read_b64_tr_b16 a[226:227], v225 offset:3584\n"
    "ds_read_b64_tr_b16 a[228:229], v225 offset:9728\n"
    "ds_read_b64_tr_b16 a[230:231], v225 offset:11776\n"
    "ds_read_b64_tr_b16 a[232:233], v225 offset:1544\n"
    "ds_read_b64_tr_b16 a[234:235], v225 offset:3592\n"
    "ds_read_b64_tr_b16 a[236:237], v225 offset:9736\n"
    "ds_read_b64_tr_b16 a[238:239], v225 offset:11784\n"
    "ds_read_b64_tr_b16 a[240:241], v225 offset:1792\n"
    "ds_read_b64_tr_b16 a[242:243], v225 offset:3840\n"
    "ds_read_b64_tr_b16 a[244:245], v225 offset:9984\n"
    "ds_read_b64_tr_b16 a[246:247], v225 offset:12032\n"
    "ds_read_b64_tr_b16 a[248:249], v225 offset:1800\n"
    "ds_read_b64_tr_b16 a[250:251], v225 offset:3848\n"
    "ds_read_b64_tr_b16 a[252:253], v225 offset:9992\n"
    "ds_read_b64_tr_b16 a[254:255], v225 offset:12040\n"
    "s_cmp_eq_u32 %14, 0\n"
    "s_cselect_b32 s81, 24576, -16\n"
    "s_and_b32 s81, s81, 0x3ffffff0\n"
    "buffer_store_dword v45, v57, s[56:59], s81 offen sc1\n"
    "s_waitcnt vmcnt(15)\n"
    "v_min3_u32 v28, v13, v15, v17\n"
    "v_max3_u32 v29, v13, v15, v17\n"
    "s_waitcnt vmcnt(10)\n"
    "v_min3_u32 v28, v28, v19, v21\n"
    "v_max3_u32 v29, v29, v19, v21\n"
    "s_waitcnt vmcnt(9)\n"
    "v_min3_u32 v28, v28, v23, v25\n"
    "v_max3_u32 v29, v29, v23, v25\n"
    "v_min3_u32 v28, v28, v27, v27\n"
    "v_max3_u32 v29, v29, v27, v27\n"
    "v_xor_b32 v28, s67, v28\n"
    "v_xor_b32 v29, s67, v29\n"
    "v_or_b32 v28, v28, v29\n"
    "v_cmp_ne_u32 vcc, 0, v28\n"
    "s_nop 1\n"
    "s_cmp_lg_u64 vcc, 0\n"
    "s_cbranch_scc1 TSC%=\n"
    "TDC%=:\n"
    "v_mul_f32 v12, 0x3e0293ee, v12\n"
    "v_mul_f32 v14, 0x3e0293ee, v14\n"
    "ds_write2_b32 v62, v12, v14 offset0:8 offset1:9\n"
    "v_mul_f32 v16, 0x3e0293ee, v16\n"
    "v_mul_f32 v18, 0x3e0293ee, v18\n"
    "ds_write2_b32 v62, v16, v18 offset0:0 offset1:1\n"
    "v_mul_f32 v20, 0x3e0293ee, v20\n"
    "v_mul_f32 v22, 0x3e0293ee, v22\n"
    "ds_write2_b32 v63, v20, v22 offset0:8 offset1:9\n"
    "v_mul_f32 v24, 0x3e0293ee, v24\n"
    "v_mul_f32 v26, 0x3e0293ee, v26\n"
    "ds_write2_b32 v63, v24, v26 offset0:0 offset1:1\n"
    "s_waitcnt lgkmcnt(0)\n"
    "ds_read_b128 v[192:195], v226 offset:0\n"
    "ds_read_b128 v[200:203], v226 offset:32\n"
    "ds_read_b128 v[196:199], v226 offset:16\n"
    "ds_read_b128 v[204:207], v226 offset:48\n"
    "s_waitcnt lgkmcnt(2)\n"
    "v_fma_mix_f32 v246, v144, v192, v200 op_sel_hi:[1,0,0]\n"
    "v_fma_mix_f32 v247, v144, v193, v201 op_sel:[1,0,0] op_sel_hi:[1,0,0]\n"
    "v_cvt_pk_bf16_f32 v144, v246, v247\n"
    "v_fma_mix_f32 v248, v145, v194, v202 op_sel_hi:[1,0,0]\n"
    "v_fma_mix_f32 v249, v145, v195, v203 op_sel:[1,0,0] op_sel_hi:[1,0,0]\n"
    "v_cvt_pk_bf16_f32 v145, v248, v249\n"
    "v_fma_mix_f32 v246, v152, v192, v200 op_sel_hi:[1,0,0]\n"
    "v_fma_mix_f32 v247, v152, v193, v201 op_sel:[1,0,0] op_sel_hi:[1,0,0]\n"
    "v_cvt_pk_bf16_f32 v152, v246, v247\n"
    "v_fma_mix_f32 v248, v153, v194, v202 op_sel_hi:[1,0,0]\n"
    "v_fma_mix_f32 v249, v153, v195, v203 op_sel:[1,0,0] op_sel_hi:[1,0,0]\n"
    "v_cvt_pk_bf16_f32 v153, v248, v249\n"
    "ds_read_b128 v[192:195], v226 offset:256\n"
    "ds_read_b128 v[200:203], v226 offset:288\n"
    "s_waitcnt lgkmcnt(2)\n"
    "v_fma_mix_f32 v246, v146, v196, v204 op_sel_hi:[1,0,0]\n"
    "v_fma_mix_f32 v247, v146, v197, v205 op_sel:[1,0,0] op_sel_hi:[1,0,0]\n"
    "v_cvt_pk_bf16_f32 v146, v246, v247\n"
    "v_fma_mix_f32 v248, v147, v198, v206 op_sel_hi:[1,0,0]\n"
    "v_fma_mix_f32 v249, v147, v199, v207 op_sel:[1,0,0] op_sel_hi:[1,0,0]\n"
    "v_cvt_pk_bf16_f32 v147, v248, v249\n"
    "v_fma_mix_f32 v246, v154, v196, v204 op_sel_hi:[1,0,0]\n"
    "v_fma_mix_f32 v247, v154, v197, v205 op_sel:[1,0,0] op_sel_hi:[1,0,0]\n"
    "v_cvt_pk_bf16_f32 v154, v246, v247\n"
    "v_fma_mix_f32 v248, v155, v198, v206 op_sel_hi:[1,0,0]\n"
    "v_fma_mix_f32 v249, v155, v199, v207 op_sel:[1,0,0] op_sel_hi:[1,0,0]\n"
    "v_cvt_pk_bf16_f32 v155, v248, v249\n"
    "ds_read_b128 v[196:199], v226 offset:272\n"
    "ds_read_b128 v[204:207], v226 offset:304\n"
    "s_waitcnt lgkmcnt(2)\n"
    "v_fma_mix_f32 v246, v148, v192, v200 op_sel_hi:[1,0,0]\n"
    "v_fma_mix_f32 v247, v148, v193, v201 op_sel:[1,0,0] op_sel_hi:[1,0,0]\n"
    "v_cvt_pk_bf16_f32 v148, v246, v247\n"
    "v_fma_mix_f32 v248, v149, v194, v202 op_sel_hi:[1,0,0]\n"
    "v_fma_mix_f32 v249, v149, v195, v203 op_sel:[1,0,0] op_sel_hi:[1,0,0]\n"
    "v_cvt_pk_bf16_f32 v149, v248, v249\n"
    "v_fma_mix_f32 v246, v156, v192, v200 op_sel_hi:[1,0,0]\n"
    "v_fma_mix_f32 v247, v156, v193, v201 op_sel:[1,0,0] op_sel_hi:[1,0,0]\n"
    "v_cvt_pk_bf16_f32 v156, v246, v247\n"
    "v_fma_mix_f32 v248, v157, v194, v202 op_sel_hi:[1,0,0]\n"
    "v_fma_mix_f32 v249, v157, v195, v203 op_sel:[1,0,0] op_sel_hi:[1,0,0]\n"
    "v_cvt_pk_bf16_f32 v157, v248, v249\n"
    "s_waitcnt lgkmcnt(0)\n"
    "v_fma_mix_f32 v246, v150, v196, v204 op_sel_hi:[1,0,0]\n"
    "v_fma_mix_f32 v247, v150, v197, v205 op_sel:[1,0,0] op_sel_hi:[1,0,0]\n"
    "v_cvt_pk_bf16_f32 v150, v246, v247\n"
    "v_fma_mix_f32 v248, v151, v198, v206 op_sel_hi:[1,0,0]\n"
    "v_fma_mix_f32 v249, v151, v199, v207 op_sel:[1,0,0] op_sel_hi:[1,0,0]\n"
    "v_cvt_pk_bf16_f32 v151, v248, v249\n"
    "v_fma_mix_f32 v246, v158, v196, v204 op_sel_hi:[1,0,0]\n"
    "v_fma_mix_f32 v247, v158, v197, v205 op_sel:[1,0,0] op_sel_hi:[1,0,0]\n"
    "v_cvt_pk_bf16_f32 v158, v246, v247\n"
    "v_fma_mix_f32 v248, v159, v198, v206 op_sel_hi:[1,0,0]\n"
    "v_fma_mix_f32 v249, v159, v199, v207 op_sel:[1,0,0] op_sel_hi:[1,0,0]\n"
    "v_cvt_pk_bf16_f32 v159, v248, v249\n"
    "v_mfma_f32_16x16x32_bf16 v[0:3], a[128:131], v[144:147], 0\n"
    "s_add_u32 s82, s90, 6144\n"
    "buffer_load_dwordx4 v[128:131], v244, s[56:59], s82 offen offset:0 sc1\n"
    "v_mfma_f32_16x16x32_bf16 v[0:3], a[132:135], v[148:151], v[0:3]\n"
    "ds_read_b128 v[192:195], v226 offset:512\n"
    "ds_read_b128 v[200:203], v226 offset:544\n"
    "v_mfma_f32_16x16x32_bf16 v[4:7], a[128:131], v[152:155], 0\n"
    "ds_read_b128 v[196:199], v226 offset:528\n"
    "ds_read_b128 v[204:207], v226 offset:560\n"
    "v_mfma_f32_16x16x32_bf16 v[4:7], a[132:135], v[156:159], v[4:7]\n"
    "buffer_load_dwordx4 v[132:135], v244, s[56:59], s82 offen offset:1024 sc1\n"
    "v_mfma_f32_16x16x32_bf16 v[8:11], a[136:139], v[144:147], 0\n"
    "s_waitcnt lgkmcnt(2)\n"
    "s_waitcnt vmcnt(10)\n"
    "v_fma_mix_f32 v246, v160, v192, v200 op_sel_hi:[1,0,0]\n"
    "v_fma_mix_f32 v247, v160, v193, v201 op_sel:[1,0,0] op_sel_hi:[1,0,0]\n"
    "v_cvt_pk_bf16_f32 v160, v246, v247\n"
    "v_mfma_f32_16x16x32_bf16 v[8:11], a[140:143], v[148:151], v[8:11]\n"
    "v_fma_mix_f32 v248, v161, v194, v202 op_sel_hi:[1,0,0]\n"
    "v_fma_mix_f32 v249, v161, v195, v203 op_sel:[1,0,0] op_sel_hi:[1,0,0]\n"
    "v_cvt_pk_bf16_f32 v161, v248, v249\n"
    "v_mfma_f32_16x16x32_bf16 v[12:15], a[136:139], v[152:155], 0\n"
    "s_waitcnt lgkmcnt(0)\n"
    "s_barrier\n"
    "s_add_u32 m0, s79, 0\n"
    "s_mov_b32 s81, 1572864\n"
    "v_mfma_f32_16x16x32_bf16 v[12:15], a[140:143], v[156:159], v[12:15]\n"
    "buffer_load_dwordx4 v228, s[48:51], s81 offen offset:0 lds\n"
    "ds_read_b64_tr_b16 a[128:129], v225 offset:16384\n"
    "v_mfma_f32_16x16x32_bf16 v[16:19], a[144:147], v[144:147], 0\n"
    "ds_read_b64_tr_b16 a[130:131], v225 offset:18432\n"
    "ds_read_b64_tr_b16 a[132:133], v225 offset:24576\n"
    "s_waitcnt vmcnt(9)\n"
    "v_fma_mix_f32 v246, v168, v192, v200 op_sel_hi:[1,0,0]\n"
    "v_fma_mix_f32 v247, v168, v193, v201 op_sel:[1,0,0] op_sel_hi:[1,0,0]\n"
    "v_mfma_f32_16x16x32_bf16 v[16:19], a[148:151], v[148:151], v[16:19]\n"
    "ds_read_b64_tr_b16 a[134:135], v225 offset:26624\n"
    "v_cvt_pk_bf16_f32 v168, v246, v247\n"
    "v_fma_mix_f32 v248, v169, v194, v202 op_sel_hi:[1,0,0]\n"
    "v_mfma_f32_16x16x32_bf16 v[20:23], a[144:147], v[152:155], 0\n"
    "ds_read_b64_tr_b16 a[136:137], v225 offset:16392\n"
    "v_fma_mix_f32 v249, v169, v195, v203 op_sel:[1,0,0] op_sel_hi:[1,0,0]\n"
    "v_cvt_pk_bf16_f32 v169, v248, v249\n"
    "v_mfma_f32_16x16x32_bf16 v[20:23], a[148:151], v[156:159], v[20:23]\n"
    "buffer_load_dwordx4 v229, s[48:51], s81 offen offset:1024 lds\n"
    "v_mfma_f32_16x16x32_bf16 v[24:27], a[152:155], v[144:147], 0\n"
    "ds_read_b64_tr_b16 a[138:139], v225 offset:18440\n"
    "ds_read_b64_tr_b16 a[140:141], v225 offset:24584\n"
    "ds_read_b128 v[192:195], v226 offset:768\n"
    "v_mfma_f32_16x16x32_bf16 v[24:27], a[156:159], v[148:151], v[24:27]\n"
    "ds_read_b64_tr_b16 a[142:143], v225 offset:26632\n"
    "ds_read_b128 v[200:203], v226 offset:800\n"
    "v_mfma_f32_16x16x32_bf16 v[28:31], a[152:155], v[152:155], 0\n"
    "ds_read_b64_tr_b16 a[144:145], v225 offset:16640\n"
    "ds_read_b64_tr_b16 a[146:147], v225 offset:18688\n"
    "v_fma_mix_f32 v246, v162, v196, v204 op_sel_hi:[1,0,0]\n"
    "v_mfma_f32_16x16x32_bf16 v[28:31], a[156:159], v[156:159], v[28:31]\n"
    "buffer_load_dwordx4 v230, s[48:51], s81 offen offset:2048 lds\n"
    "v_mfma_f32_16x16x32_bf16 v[32:35], a[160:163], v[144:147], 0\n"
    "ds_read_b64_tr_b16 a[148:149], v225 offset:24832\n"
    "ds_read_b64_tr_b16 a[150:151], v225 offset:26880\n"
    "v_fma_mix_f32 v247, v162, v197, v205 op_sel:[1,0,0] op_sel_hi:[1,0,0]\n"
    "v_mfma_f32_16x16x32_bf16 v[32:35], a[164:167], v[148:151], v[32:35]\n"
    "ds_read_b64_tr_b16 a[152:153], v225 offset:16648\n"
    "v_cvt_pk_bf16_f32 v162, v246, v247\n"
    "v_mfma_f32_16x16x32_bf16 v[36:39], a[160:163], v[152:155], 0\n"
    "ds_read_b64_tr_b16 a[154:155], v225 offset:18696\n"
    "v_fma_mix_f32 v248, v163, v198, v206 op_sel_hi:[1,0,0]\n"
    "v_mfma_f32_16x16x32_bf16 v[36:39], a[164:167], v[156:159], v[36:39]\n"
    "buffer_load_dwordx4 v231, s[48:51], s81 offen offset:3072 lds\n"
    "v_mfma_f32_16x16x32_bf16 v[40:43], a[168:171], v[144:147], 0\n"
    "s_add_u32 m0, s79, 49160\n"
    "ds_read_b64_tr_b16 a[156:157], v225 offset:24840\n"
    "ds_read_b64_tr_b16 a[158:159], v225 offset:26888\n"
    "v_fma_mix_f32 v249, v163, v199, v207 op_sel:[1,0,0] op_sel_hi:[1,0,0]\n"
    "v_mfma_f32_16x16x32_bf16 v[40:43], a[172:175], v[148:151], v[40:43]\n"
    "s_mov_b32 s81, 1638400\n"
    "ds_read_b64_tr_b16 a[160:161], v225 offset:16896\n"
    "ds_read_b64_tr_b16 a[162:163], v225 offset:18944\n"
    "v_mfma_f32_16x16x32_bf16 v[44:47], a[168:171], v[152:155], 0\n"
    "ds_read_b64_tr_b16 a[164:165], v225 offset:25088\n"
    "v_cvt_pk_bf16_f32 v163, v248, v249\n"
    "v_mfma_f32_16x16x32_bf16 v[44:47], a[172:175], v[156:159], v[44:47]\n"
    "buffer_load_dwordx4 v228, s[48:51], s81 offen offset:0 lds\n"
    "v_mfma_f32_16x16x32_bf16 v[48:51], a[176:179], v[144:147], 0\n"
    "ds_read_b64_tr_b16 a[166:167], v225 offset:27136\n"
    "ds_read_b64_tr_b16 a[168:169], v225 offset:16904\n"
    "v_fma_mix_f32 v246, v170, v196, v204 op_sel_hi:[1,0,0]\n"
    "v_mfma_f32_16x16x32_bf16 v[48:51], a[180:183], v[148:151], v[48:51]\n"
    "ds_read_b64_tr_b16 a[170:171], v225 offset:18952\n"
    "v_fma_mix_f32 v247, v170, v197, v205 op_sel:[1,0,0] op_sel_hi:[1,0,0]\n"
    "v_mfma_f32_16x16x32_bf16 v[52:55], a[176:179], v[152:155], 0\n"
    "ds_read_b64_tr_b16 a[172:173], v225 offset:25096\n"
    "v_cvt_pk_bf16_f32 v170, v246, v247\n"
    "v_mfma_f32_16x16x32_bf16 v[52:55], a[180:183], v[156:159], v[52:55]\n"
    "buffer_load_dwordx4 v229, s[48:51], s81 offen offset:1024 lds\n"
    "v_mfma_f32_16x16x32_bf16 v[56:59], a[184:187], v[144:147], 0\n"
    "ds_read_b64_tr_b16 a[174:175], v225 offset:27144\n"
    "ds_read_b64_tr_b16 a[176:177], v225 offset:17152\n"
    "ds_read_b64_tr_b16 a[178:179], v225 offset:19200\n"
    "v_fma_mix_f32 v248, v171, v198, v206 op_sel_hi:[1,0,0]\n"
    "v_mfma_f32_16x16x32_bf16 v[56:59], a[188:191], v[148:151], v[56:59]\n"
    "ds_read_b64_tr_b16 a[180:181], v225 offset:25344\n"
    "v_fma_mix_f32 v249, v171, v199, v207 op_sel:[1,0,0] op_sel_hi:[1,0,0]\n"
    "v_mfma_f32_16x16x32_bf16 v[60:63], a[184:187], v[152:155], 0\n"
    "ds_read_b64_tr_b16 a[182:183], v225 offset:27392\n"
    "v_cvt_pk_bf16_f32 v171, v248, v249\n"
    "v_mfma_f32_16x16x32_bf16 v[60:63], a[188:191], v[156:159], v[60:63]\n"
    "buffer_load_dwordx4 v230, s[48:51], s81 offen offset:2048 lds\n"
    "v_mfma_f32_16x16x32_bf16 v[64:67], a[192:195], v[144:147], 0\n"
    "ds_read_b64_tr_b16 a[184:185], v225 offset:17160\n"
    "ds_read_b64_tr_b16 a[186:187], v225 offset:19208\n"
    "ds_read_b128 v[196:199], v226 offset:784\n"
    "v_mfma_f32_16x16x32_bf16 v[64:67], a[196:199], v[148:151], v[64:67]\n"
    "ds_read_b64_tr_b16 a[188:189], v225 offset:25352\n"
    "ds_read_b128 v[204:207], v226 offset:816\n"
    "v_mfma_f32_16x16x32_bf16 v[68:71], a[192:195], v[152:155], 0\n"
    "ds_read_b64_tr_b16 a[190:191], v225 offset:27400\n"
    "s_waitcnt lgkmcnt(6)\n"
    "v_fma_mix_f32 v246, v164, v192, v200 op_sel_hi:[1,0,0]\n"
    "v_mfma_f32_16x16x32_bf16 v[68:71], a[196:199], v[156:159], v[68:71]\n"
    "buffer_load_dwordx4 v231, s[48:51], s81 offen offset:3072 lds\n"
    "v_mfma_f32_16x16x32_bf16 v[72:75], a[200:203], v[144:147], 0\n"
    "ds_read_b64_tr_b16 a[192:193], v225 offset:17408\n"
    "ds_read_b64_tr_b16 a[194:195], v225 offset:19456\n"
    "ds_read_b64_tr_b16 a[196:197], v225 offset:25600\n"
    "v_fma_mix_f32 v247, v164, v193, v201 op_sel:[1,0,0] op_sel_hi:[1,0,0]\n"
    "v_mfma_f32_16x16x32_bf16 v[72:75], a[204:207], v[148:151], v[72:75]\n"
    "ds_read_b64_tr_b16 a[198:199], v225 offset:27648\n"
    "v_cvt_pk_bf16_f32 v164, v246, v247\n"
    "v_mfma_f32_16x16x32_bf16 v[76:79], a[200:203], v[152:155], 0\n"
    "ds_read_b64_tr_b16 a[200:201], v225 offset:17416\n"
    "v_fma_mix_f32 v248, v165, v194, v202 op_sel_hi:[1,0,0]\n"
    "v_mfma_f32_16x16x32_bf16 v[76:79], a[204:207], v[156:159], v[76:79]\n"
    "buffer_load_dwordx4 v[136:139], v245, s[56:59], s82 offen offset:0 sc1\n"
    "v_mfma_f32_16x16x32_bf16 v[80:83], a[208:211], v[144:147], 0\n"
    "ds_read_b64_tr_b16 a[202:203], v225 offset:19464\n"
    "ds_read_b64_tr_b16 a[204:205], v225 offset:25608\n"
    "v_fma_mix_f32 v249, v165, v195, v203 op_sel:[1,0,0] op_sel_hi:[1,0,0]\n"
    "v_mfma_f32_16x16x32_bf16 v[80:83], a[212:215], v[148:151], v[80:83]\n"
    "ds_read_b64_tr_b16 a[206:207], v225 offset:27656\n"
    "v_cvt_pk_bf16_f32 v165, v248, v249\n"
    "v_mfma_f32_16x16x32_bf16 v[84:87], a[208:211], v[152:155], 0\n"
    "ds_read_b64_tr_b16 a[208:209], v225 offset:17664\n"
    "ds_read_b64_tr_b16 a[210:211], v225 offset:19712\n"
    "s_waitcnt vmcnt(16)\n"
    "v_fma_mix_f32 v246, v172, v192, v200 op_sel_hi:[1,0,0]\n"
    "v_mfma_f32_16x16x32_bf16 v[84:87], a[212:215], v[156:159], v[84:87]\n"
    "buffer_load_dwordx4 v[140:143], v245, s[56:59], s82 offen offset:1024 sc1\n"
    "v_mfma_f32_16x16x32_bf16 v[88:91], a[216:219], v[144:147], 0\n"
    "ds_read_b64_tr_b16 a[212:213], v225 offset:25856\n"
    "v_fma_mix_f32 v247, v172, v193, v201 op_sel:[1,0,0] op_sel_hi:[1,0,0]\n"
    "v_mfma_f32_16x16x32_bf16 v[88:91], a[220:223], v[148:151], v[88:91]\n"
    "ds_read_b64_tr_b16 a[214:215], v225 offset:27904\n"
    "v_cvt_pk_bf16_f32 v172, v246, v247\n"
    "v_mfma_f32_16x16x32_bf16 v[92:95], a[216:219], v[152:155], 0\n"
    "ds_read_b64_tr_b16 a[216:217], v225 offset:17672\n"
    "v_fma_mix_f32 v248, v173, v194, v202 op_sel_hi:[1,0,0]\n"
    "v_mfma_f32_16x16x32_bf16 v[92:95], a[220:223], v[156:159], v[92:95]\n"
    "ds_read_b64_tr_b16 a[218:219], v225 offset:19720\n"
    "v_fma_mix_f32 v249, v173, v195, v203 op_sel:[1,0,0] op_sel_hi:[1,0,0]\n"
    "v_mfma_f32_16x16x32_bf16 v[96:99], a[224:227], v[144:147], 0\n"
    "ds_read_b64_tr_b16 a[220:221], v225 offset:25864\n"
    "v_cvt_pk_bf16_f32 v173, v248, v249\n"
    "v_mfma_f32_16x16x32_bf16 v[96:99], a[228:231], v[148:151], v[96:99]\n"
    "ds_read_b64_tr_b16 a[222:223], v225 offset:27912\n"
    "s_waitcnt lgkmcnt(4)\n"
    "v_fma_mix_f32 v246, v166, v196, v204 op_sel_hi:[1,0,0]\n"
    "v_mfma_f32_16x16x32_bf16 v[100:103], a[224:227], v[152:155], 0\n"
    "ds_read_b64_tr_b16 a[224:225], v225 offset:17920\n"
    "v_fma_mix_f32 v247, v166, v197, v205 op_sel:[1,0,0] op_sel_hi:[1,0,0]\n"
    "v_mfma_f32_16x16x32_bf16 v[100:103], a[228:231], v[156:159], v[100:103]\n"
    "ds_read_b64_tr_b16 a[226:227], v225 offset:19968\n"
    "v_cvt_pk_bf16_f32 v166, v246, v247\n"
    "v_mfma_f32_16x16x32_bf16 v[104:107], a[232:235], v[144:147], 0\n"
    "ds_read_b64_tr_b16 a[228:229], v225 offset:26112\n"
    "v_fma_mix_f32 v248, v167, v198, v206 op_sel_hi:[1,0,0]\n"
    "v_mfma_f32_16x16x32_bf16 v[104:107], a[236:239], v[148:151], v[104:107]\n"
    "ds_read_b64_tr_b16 a[230:231], v225 offset:28160\n"
    "v_fma_mix_f32 v249, v167, v199, v207 op_sel:[1,0,0] op_sel_hi:[1,0,0]\n"
    "v_mfma_f32_16x16x32_bf16 v[108:111], a[232:235], v[152:155], 0\n"
    "ds_read_b64_tr_b16 a[232:233], v225 offset:17928\n"
    "v_cvt_pk_bf16_f32 v167, v248, v249\n"
    "v_mfma_f32_16x16x32_bf16 v[108:111], a[236:239], v[156:159], v[108:111]\n"
    "ds_read_b64_tr_b16 a[234:235], v225 offset:19976\n"
    "v_fma_mix_f32 v246, v174, v196, v204 op_sel_hi:[1,0,0]\n"
    "v_mfma_f32_16x16x32_bf16 v[112:115], a[240:243], v[144:147], 0\n"
    "ds_read_b64_tr_b16 a[236:237], v225 offset:26120\n"
    "v_fma_mix_f32 v247, v174, v197, v205 op_sel:[1,0,0] op_sel_hi:[1,0,0]\n"
    "v_mfma_f32_16x16x32_bf16 v[112:115], a[244:247], v[148:151], v[112:115]\n"
    "ds_read_b64_tr_b16 a[238:239], v225 offset:28168\n"
    "v_cvt_pk_bf16_f32 v174, v246, v247\n"
    "v_mfma_f32_16x16x32_bf16 v[116:119], a[240:243], v[152:155], 0\n"
    "ds_read_b64_tr_b16 a[240:241], v225 offset:18176\n"
    "v_fma_mix_f32 v248, v175, v198, v206 op_sel_hi:[1,0,0]\n"
    "v_mfma_f32_16x16x32_bf16 v[116:119], a[244:247], v[156:159], v[116:119]\n"
    "ds_read_b64_tr_b16 a[242:243], v225 offset:20224\n"
    "v_fma_mix_f32 v249, v175, v199, v207 op_sel:[1,0,0] op_sel_hi:[1,0,0]\n"
    "v_mfma_f32_16x16x32_bf16 v[120:123], a[248:251], v[144:147], 0\n"
    "ds_read_b64_tr_b16 a[244:245], v225 offset:26368\n"
    "v_cvt_pk_bf16_f32 v175, v248, v249\n"
    "v_mfma_f32_16x16x32_bf16 v[120:123], a[252:255], v[148:151], v[120:123]\n"
    "ds_read_b64_tr_b16 a[246:247], v225 offset:28416\n"
    "ds_read_b128 v[192:195], v226 offset:1024\n"
    "ds_read_b128 v[200:203], v226 offset:1056\n"
    "v_mfma_f32_16x16x32_bf16 v[124:127], a[248:251], v[152:155], 0\n"
    "ds_read_b64_tr_b16 a[248:249], v225 offset:18184\n"
    "ds_read_b64_tr_b16 a[250:251], v225 offset:20232\n"
    "ds_read_b128 v[196:199], v226 offset:1040\n"
    "v_mfma_f32_16x16x32_bf16 v[124:127], a[252:255], v[156:159], v[124:127]\n"
    "ds_read_b64_tr_b16 a[252:253], v225 offset:26376\n"
    "ds_read_b128 v[204:207], v226 offset:1072\n"
    "v_mfma_f32_16x16x32_bf16 v[0:3], a[128:131], v[160:163], v[0:3]\n"
    "s_add_u32 s82, s90, 8192\n"
    "buffer_load_dwordx4 v[144:147], v244, s[56:59], s82 offen offset:0 sc1\n"
    "v_mfma_f32_16x16x32_bf16 v[0:3], a[132:135], v[164:167], v[0:3]\n"
    "ds_read_b64_tr_b16 a[254:255], v225 offset:28424\n"
    "s_waitcnt lgkmcnt(5)\n"
    "s_waitcnt vmcnt(17)\n"
    "v_fma_mix_f32 v246, v176, v192, v200 op_sel_hi:[1,0,0]\n"
    "v_mfma_f32_16x16x32_bf16 v[4:7], a[128:131], v[168:171], v[4:7]\n"
    "v_fma_mix_f32 v247, v176, v193, v201 op_sel:[1,0,0] op_sel_hi:[1,0,0]\n"
    "v_cvt_pk_bf16_f32 v176, v246, v247\n"
    "v_mfma_f32_16x16x32_bf16 v[4:7], a[132:135], v[172:175], v[4:7]\n"
    "buffer_load_dwordx4 v[148:151], v244, s[56:59], s82 offen offset:1024 sc1\n"
    "v_mfma_f32_16x16x32_bf16 v[8:11], a[136:139], v[160:163], v[8:11]\n"
    "v_fma_mix_f32 v248, v177, v194, v202 op_sel_hi:[1,0,0]\n"
    "v_fma_mix_f32 v249, v177, v195, v203 op_sel:[1,0,0] op_sel_hi:[1,0,0]\n"
    "v_cvt_pk_bf16_f32 v177, v248, v249\n"
    "v_mfma_f32_16x16x32_bf16 v[8:11], a[140:143], v[164:167], v[8:11]\n"
    "s_waitcnt vmcnt(16)\n"
    "v_fma_mix_f32 v246, v184, v192, v200 op_sel_hi:[1,0,0]\n"
    "v_fma_mix_f32 v247, v184, v193, v201 op_sel:[1,0,0] op_sel_hi:[1,0,0]\n"
    "v_cvt_pk_bf16_f32 v184, v246, v247\n"
    "v_mfma_f32_16x16x32_bf16 v[12:15], a[136:139], v[168:171], v[12:15]\n"
    "s_waitcnt lgkmcnt(0)\n"
    "s_barrier\n"
    "s_add_u32 m0, s79, 16384\n"
    "v_mfma_f32_16x16x32_bf16 v[12:15], a[140:143], v[172:175], v[12:15]\n"
    "s_mov_b32 s81, 2097152\n"
    "buffer_load_dwordx4 v228, s[48:51], s81 offen offset:0 lds\n"
    "v_mfma_f32_16x16x32_bf16 v[16:19], a[144:147], v[160:163], v[16:19]\n"
    "ds_read_b64_tr_b16 a[128:129], v225 offset:32768\n"
    "ds_read_b64_tr_b16 a[130:131], v225 offset:34816\n"
    "ds_read_b64_tr_b16 a[132:133], v225 offset:40960\n"
    "v_fma_mix_f32 v248, v185, v194, v202 op_sel_hi:[1,0,0]\n"
    "v_mfma_f32_16x16x32_bf16 v[16:19], a[148:151], v[164:167], v[16:19]\n"
    "ds_read_b64_tr_b16 a[134:135], v225 offset:43008\n"
    "v_fma_mix_f32 v249, v185, v195, v203 op_sel:[1,0,0] op_sel_hi:[1,0,0]\n"
    "v_mfma_f32_16x16x32_bf16 v[20:23], a[144:147], v[168:171], v[20:23]\n"
    "ds_read_b64_tr_b16 a[136:137], v225 offset:32776\n"
    "v_cvt_pk_bf16_f32 v185, v248, v249\n"
    "v_mfma_f32_16x16x32_bf16 v[20:23], a[148:151], v[172:175], v[20:23]\n"
    "buffer_load_dwordx4 v229, s[48:51], s81 offen offset:1024 lds\n"
    "v_mfma_f32_16x16x32_bf16 v[24:27], a[152:155], v[160:163], v[24:27]\n"
    "ds_read_b64_tr_b16 a[138:139], v225 offset:34824\n"
    "ds_read_b64_tr_b16 a[140:141], v225 offset:40968\n"
    "ds_read_b128 v[192:195], v226 offset:1280\n"
    "v_mfma_f32_16x16x32_bf16 v[24:27], a[156:159], v[164:167], v[24:27]\n"
    "ds_read_b64_tr_b16 a[142:143], v225 offset:43016\n"
    "ds_read_b128 v[200:203], v226 offset:1312\n"
    "v_mfma_f32_16x16x32_bf16 v[28:31], a[152:155], v[168:171], v[28:31]\n"
    "ds_read_b64_tr_b16 a[144:145], v225 offset:33024\n"
    "ds_read_b64_tr_b16 a[146:147], v225 offset:35072\n"
    "v_fma_mix_f32 v246, v178, v196, v204 op_sel_hi:[1,0,0]\n"
    "v_mfma_f32_16x16x32_bf16 v[28:31], a[156:159], v[172:175], v[28:31]\n"
    "buffer_load_dwordx4 v230, s[48:51], s81 offen offset:2048 lds\n"
    "v_mfma_f32_16x16x32_bf16 v[32:35], a[160:163], v[160:163], v[32:35]\n"
    "ds_read_b64_tr_b16 a[148:149], v225 offset:41216\n"
    "ds_read_b64_tr_b16 a[150:151], v225 offset:43264\n"
    "v_fma_mix_f32 v247, v178, v197, v205 op_sel:[1,0,0] op_sel_hi:[1,0,0]\n"
    "v_mfma_f32_16x16x32_bf16 v[32:35], a[164:167], v[164:167], v[32:35]\n"
    "ds_read_b64_tr_b16 a[152:153], v225 offset:33032\n"
    "v_cvt_pk_bf16_f32 v178, v246, v247\n"
    "v_mfma_f32_16x16x32_bf16 v[36:39], a[160:163], v[168:171], v[36:39]\n"
    "ds_read_b64_tr_b16 a[154:155], v225 offset:35080\n"
    "v_fma_mix_f32 v248, v179, v198, v206 op_sel_hi:[1,0,0]\n"
    "v_mfma_f32_16x16x32_bf16 v[36:39], a[164:167], v[172:175], v[36:39]\n"
    "buffer_load_dwordx4 v231, s[48:51], s81 offen offset:3072 lds\n"
    "v_mfma_f32_16x16x32_bf16 v[40:43], a[168:171], v[160:163], v[40:43]\n"
    "s_add_u32 m0, s79, 65544\n"
    "ds_read_b64_tr_b16 a[156:157], v225 offset:41224\n"
    "ds_read_b64_tr_b16 a[158:159], v225 offset:43272\n"
    "v_fma_mix_f32 v249, v179, v199, v207 op_sel:[1,0,0] op_sel_hi:[1,0,0]\n"
    "v_mfma_f32_16x16x32_bf16 v[40:43], a[172:175], v[164:167], v[40:43]\n"
    "s_mov_b32 s81, 2162688\n"
    "ds_read_b64_tr_b16 a[160:161], v225 offset:33280\n"
    "ds_read_b64_tr_b16 a[162:163], v225 offset:35328\n"
    "v_mfma_f32_16x16x32_bf16 v[44:47], a[168:171], v[168:171], v[44:47]\n"
    "ds_read_b64_tr_b16 a[164:165], v225 offset:41472\n"
    "v_cvt_pk_bf16_f32 v179, v248, v249\n"
    "v_mfma_f32_16x16x32_bf16 v[44:47], a[172:175], v[172:175], v[44:47]\n"
    "buffer_load_dwordx4 v228, s[48:51], s81 offen offset:0 lds\n"
    "v_mfma_f32_16x16x32_bf16 v[48:51], a[176:179], v[160:163], v[48:51]\n"
    "ds_read_b64_tr_b16 a[166:167], v225 offset:43520\n"
    "ds_read_b64_tr_b16 a[168:169], v225 offset:33288\n"
    "v_fma_mix_f32 v246, v186, v196, v204 op_sel_hi:[1,0,0]\n"
    "v_mfma_f32_16x16x32_bf16 v[48:51], a[180:183], v[164:167], v[48:51]\n"
    "ds_read_b64_tr_b16 a[170:171], v225 offset:35336\n"
    "v_fma_mix_f32 v247, v186, v197, v205 op_sel:[1,0,0] op_sel_hi:[1,0,0]\n"
    "v_mfma_f32_16x16x32_bf16 v[52:55], a[176:179], v[168:171], v[52:55]\n"
    "ds_read_b64_tr_b16 a[172:173], v225 offset:41480\n"
    "v_cvt_pk_bf16_f32 v186, v246, v247\n"
    "v_mfma_f32_16x16x32_bf16 v[52:55], a[180:183], v[172:175], v[52:55]\n"
    "buffer_load_dwordx4 v229, s[48:51], s81 offen offset:1024 lds\n"
    "v_mfma_f32_16x16x32_bf16 v[56:59], a[184:187], v[160:163], v[56:59]\n"
    "ds_read_b64_tr_b16 a[174:175], v225 offset:43528\n"
    "ds_read_b64_tr_b16 a[176:177], v225 offset:33536\n"
    "ds_read_b64_tr_b16 a[178:179], v225 offset:35584\n"
    "v_fma_mix_f32 v248, v187, v198, v206 op_sel_hi:[1,0,0]\n"
    "v_mfma_f32_16x16x32_bf16 v[56:59], a[188:191], v[164:167], v[56:59]\n"
    "ds_read_b64_tr_b16 a[180:181], v225 offset:41728\n"
    "v_fma_mix_f32 v249, v187, v199, v207 op_sel:[1,0,0] op_sel_hi:[1,0,0]\n"
    "v_mfma_f32_16x16x32_bf16 v[60:63], a[184:187], v[168:171], v[60:63]\n"
    "ds_read_b64_tr_b16 a[182:183], v225 offset:43776\n"
    "v_cvt_pk_bf16_f32 v187, v248, v249\n"
    "v_mfma_f32_16x16x32_bf16 v[60:63], a[188:191], v[172:175], v[60:63]\n"
    "buffer_load_dwordx4 v230, s[48:51], s81 offen offset:2048 lds\n"
    "v_mfma_f32_16x16x32_bf16 v[64:67], a[192:195], v[160:163], v[64:67]\n"
    "ds_read_b64_tr_b16 a[184:185], v225 offset:33544\n"
    "ds_read_b64_tr_b16 a[186:187], v225 offset:35592\n"
    "ds_read_b128 v[196:199], v226 offset:1296\n"
    "v_mfma_f32_16x16x32_bf16 v[64:67], a[196:199], v[164:167], v[64:67]\n"
    "ds_read_b64_tr_b16 a[188:189], v225 offset:41736\n"
    "ds_read_b128 v[204:207], v226 offset:1328\n"
    "v_mfma_f32_16x16x32_bf16 v[68:71], a[192:195], v[168:171], v[68:71]\n"
    "ds_read_b64_tr_b16 a[190:191], v225 offset:43784\n"
    "s_waitcnt lgkmcnt(6)\n"
    "v_fma_mix_f32 v246, v180, v192, v200 op_sel_hi:[1,0,0]\n"
    "v_mfma_f32_16x16x32_bf16 v[68:71], a[196:199], v[172:175], v[68:71]\n"
    "buffer_load_dwordx4 v231, s[48:51], s81 offen offset:3072 lds\n"
    "v_mfma_f32_16x16x32_bf16 v[72:75], a[200:203], v[160:163], v[72:75]\n"
    "ds_read_b64_tr_b16 a[192:193], v225 offset:33792\n"
    "ds_read_b64_tr_b16 a[194:195], v225 offset:35840\n"
    "ds_read_b64_tr_b16 a[196:197], v225 offset:41984\n"
    "v_fma_mix_f32 v247, v180, v193, v201 op_sel:[1,0,0] op_sel_hi:[1,0,0]\n"
    "v_mfma_f32_16x16x32_bf16 v[72:75], a[204:207], v[164:167], v[72:75]\n"
    "ds_read_b64_tr_b16 a[198:199], v225 offset:44032\n"
    "v_cvt_pk_bf16_f32 v180, v246, v247\n"
    "v_mfma_f32_16x16x32_bf16 v[76:79], a[200:203], v[168:171], v[76:79]\n"
    "ds_read_b64_tr_b16 a[200:201], v225 offset:33800\n"
    "v_fma_mix_f32 v248, v181, v194, v202 op_sel_hi:[1,0,0]\n"
    "v_mfma_f32_16x16x32_bf16 v[76:79], a[204:207], v[172:175], v[76:79]\n"
    "buffer_load_dwordx4 v[152:155], v245, s[56:59], s82 offen offset:0 sc1\n"
    "v_mfma_f32_16x16x32_bf16 v[80:83], a[208:211], v[160:163], v[80:83]\n"
    "ds_read_b64_tr_b16 a[202:203], v225 offset:35848\n"
    "ds_read_b64_tr_b16 a[204:205], v225 offset:41992\n"
    "v_fma_mix_f32 v249, v181, v195, v203 op_sel:[1,0,0] op_sel_hi:[1,0,0]\n"
    "v_mfma_f32_16x16x32_bf16 v[80:83], a[212:215], v[164:167], v[80:83]\n"
    "ds_read_b64_tr_b16 a[206:207], v225 offset:44040\n"
    "v_cvt_pk_bf16_f32 v181, v248, v249\n"
    "v_mfma_f32_16x16x32_bf16 v[84:87], a[208:211], v[168:171], v[84:87]\n"
    "ds_read_b64_tr_b16 a[208:209], v225 offset:34048\n"
    "ds_read_b64_tr_b16 a[210:211], v225 offset:36096\n"
    "s_waitcnt vmcnt(24)\n"
    "v_fma_mix_f32 v246, v188, v192, v200 op_sel_hi:[1,0,0]\n"
    "v_mfma_f32_16x16x32_bf16 v[84:87], a[212:215], v[172:175], v[84:87]\n"
    "buffer_load_dwordx4 v[156:159], v245, s[56:59], s82 offen offset:1024 sc1\n"
    "v_mfma_f32_16x16x32_bf16 v[88:91], a[216:219], v[160:163], v[88:91]\n"
    "ds_read_b64_tr_b16 a[212:213], v225 offset:42240\n"
    "v_fma_mix_f32 v247, v188, v193, v201 op_sel:[1,0,0] op_sel_hi:[1,0,0]\n"
    "v_mfma_f32_16x16x32_bf16 v[88:91], a[220:223], v[164:167], v[88:91]\n"
    "ds_read_b64_tr_b16 a[214:215], v225 offset:44288\n"
    "v_cvt_pk_bf16_f32 v188, v246, v247\n"
    "v_mfma_f32_16x16x32_bf16 v[92:95], a[216:219], v[168:171], v[92:95]\n"
    "ds_read_b64_tr_b16 a[216:217], v225 offset:34056\n"
    "v_fma_mix_f32 v248, v189, v194, v202 op_sel_hi:[1,0,0]\n"
    "v_mfma_f32_16x16x32_bf16 v[92:95], a[220:223], v[172:175], v[92:95]\n"
    "ds_read_b64_tr_b16 a[218:219], v225 offset:36104\n"
    "v_fma_mix_f32 v249, v189, v195, v203 op_sel:[1,0,0] op_sel_hi:[1,0,0]\n"
    "v_mfma_f32_16x16x32_bf16 v[96:99], a[224:227], v[160:163], v[96:99]\n"
    "ds_read_b64_tr_b16 a[220:221], v225 offset:42248\n"
    "v_cvt_pk_bf16_f32 v189, v248, v249\n"
    "v_mfma_f32_16x16x32_bf16 v[96:99], a[228:231], v[164:167], v[96:99]\n"
    "ds_read_b64_tr_b16 a[222:223], v225 offset:44296\n"
    "s_waitcnt lgkmcnt(4)\n"
    "v_fma_mix_f32 v246, v182, v196, v204 op_sel_hi:[1,0,0]\n"
    "v_mfma_f32_16x16x32_bf16 v[100:103], a[224:227], v[168:171], v[100:103]\n"
    "ds_read_b64_tr_b16 a[224:225], v225 offset:34304\n"
    "v_fma_mix_f32 v247, v182, v197, v205 op_sel:[1,0,0] op_sel_hi:[1,0,0]\n"
    "v_mfma_f32_16x16x32_bf16 v[100:103], a[228:231], v[172:175], v[100:103]\n"
    "ds_read_b64_tr_b16 a[226:227], v225 offset:36352\n"
    "v_cvt_pk_bf16_f32 v182, v246, v247\n"
    "v_mfma_f32_16x16x32_bf16 v[104:107], a[232:235], v[160:163], v[104:107]\n"
    "ds_read_b64_tr_b16 a[228:229], v225 offset:42496\n"
    "v_fma_mix_f32 v248, v183, v198, v206 op_sel_hi:[1,0,0]\n"
    "v_mfma_f32_16x16x32_bf16 v[104:107], a[236:239], v[164:167], v[104:107]\n"
    "ds_read_b64_tr_b16 a[230:231], v225 offset:44544\n"
    "v_fma_mix_f32 v249, v183, v199, v207 op_sel:[1,0,0] op_sel_hi:[1,0,0]\n"
    "v_mfma_f32_16x16x32_bf16 v[108:111], a[232:235], v[168:171], v[108:111]\n"
    "ds_read_b64_tr_b16 a[232:233], v225 offset:34312\n"
    "v_cvt_pk_bf16_f32 v183, v248, v249\n"
    "v_mfma_f32_16x16x32_bf16 v[108:111], a[236:239], v[172:175], v[108:111]\n"
    "ds_read_b64_tr_b16 a[234:235], v225 offset:36360\n"
    "v_fma_mix_f32 v246, v190, v196, v204 op_sel_hi:[1,0,0]\n"
    "v_mfma_f32_16x16x32_bf16 v[112:115], a[240:243], v[160:163], v[112:115]\n"
    "ds_read_b64_tr_b16 a[236:237], v225 offset:42504\n"
    "v_fma_mix_f32 v247, v190, v197, v205 op_sel:[1,0,0] op_sel_hi:[1,0,0]\n"
    "v_mfma_f32_16x16x32_bf16 v[112:115], a[244:247], v[164:167], v[112:115]\n"
    "ds_read_b64_tr_b16 a[238:239], v225 offset:44552\n"
    "v_cvt_pk_bf16_f32 v190, v246, v247\n"
    "v_mfma_f32_16x16x32_bf16 v[116:119], a[240:243], v[168:171], v[116:119]\n"
    "ds_read_b64_tr_b16 a[240:241], v225 offset:34560\n"
    "v_fma_mix_f32 v248, v191, v198, v206 op_sel_hi:[1,0,0]\n"
    "v_mfma_f32_16x16x32_bf16 v[116:119], a[244:247], v[172:175], v[116:119]\n"
    "ds_read_b64_tr_b16 a[242:243], v225 offset:36608\n"
    "v_fma_mix_f32 v249, v191, v199, v207 op_sel:[1,0,0] op_sel_hi:[1,0,0]\n"
    "v_mfma_f32_16x16x32_bf16 v[120:123], a[248:251], v[160:163], v[120:123]\n"
    "ds_read_b64_tr_b16 a[244:245], v225 offset:42752\n"
    "v_cvt_pk_bf16_f32 v191, v248, v249\n"
    "v_mfma_f32_16x16x32_bf16 v[120:123], a[252:255], v[164:167], v[120:123]\n"
    "ds_read_b64_tr_b16 a[246:247], v225 offset:44800\n"
    "ds_read_b128 v[192:195], v226 offset:1536\n"
    "ds_read_b128 v[200:203], v226 offset:1568\n"
    "v_mfma_f32_16x16x32_bf16 v[124:127], a[248:251], v[168:171], v[124:127]\n"
    "ds_read_b64_tr_b16 a[248:249], v225 offset:34568\n"
    "ds_read_b64_tr_b16 a[250:251], v225 offset:36616\n"
    "ds_read_b128 v[196:199], v226 offset:1552\n"
    "v_mfma_f32_16x16x32_bf16 v[124:127], a[252:255], v[172:175], v[124:127]\n"
    "ds_read_b64_tr_b16 a[252:253], v225 offset:42760\n"
    "ds_read_b128 v[204:207], v226 offset:1584\n"
    "v_mfma_f32_16x16x32_bf16 v[0:3], a[128:131], v[176:179], v[0:3]\n"
    "s_add_u32 s82, s90, 10240\n"
    "buffer_load_dwordx4 v[160:163], v244, s[56:59], s82 offen offset:0 sc1\n"
    "v_mfma_f32_16x16x32_bf16 v[0:3], a[132:135], v[180:183], v[0:3]\n"
    "ds_read_b64_tr_b16 a[254:255], v225 offset:44808\n"
    "s_waitcnt lgkmcnt(5)\n"
    "s_waitcnt vmcnt(24)\n"
    "v_fma_mix_f32 v246, v128, v192, v200 op_sel_hi:[1,0,0]\n"
    "v_fma_mix_f32 v247, v128, v193, v201 op_sel:[1,0,0] op_sel_hi:[1,0,0]\n"
    "v_mfma_f32_16x16x32_bf16 v[4:7], a[128:131], v[184:187], v[4:7]\n"
    "v_cvt_pk_bf16_f32 v128, v246, v247\n"
    "v_fma_mix_f32 v248, v129, v194, v202 op_sel_hi:[1,0,0]\n"
    "v_mfma_f32_16x16x32_bf16 v[4:7], a[132:135], v[188:191], v[4:7]\n"
    "buffer_load_dwordx4 v[164:167], v244, s[56:59], s82 offen offset:1024 sc1\n"
    "v_mfma_f32_16x16x32_bf16 v[8:11], a[136:139], v[176:179], v[8:11]\n"
    "v_fma_mix_f32 v249, v129, v195, v203 op_sel:[1,0,0] op_sel_hi:[1,0,0]\n"
    "v_cvt_pk_bf16_f32 v129, v248, v249\n"
    "s_waitcnt vmcnt(15)\n"
    "v_fma_mix_f32 v246, v136, v192, v200 op_sel_hi:[1,0,0]\n"
    "v_mfma_f32_16x16x32_bf16 v[8:11], a[140:143], v[180:183], v[8:11]\n"
    "v_fma_mix_f32 v247, v136, v193, v201 op_sel:[1,0,0] op_sel_hi:[1,0,0]\n"
    "v_cvt_pk_bf16_f32 v136, v246, v247\n"
    "v_mfma_f32_16x16x32_bf16 v[12:15], a[136:139], v[184:187], v[12:15]\n"
    "s_waitcnt lgkmcnt(0)\n"
    "s_barrier\n"
    "s_add_u32 m0, s79, 32768\n"
    "v_mfma_f32_16x16x32_bf16 v[12:15], a[140:143], v[188:191], v[12:15]\n"
    "s_mov_b32 s81, 2621440\n"
    "buffer_load_dwordx4 v228, s[48:51], s81 offen offset:0 lds\n"
    "v_mfma_f32_16x16x32_bf16 v[16:19], a[144:147], v[176:179], v[16:19]\n"
    "ds_read_b64_tr_b16 a[128:129], v225 offset:0\n"
    "ds_read_b64_tr_b16 a[130:131], v225 offset:2048\n"
    "ds_read_b64_tr_b16 a[132:133], v225 offset:8192\n"
    "v_fma_mix_f32 v248, v137, v194, v202 op_sel_hi:[1,0,0]\n"
    "v_mfma_f32_16x16x32_bf16 v[16:19], a[148:151], v[180:183], v[16:19]\n"
    "ds_read_b64_tr_b16 a[134:135], v225 offset:10240\n"
    "v_fma_mix_f32 v249, v137, v195, v203 op_sel:[1,0,0] op_sel_hi:[1,0,0]\n"
    "v_mfma_f32_16x16x32_bf16 v[20:23], a[144:147], v[184:187], v[20:23]\n"
    "ds_read_b64_tr_b16 a[136:137], v225 offset:8\n"
    "v_cvt_pk_bf16_f32 v137, v248, v249\n"
    "v_mfma_f32_16x16x32_bf16 v[20:23], a[148:151], v[188:191], v[20:23]\n"
    "buffer_load_dwordx4 v229, s[48:51], s81 offen offset:1024 lds\n"
    "v_mfma_f32_16x16x32_bf16 v[24:27], a[152:155], v[176:179], v[24:27]\n"
    "ds_read_b64_tr_b16 a[138:139], v225 offset:2056\n"
    "ds_read_b64_tr_b16 a[140:141], v225 offset:8200\n"
    "ds_read_b128 v[192:195], v226 offset:1792\n"
    "v_mfma_f32_16x16x32_bf16 v[24:27], a[156:159], v[180:183], v[24:27]\n"
    "ds_read_b64_tr_b16 a[142:143], v225 offset:10248\n"
    "ds_read_b128 v[200:203], v226 offset:1824\n"
    "v_mfma_f32_16x16x32_bf16 v[28:31], a[152:155], v[184:187], v[28:31]\n"
    "ds_read_b64_tr_b16 a[144:145], v225 offset:256\n"
    "ds_read_b64_tr_b16 a[146:147], v225 offset:2304\n"
    "v_fma_mix_f32 v246, v130, v196, v204 op_sel_hi:[1,0,0]\n"
    "v_mfma_f32_16x16x32_bf16 v[28:31], a[156:159], v[188:191], v[28:31]\n"
    "buffer_load_dwordx4 v230, s[48:51], s81 offen offset:2048 lds\n"
    "v_mfma_f32_16x16x32_bf16 v[32:35], a[160:163], v[176:179], v[32:35]\n"
    "ds_read_b64_tr_b16 a[148:149], v225 offset:8448\n"
    "ds_read_b64_tr_b16 a[150:151], v225 offset:10496\n"
    "v_fma_mix_f32 v247, v130, v197, v205 op_sel:[1,0,0] op_sel_hi:[1,0,0]\n"
    "v_mfma_f32_16x16x32_bf16 v[32:35], a[164:167], v[180:183], v[32:35]\n"
    "ds_read_b64_tr_b16 a[152:153], v225 offset:264\n"
    "v_cvt_pk_bf16_f32 v130, v246, v247\n"
    "v_mfma_f32_16x16x32_bf16 v[36:39], a[160:163], v[184:187], v[36:39]\n"
    "ds_read_b64_tr_b16 a[154:155], v225 offset:2312\n"
    "v_fma_mix_f32 v248, v131, v198, v206 op_sel_hi:[1,0,0]\n"
    "v_mfma_f32_16x16x32_bf16 v[36:39], a[164:167], v[188:191], v[36:39]\n"
    "buffer_load_dwordx4 v231, s[48:51], s81 offen offset:3072 lds\n"
    "v_mfma_f32_16x16x32_bf16 v[40:43], a[168:171], v[176:179], v[40:43]\n"
    "s_add_u32 m0, s79, 81928\n"
    "ds_read_b64_tr_b16 a[156:157], v225 offset:8456\n"
    "ds_read_b64_tr_b16 a[158:159], v225 offset:10504\n"
    "v_fma_mix_f32 v249, v131, v199, v207 op_sel:[1,0,0] op_sel_hi:[1,0,0]\n"
    "v_mfma_f32_16x16x32_bf16 v[40:43], a[172:175], v[180:183], v[40:43]\n"
    "s_mov_b32 s81, 2686976\n"
    "ds_read_b64_tr_b16 a[160:161], v225 offset:512\n"
    "ds_read_b64_tr_b16 a[162:163], v225 offset:2560\n"
    "v_mfma_f32_16x16x32_bf16 v[44:47], a[168:171], v[184:187], v[44:47]\n"
    "ds_read_b64_tr_b16 a[164:165], v225 offset:8704\n"
    "v_cvt_pk_bf16_f32 v131, v248, v249\n"
    "v_mfma_f32_16x16x32_bf16 v[44:47], a[172:175], v[188:191], v[44:47]\n"
    "buffer_load_dwordx4 v228, s[48:51], s81 offen offset:0 lds\n"
    "v_mfma_f32_16x16x32_bf16 v[48:51], a[176:179], v[176:179], v[48:51]\n"
    "ds_read_b64_tr_b16 a[166:167], v225 offset:10752\n"
    "ds_read_b64_tr_b16 a[168:169], v225 offset:520\n"
    "v_fma_mix_f32 v246, v138, v196, v204 op_sel_hi:[1,0,0]\n"
    "v_mfma_f32_16x16x32_bf16 v[48:51], a[180:183], v[180:183], v[48:51]\n"
    "ds_read_b64_tr_b16 a[170:171], v225 offset:2568\n"
    "v_fma_mix_f32 v247, v138, v197, v205 op_sel:[1,0,0] op_sel_hi:[1,0,0]\n"
    "v_mfma_f32_16x16x32_bf16 v[52:55], a[176:179], v[184:187], v[52:55]\n"
    "ds_read_b64_tr_b16 a[172:173], v225 offset:8712\n"
    "v_cvt_pk_bf16_f32 v138, v246, v247\n"
    "v_mfma_f32_16x16x32_bf16 v[52:55], a[180:183], v[188:191], v[52:55]\n"
    "buffer_load_dwordx4 v229, s[48:51], s81 offen offset:1024 lds\n"
    "v_mfma_f32_16x16x32_bf16 v[56:59], a[184:187], v[176:179], v[56:59]\n"
    "ds_read_b64_tr_b16 a[174:175], v225 offset:10760\n"
    "ds_read_b64_tr_b16 a[176:177], v225 offset:768\n"
    "ds_read_b64_tr_b16 a[178:179], v225 offset:2816\n"
    "v_fma_mix_f32 v248, v139, v198, v206 op_sel_hi:[1,0,0]\n"
    "v_mfma_f32_16x16x32_bf16 v[56:59], a[188:191], v[180:183], v[56:59]\n"
    "ds_read_b64_tr_b16 a[180:181], v225 offset:8960\n"
    "v_fma_mix_f32 v249, v139, v199, v207 op_sel:[1,0,0] op_sel_hi:[1,0,0]\n"
    "v_mfma_f32_16x16x32_bf16 v[60:63], a[184:187], v[184:187], v[60:63]\n"
    "ds_read_b64_tr_b16 a[182:183], v225 offset:11008\n"
    "v_cvt_pk_bf16_f32 v139, v248, v249\n"
    "v_mfma_f32_16x16x32_bf16 v[60:63], a[188:191], v[188:191], v[60:63]\n"
    "buffer_load_dwordx4 v230, s[48:51], s81 offen offset:2048 lds\n"
    "v_mfma_f32_16x16x32_bf16 v[64:67], a[192:195], v[176:179], v[64:67]\n"
    "ds_read_b64_tr_b16 a[184:185], v225 offset:776\n"
    "ds_read_b64_tr_b16 a[186:187], v225 offset:2824\n"
    "ds_read_b128 v[196:199], v226 offset:1808\n"
    "v_mfma_f32_16x16x32_bf16 v[64:67], a[196:199], v[180:183], v[64:67]\n"
    "ds_read_b64_tr_b16 a[188:189], v225 offset:8968\n"
    "ds_read_b128 v[204:207], v226 offset:1840\n"
    "v_mfma_f32_16x16x32_bf16 v[68:71], a[192:195], v[184:187], v[68:71]\n"
    "ds_read_b64_tr_b16 a[190:191], v225 offset:11016\n"
    "s_waitcnt lgkmcnt(6)\n"
    "v_fma_mix_f32 v246, v132, v192, v200 op_sel_hi:[1,0,0]\n"
    "v_mfma_f32_16x16x32_bf16 v[68:71], a[196:199], v[188:191], v[68:71]\n"
    "buffer_load_dwordx4 v231, s[48:51], s81 offen offset:3072 lds\n"
    "v_mfma_f32_16x16x32_bf16 v[72:75], a[200:203], v[176:179], v[72:75]\n"
    "ds_read_b64_tr_b16 a[192:193], v225 offset:1024\n"
    "ds_read_b64_tr_b16 a[194:195], v225 offset:3072\n"
    "ds_read_b64_tr_b16 a[196:197], v225 offset:9216\n"
    "v_fma_mix_f32 v247, v132, v193, v201 op_sel:[1,0,0] op_sel_hi:[1,0,0]\n"
    "v_mfma_f32_16x16x32_bf16 v[72:75], a[204:207], v[180:183], v[72:75]\n"
    "ds_read_b64_tr_b16 a[198:199], v225 offset:11264\n"
    "v_cvt_pk_bf16_f32 v132, v246, v247\n"
    "v_mfma_f32_16x16x32_bf16 v[76:79], a[200:203], v[184:187], v[76:79]\n"
    "ds_read_b64_tr_b16 a[200:201], v225 offset:1032\n"
    "v_fma_mix_f32 v248, v133, v194, v202 op_sel_hi:[1,0,0]\n"
    "v_mfma_f32_16x16x32_bf16 v[76:79], a[204:207], v[188:191], v[76:79]\n"
    "buffer_load_dwordx4 v[168:171], v245, s[56:59], s82 offen offset:0 sc1\n"
    "v_mfma_f32_16x16x32_bf16 v[80:83], a[208:211], v[176:179], v[80:83]\n"
    "ds_read_b64_tr_b16 a[202:203], v225 offset:3080\n"
    "ds_read_b64_tr_b16 a[204:205], v225 offset:9224\n"
    "v_fma_mix_f32 v249, v133, v195, v203 op_sel:[1,0,0] op_sel_hi:[1,0,0]\n"
    "v_mfma_f32_16x16x32_bf16 v[80:83], a[212:215], v[180:183], v[80:83]\n"
    "ds_read_b64_tr_b16 a[206:207], v225 offset:11272\n"
    "v_cvt_pk_bf16_f32 v133, v248, v249\n"
    "v_mfma_f32_16x16x32_bf16 v[84:87], a[208:211], v[184:187], v[84:87]\n"
    "ds_read_b64_tr_b16 a[208:209], v225 offset:1280\n"
    "ds_read_b64_tr_b16 a[210:211], v225 offset:3328\n"
    "s_waitcnt vmcnt(23)\n"
    "v_fma_mix_f32 v246, v140, v192, v200 op_sel_hi:[1,0,0]\n"
    "v_mfma_f32_16x16x32_bf16 v[84:87], a[212:215], v[188:191], v[84:87]\n"
    "buffer_load_dwordx4 v[172:175], v245, s[56:59], s82 offen offset:1024 sc1\n"
    "v_mfma_f32_16x16x32_bf16 v[88:91], a[216:219], v[176:179], v[88:91]\n"
    "ds_read_b64_tr_b16 a[212:213], v225 offset:9472\n"
    "v_fma_mix_f32 v247, v140, v193, v201 op_sel:[1,0,0] op_sel_hi:[1,0,0]\n"
    "v_mfma_f32_16x16x32_bf16 v[88:91], a[220:223], v[180:183], v[88:91]\n"
    "ds_read_b64_tr_b16 a[214:215], v225 offset:11520\n"
    "v_cvt_pk_bf16_f32 v140, v246, v247\n"
    "v_mfma_f32_16x16x32_bf16 v[92:95], a[216:219], v[184:187], v[92:95]\n"
    "ds_read_b64_tr_b16 a[216:217], v225 offset:1288\n"
    "v_fma_mix_f32 v248, v141, v194, v202 op_sel_hi:[1,0,0]\n"
    "v_mfma_f32_16x16x32_bf16 v[92:95], a[220:223], v[188:191], v[92:95]\n"
    "ds_read_b64_tr_b16 a[218:219], v225 offset:3336\n"
    "v_fma_mix_f32 v249, v141, v195, v203 op_sel:[1,0,0] op_sel_hi:[1,0,0]\n"
    "v_mfma_f32_16x16x32_bf16 v[96:99], a[224:227], v[176:179], v[96:99]\n"
    "ds_read_b64_tr_b16 a[220:221], v225 offset:9480\n"
    "v_cvt_pk_bf16_f32 v141, v248, v249\n"
    "v_mfma_f32_16x16x32_bf16 v[96:99], a[228:231], v[180:183], v[96:99]\n"
    "ds_read_b64_tr_b16 a[222:223], v225 offset:11528\n"
    "s_waitcnt lgkmcnt(4)\n"
    "v_fma_mix_f32 v246, v134, v196, v204 op_sel_hi:[1,0,0]\n"
    "v_mfma_f32_16x16x32_bf16 v[100:103], a[224:227], v[184:187], v[100:103]\n"
    "ds_read_b64_tr_b16 a[224:225], v225 offset:1536\n"
    "v_fma_mix_f32 v247, v134, v197, v205 op_sel:[1,0,0] op_sel_hi:[1,0,0]\n"
    "v_mfma_f32_16x16x32_bf16 v[100:103], a[228:231], v[188:191], v[100:103]\n"
    "ds_read_b64_tr_b16 a[226:227], v225 offset:3584\n"
    "v_cvt_pk_bf16_f32 v134, v246, v247\n"
    "v_mfma_f32_16x16x32_bf16 v[104:107], a[232:235], v[176:179], v[104:107]\n"
    "ds_read_b64_tr_b16 a[228:229], v225 offset:9728\n"
    "v_fma_mix_f32 v248, v135, v198, v206 op_sel_hi:[1,0,0]\n"
    "v_mfma_f32_16x16x32_bf16 v[104:107], a[236:239], v[180:183], v[104:107]\n"
    "ds_read_b64_tr_b16 a[230:231], v225 offset:11776\n"
    "v_fma_mix_f32 v249, v135, v199, v207 op_sel:[1,0,0] op_sel_hi:[1,0,0]\n"
    "v_mfma_f32_16x16x32_bf16 v[108:111], a[232:235], v[184:187], v[108:111]\n"
    "ds_read_b64_tr_b16 a[232:233], v225 offset:1544\n"
    "v_cvt_pk_bf16_f32 v135, v248, v249\n"
    "v_mfma_f32_16x16x32_bf16 v[108:111], a[236:239], v[188:191], v[108:111]\n"
    "ds_read_b64_tr_b16 a[234:235], v225 offset:3592\n"
    "v_fma_mix_f32 v246, v142, v196, v204 op_sel_hi:[1,0,0]\n"
    "v_mfma_f32_16x16x32_bf16 v[112:115], a[240:243], v[176:179], v[112:115]\n"
    "ds_read_b64_tr_b16 a[236:237], v225 offset:9736\n"
    "v_fma_mix_f32 v247, v142, v197, v205 op_sel:[1,0,0] op_sel_hi:[1,0,0]\n"
    "v_mfma_f32_16x16x32_bf16 v[112:115], a[244:247], v[180:183], v[112:115]\n"
    "ds_read_b64_tr_b16 a[238:239], v225 offset:11784\n"
    "v_cvt_pk_bf16_f32 v142, v246, v247\n"
    "v_mfma_f32_16x16x32_bf16 v[116:119], a[240:243], v[184:187], v[116:119]\n"
    "ds_read_b64_tr_b16 a[240:241], v225 offset:1792\n"
    "v_fma_mix_f32 v248, v143, v198, v206 op_sel_hi:[1,0,0]\n"
    "v_mfma_f32_16x16x32_bf16 v[116:119], a[244:247], v[188:191], v[116:119]\n"
    "ds_read_b64_tr_b16 a[242:243], v225 offset:3840\n"
    "v_fma_mix_f32 v249, v143, v199, v207 op_sel:[1,0,0] op_sel_hi:[1,0,0]\n"
    "v_mfma_f32_16x16x32_bf16 v[120:123], a[248:251], v[176:179], v[120:123]\n"
    "ds_read_b64_tr_b16 a[244:245], v225 offset:9984\n"
    "v_cvt_pk_bf16_f32 v143, v248, v249\n"
    "v_mfma_f32_16x16x32_bf16 v[120:123], a[252:255], v[180:183], v[120:123]\n"
    "ds_read_b64_tr_b16 a[246:247], v225 offset:12032\n"
    "ds_read_b128 v[192:195], v226 offset:2048\n"
    "ds_read_b128 v[200:203], v226 offset:2080\n"
    "v_mfma_f32_16x16x32_bf16 v[124:127], a[248:251], v[184:187], v[124:127]\n"
    "ds_read_b64_tr_b16 a[248:249], v225 offset:1800\n"
    "ds_read_b64_tr_b16 a[250:251], v225 offset:3848\n"
    "ds_read_b128 v[196:199], v226 offset:2064\n"
    "v_mfma_f32_16x16x32_bf16 v[124:127], a[252:255], v[188:191], v[124:127]\n"
    "ds_read_b64_tr_b16 a[252:253], v225 offset:9992\n"
    "ds_read_b64_tr_b16 a[254:255], v225 offset:12040\n"
    "ds_read_b128 v[204:207], v226 offset:2096\n"
    "v_mfma_f32_16x16x32_bf16 v[0:3], a[128:131], v[128:131], v[0:3]\n"
    "s_add_u32 s82, s90, 12288\n"
    "buffer_load_dwordx4 v[176:179], v244, s[56:59], s82 offen offset:0 sc1\n"
    "v_mfma_f32_16x16x32_bf16 v[0:3], a[132:135], v[132:135], v[0:3]\n"
    "s_waitcnt lgkmcnt(5)\n"
    "s_waitcnt vmcnt(24)\n"
    "v_fma_mix_f32 v246, v144, v192, v200 op_sel_hi:[1,0,0]\n"
    "v_fma_mix_f32 v247, v144, v193, v201 op_sel:[1,0,0] op_sel_hi:[1,0,0]\n"
    "v_mfma_f32_16x16x32_bf16 v[4:7], a[128:131], v[136:139], v[4:7]\n"
    "v_cvt_pk_bf16_f32 v144, v246, v247\n"
    "v_fma_mix_f32 v248, v145, v194, v202 op_sel_hi:[1,0,0]\n"
    "v_mfma_f32_16x16x32_bf16 v[4:7], a[132:135], v[140:143], v[4:7]\n"
    "buffer_load_dwordx4 v[180:183], v244, s[56:59], s82 offen offset:1024 sc1\n"
    "v_mfma_f32_16x16x32_bf16 v[8:11], a[136:139], v[128:131], v[8:11]\n"
    "v_fma_mix_f32 v249, v145, v195, v203 op_sel:[1,0,0] op_sel_hi:[1,0,0]\n"
    "v_cvt_pk_bf16_f32 v145, v248, v249\n"
    "s_waitcnt vmcnt(15)\n"
    "v_fma_mix_f32 v246, v152, v192, v200 op_sel_hi:[1,0,0]\n"
    "v_mfma_f32_16x16x32_bf16 v[8:11], a[140:143], v[132:135], v[8:11]\n"
    "v_fma_mix_f32 v247, v152, v193, v201 op_sel:[1,0,0] op_sel_hi:[1,0,0]\n"
    "v_cvt_pk_bf16_f32 v152, v246, v247\n"
    "v_mfma_f32_16x16x32_bf16 v[12:15], a[136:139], v[136:139], v[12:15]\n"
    "s_waitcnt lgkmcnt(0)\n"
    "s_barrier\n"
    "s_add_u32 m0, s79, 0\n"
    "v_mfma_f32_16x16x32_bf16 v[12:15], a[140:143], v[140:143], v[12:15]\n"
    "s_mov_b32 s81, 3145728\n"
    "buffer_load_dwordx4 v228, s[48:51], s81 offen offset:0 lds\n"
    "v_mfma_f32_16x16x32_bf16 v[16:19], a[144:147], v[128:131], v[16:19]\n"
    "ds_read_b64_tr_b16 a[128:129], v225 offset:16384\n"
    "ds_read_b64_tr_b16 a[130:131], v225 offset:18432\n"
    "ds_read_b64_tr_b16 a[132:133], v225 offset:24576\n"
    "v_fma_mix_f32 v248, v153, v194, v202 op_sel_hi:[1,0,0]\n"
    "v_mfma_f32_16x16x32_bf16 v[16:19], a[148:151], v[132:135], v[16:19]\n"
    "ds_read_b64_tr_b16 a[134:135], v225 offset:26624\n"
    "v_fma_mix_f32 v249, v153, v195, v203 op_sel:[1,0,0] op_sel_hi:[1,0,0]\n"
    "v_mfma_f32_16x16x32_bf16 v[20:23], a[144:147], v[136:139], v[20:23]\n"
    "ds_read_b64_tr_b16 a[136:137], v225 offset:16392\n"
    "v_cvt_pk_bf16_f32 v153, v248, v249\n"
    "v_mfma_f32_16x16x32_bf16 v[20:23], a[148:151], v[140:143], v[20:23]\n"
    "buffer_load_dwordx4 v229, s[48:51], s81 offen offset:1024 lds\n"
    "v_mfma_f32_16x16x32_bf16 v[24:27], a[152:155], v[128:131], v[24:27]\n"
    "ds_read_b64_tr_b16 a[138:139], v225 offset:18440\n"
    "ds_read_b64_tr_b16 a[140:141], v225 offset:24584\n"
    "ds_read_b128 v[192:195], v226 offset:2304\n"
    "v_mfma_f32_16x16x32_bf16 v[24:27], a[156:159], v[132:135], v[24:27]\n"
    "ds_read_b64_tr_b16 a[142:143], v225 offset:26632\n"
    "ds_read_b128 v[200:203], v226 offset:2336\n"
    "v_mfma_f32_16x16x32_bf16 v[28:31], a[152:155], v[136:139], v[28:31]\n"
    "ds_read_b64_tr_b16 a[144:145], v225 offset:16640\n"
    "ds_read_b64_tr_b16 a[146:147], v225 offset:18688\n"
    "v_fma_mix_f32 v246, v146, v196, v204 op_sel_hi:[1,0,0]\n"
    "v_mfma_f32_16x16x32_bf16 v[28:31], a[156:159], v[140:143], v[28:31]\n"
    "buffer_load_dwordx4 v230, s[48:51], s81 offen offset:2048 lds\n"
    "v_mfma_f32_16x16x32_bf16 v[32:35], a[160:163], v[128:131], v[32:35]\n"
    "ds_read_b64_tr_b16 a[148:149], v225 offset:24832\n"
    "ds_read_b64_tr_b16 a[150:151], v225 offset:26880\n"
    "v_fma_mix_f32 v247, v146, v197, v205 op_sel:[1,0,0] op_sel_hi:[1,0,0]\n"
    "v_mfma_f32_16x16x32_bf16 v[32:35], a[164:167], v[132:135], v[32:35]\n"
    "ds_read_b64_tr_b16 a[152:153], v225 offset:16648\n"
    "v_cvt_pk_bf16_f32 v146, v246, v247\n"
    "v_mfma_f32_16x16x32_bf16 v[36:39], a[160:163], v[136:139], v[36:39]\n"
    "ds_read_b64_tr_b16 a[154:155], v225 offset:18696\n"
    "v_fma_mix_f32 v248, v147, v198, v206 op_sel_hi:[1,0,0]\n"
    "v_mfma_f32_16x16x32_bf16 v[36:39], a[164:167], v[140:143], v[36:39]\n"
    "buffer_load_dwordx4 v231, s[48:51], s81 offen offset:3072 lds\n"
    "v_mfma_f32_16x16x32_bf16 v[40:43], a[168:171], v[128:131], v[40:43]\n"
    "s_add_u32 m0, s79, 49160\n"
    "ds_read_b64_tr_b16 a[156:157], v225 offset:24840\n"
    "ds_read_b64_tr_b16 a[158:159], v225 offset:26888\n"
    "v_fma_mix_f32 v249, v147, v199, v207 op_sel:[1,0,0] op_sel_hi:[1,0,0]\n"
    "v_mfma_f32_16x16x32_bf16 v[40:43], a[172:175], v[132:135], v[40:43]\n"
    "s_mov_b32 s81, 3211264\n"
    "ds_read_b64_tr_b16 a[160:161], v225 offset:16896\n"
    "ds_read_b64_tr_b16 a[162:163], v225 offset:18944\n"
    "v_mfma_f32_16x16x32_bf16 v[44:47], a[168:171], v[136:139], v[44:47]\n"
    "ds_read_b64_tr_b16 a[164:165], v225 offset:25088\n"
    "v_cvt_pk_bf16_f32 v147, v248, v249\n"
    "v_mfma_f32_16x16x32_bf16 v[44:47], a[172:175], v[140:143], v[44:47]\n"
    "buffer_load_dwordx4 v228, s[48:51], s81 offen offset:0 lds\n"
    "v_mfma_f32_16x16x32_bf16 v[48:51], a[176:179], v[128:131], v[48:51]\n"
    "ds_read_b64_tr_b16 a[166:167], v225 offset:27136\n"
    "ds_read_b64_tr_b16 a[168:169], v225 offset:16904\n"
    "v_fma_mix_f32 v246, v154, v196, v204 op_sel_hi:[1,0,0]\n"
    "v_mfma_f32_16x16x32_bf16 v[48:51], a[180:183], v[132:135], v[48:51]\n"
    "ds_read_b64_tr_b16 a[170:171], v225 offset:18952\n"
    "v_fma_mix_f32 v247, v154, v197, v205 op_sel:[1,0,0] op_sel_hi:[1,0,0]\n"
    "v_mfma_f32_16x16x32_bf16 v[52:55], a[176:179], v[136:139], v[52:55]\n"
    "ds_read_b64_tr_b16 a[172:173], v225 offset:25096\n"
    "v_cvt_pk_bf16_f32 v154, v246, v247\n"
    "v_mfma_f32_16x16x32_bf16 v[52:55], a[180:183], v[140:143], v[52:55]\n"
    "buffer_load_dwordx4 v229, s[48:51], s81 offen offset:1024 lds\n"
    "v_mfma_f32_16x16x32_bf16 v[56:59], a[184:187], v[128:131], v[56:59]\n"
    "ds_read_b64_tr_b16 a[174:175], v225 offset:27144\n"
    "ds_read_b64_tr_b16 a[176:177], v225 offset:17152\n"
    "ds_read_b64_tr_b16 a[178:179], v225 offset:19200\n"
    "v_fma_mix_f32 v248, v155, v198, v206 op_sel_hi:[1,0,0]\n"
    "v_mfma_f32_16x16x32_bf16 v[56:59], a[188:191], v[132:135], v[56:59]\n"
    "ds_read_b64_tr_b16 a[180:181], v225 offset:25344\n"
    "v_fma_mix_f32 v249, v155, v199, v207 op_sel:[1,0,0] op_sel_hi:[1,0,0]\n"
    "v_mfma_f32_16x16x32_bf16 v[60:63], a[184:187], v[136:139], v[60:63]\n"
    "ds_read_b64_tr_b16 a[182:183], v225 offset:27392\n"
    "v_cvt_pk_bf16_f32 v155, v248, v249\n"
    "v_mfma_f32_16x16x32_bf16 v[60:63], a[188:191], v[140:143], v[60:63]\n"
    "buffer_load_dwordx4 v230, s[48:51], s81 offen offset:2048 lds\n"
    "v_mfma_f32_16x16x32_bf16 v[64:67], a[192:195], v[128:131], v[64:67]\n"
    "ds_read_b64_tr_b16 a[184:185], v225 offset:17160\n"
    "ds_read_b64_tr_b16 a[186:187], v225 offset:19208\n"
    "ds_read_b128 v[196:199], v226 offset:2320\n"
    "v_mfma_f32_16x16x32_bf16 v[64:67], a[196:199], v[132:135], v[64:67]\n"
    "ds_read_b64_tr_b16 a[188:189], v225 offset:25352\n"
    "ds_read_b128 v[204:207], v226 offset:2352\n"
    "v_mfma_f32_16x16x32_bf16 v[68:71], a[192:195], v[136:139], v[68:71]\n"
    "ds_read_b64_tr_b16 a[190:191], v225 offset:27400\n"
    "s_waitcnt lgkmcnt(6)\n"
    "v_fma_mix_f32 v246, v148, v192, v200 op_sel_hi:[1,0,0]\n"
    "v_mfma_f32_16x16x32_bf16 v[68:71], a[196:199], v[140:143], v[68:71]\n"
    "buffer_load_dwordx4 v231, s[48:51], s81 offen offset:3072 lds\n"
    "v_mfma_f32_16x16x32_bf16 v[72:75], a[200:203], v[128:131], v[72:75]\n"
    "ds_read_b64_tr_b16 a[192:193], v225 offset:17408\n"
    "ds_read_b64_tr_b16 a[194:195], v225 offset:19456\n"
    "ds_read_b64_tr_b16 a[196:197], v225 offset:25600\n"
    "v_fma_mix_f32 v247, v148, v193, v201 op_sel:[1,0,0] op_sel_hi:[1,0,0]\n"
    "v_mfma_f32_16x16x32_bf16 v[72:75], a[204:207], v[132:135], v[72:75]\n"
    "ds_read_b64_tr_b16 a[198:199], v225 offset:27648\n"
    "v_cvt_pk_bf16_f32 v148, v246, v247\n"
    "v_mfma_f32_16x16x32_bf16 v[76:79], a[200:203], v[136:139], v[76:79]\n"
    "ds_read_b64_tr_b16 a[200:201], v225 offset:17416\n"
    "v_fma_mix_f32 v248, v149, v194, v202 op_sel_hi:[1,0,0]\n"
    "v_mfma_f32_16x16x32_bf16 v[76:79], a[204:207], v[140:143], v[76:79]\n"
    "buffer_load_dwordx4 v[184:187], v245, s[56:59], s82 offen offset:0 sc1\n"
    "v_mfma_f32_16x16x32_bf16 v[80:83], a[208:211], v[128:131], v[80:83]\n"
    "ds_read_b64_tr_b16 a[202:203], v225 offset:19464\n"
    "ds_read_b64_tr_b16 a[204:205], v225 offset:25608\n"
    "v_fma_mix_f32 v249, v149, v195, v203 op_sel:[1,0,0] op_sel_hi:[1,0,0]\n"
    "v_mfma_f32_16x16x32_bf16 v[80:83], a[212:215], v[132:135], v[80:83]\n"
    "ds_read_b64_tr_b16 a[206:207], v225 offset:27656\n"
    "v_cvt_pk_bf16_f32 v149, v248, v249\n"
    "v_mfma_f32_16x16x32_bf16 v[84:87], a[208:211], v[136:139], v[84:87]\n"
    "ds_read_b64_tr_b16 a[208:209], v225 offset:17664\n"
    "ds_read_b64_tr_b16 a[210:211], v225 offset:19712\n"
    "s_waitcnt vmcnt(23)\n"
    "v_fma_mix_f32 v246, v156, v192, v200 op_sel_hi:[1,0,0]\n"
    "v_mfma_f32_16x16x32_bf16 v[84:87], a[212:215], v[140:143], v[84:87]\n"
    "buffer_load_dwordx4 v[188:191], v245, s[56:59], s82 offen offset:1024 sc1\n"
    "v_mfma_f32_16x16x32_bf16 v[88:91], a[216:219], v[128:131], v[88:91]\n"
    "ds_read_b64_tr_b16 a[212:213], v225 offset:25856\n"
    "v_fma_mix_f32 v247, v156, v193, v201 op_sel:[1,0,0] op_sel_hi:[1,0,0]\n"
    "v_mfma_f32_16x16x32_bf16 v[88:91], a[220:223], v[132:135], v[88:91]\n"
    "ds_read_b64_tr_b16 a[214:215], v225 offset:27904\n"
    "v_cvt_pk_bf16_f32 v156, v246, v247\n"
    "v_mfma_f32_16x16x32_bf16 v[92:95], a[216:219], v[136:139], v[92:95]\n"
    "ds_read_b64_tr_b16 a[216:217], v225 offset:17672\n"
    "v_fma_mix_f32 v248, v157, v194, v202 op_sel_hi:[1,0,0]\n"
    "v_mfma_f32_16x16x32_bf16 v[92:95], a[220:223], v[140:143], v[92:95]\n"
    "ds_read_b64_tr_b16 a[218:219], v225 offset:19720\n"
    "v_fma_mix_f32 v249, v157, v195, v203 op_sel:[1,0,0] op_sel_hi:[1,0,0]\n"
    "v_mfma_f32_16x16x32_bf16 v[96:99], a[224:227], v[128:131], v[96:99]\n"
    "ds_read_b64_tr_b16 a[220:221], v225 offset:25864\n"
    "v_cvt_pk_bf16_f32 v157, v248, v249\n"
    "v_mfma_f32_16x16x32_bf16 v[96:99], a[228:231], v[132:135], v[96:99]\n"
    "ds_read_b64_tr_b16 a[222:223], v225 offset:27912\n"
    "s_waitcnt lgkmcnt(4)\n"
    "v_fma_mix_f32 v246, v150, v196, v204 op_sel_hi:[1,0,0]\n"
    "v_mfma_f32_16x16x32_bf16 v[100:103], a[224:227], v[136:139], v[100:103]\n"
    "ds_read_b64_tr_b16 a[224:225], v225 offset:17920\n"
    "v_fma_mix_f32 v247, v150, v197, v205 op_sel:[1,0,0] op_sel_hi:[1,0,0]\n"
    "v_mfma_f32_16x16x32_bf16 v[100:103], a[228:231], v[140:143], v[100:103]\n"
    "ds_read_b64_tr_b16 a[226:227], v225 offset:19968\n"
    "v_cvt_pk_bf16_f32 v150, v246, v247\n"
    "v_mfma_f32_16x16x32_bf16 v[104:107], a[232:235], v[128:131], v[104:107]\n"
    "ds_read_b64_tr_b16 a[228:229], v225 offset:26112\n"
    "v_fma_mix_f32 v248, v151, v198, v206 op_sel_hi:[1,0,0]\n"
    "v_mfma_f32_16x16x32_bf16 v[104:107], a[236:239], v[132:135], v[104:107]\n"
    "ds_read_b64_tr_b16 a[230:231], v225 offset:28160\n"
    "v_fma_mix_f32 v249, v151, v199, v207 op_sel:[1,0,0] op_sel_hi:[1,0,0]\n"
    "v_mfma_f32_16x16x32_bf16 v[108:111], a[232:235], v[136:139], v[108:111]\n"
    "ds_read_b64_tr_b16 a[232:233], v225 offset:17928\n"
    "v_cvt_pk_bf16_f32 v151, v248, v249\n"
    "v_mfma_f32_16x16x32_bf16 v[108:111], a[236:239], v[140:143], v[108:111]\n"
    "ds_read_b64_tr_b16 a[234:235], v225 offset:19976\n"
    "v_fma_mix_f32 v246, v158, v196, v204 op_sel_hi:[1,0,0]\n"
    "v_mfma_f32_16x16x32_bf16 v[112:115], a[240:243], v[128:131], v[112:115]\n"
    "ds_read_b64_tr_b16 a[236:237], v225 offset:26120\n"
    "v_fma_mix_f32 v247, v158, v197, v205 op_sel:[1,0,0] op_sel_hi:[1,0,0]\n"
    "v_mfma_f32_16x16x32_bf16 v[112:115], a[244:247], v[132:135], v[112:115]\n"
    "ds_read_b64_tr_b16 a[238:239], v225 offset:28168\n"
    "v_cvt_pk_bf16_f32 v158, v246, v247\n"
    "v_mfma_f32_16x16x32_bf16 v[116:119], a[240:243], v[136:139], v[116:119]\n"
    "ds_read_b64_tr_b16 a[240:241], v225 offset:18176\n"
    "v_fma_mix_f32 v248, v159, v198, v206 op_sel_hi:[1,0,0]\n"
    "v_mfma_f32_16x16x32_bf16 v[116:119], a[244:247], v[140:143], v[116:119]\n"
    "ds_read_b64_tr_b16 a[242:243], v225 offset:20224\n"
    "v_fma_mix_f32 v249, v159, v199, v207 op_sel:[1,0,0] op_sel_hi:[1,0,0]\n"
    "v_mfma_f32_16x16x32_bf16 v[120:123], a[248:251], v[128:131], v[120:123]\n"
    "ds_read_b64_tr_b16 a[244:245], v225 offset:26368\n"
    "v_cvt_pk_bf16_f32 v159, v248, v249\n"
    "v_mfma_f32_16x16x32_bf16 v[120:123], a[252:255], v[132:135], v[120:123]\n"
    "ds_read_b64_tr_b16 a[246:247], v225 offset:28416\n"
    "ds_read_b128 v[192:195], v226 offset:2560\n"
    "ds_read_b128 v[200:203], v226 offset:2592\n"
    "v_mfma_f32_16x16x32_bf16 v[124:127], a[248:251], v[136:139], v[124:127]\n"
    "ds_read_b64_tr_b16 a[248:249], v225 offset:18184\n"
    "ds_read_b64_tr_b16 a[250:251], v225 offset:20232\n"
    "ds_read_b128 v[196:199], v226 offset:2576\n"
    "v_mfma_f32_16x16x32_bf16 v[124:127], a[252:255], v[140:143], v[124:127]\n"
    "ds_read_b64_tr_b16 a[252:253], v225 offset:26376\n"
    "ds_read_b128 v[204:207], v226 offset:2608\n"
    "v_mfma_f32_16x16x32_bf16 v[0:3], a[128:131], v[144:147], v[0:3]\n"
    "s_add_u32 s82, s90, 14336\n"
    "buffer_load_dwordx4 v[128:131], v244, s[56:59], s82 offen offset:0 sc1\n"
    "v_mfma_f32_16x16x32_bf16 v[0:3], a[132:135], v[148:151], v[0:3]\n"
    "ds_read_b64_tr_b16 a[254:255], v225 offset:28424\n"
    "s_waitcnt lgkmcnt(5)\n"
    "s_waitcnt vmcnt(24)\n"
    "v_fma_mix_f32 v246, v160, v192, v200 op_sel_hi:[1,0,0]\n"
    "v_mfma_f32_16x16x32_bf16 v[4:7], a[128:131], v[152:155], v[4:7]\n"
    "v_fma_mix_f32 v247, v160, v193, v201 op_sel:[1,0,0] op_sel_hi:[1,0,0]\n"
    "v_cvt_pk_bf16_f32 v160, v246, v247\n"
    "v_mfma_f32_16x16x32_bf16 v[4:7], a[132:135], v[156:159], v[4:7]\n"
    "buffer_load_dwordx4 v[132:135], v244, s[56:59], s82 offen offset:1024 sc1\n"
    "v_mfma_f32_16x16x32_bf16 v[8:11], a[136:139], v[144:147], v[8:11]\n"
    "v_fma_mix_f32 v248, v161, v194, v202 op_sel_hi:[1,0,0]\n"
    "v_fma_mix_f32 v249, v161, v195, v203 op_sel:[1,0,0] op_sel_hi:[1,0,0]\n"
    "v_cvt_pk_bf16_f32 v161, v248, v249\n"
    "v_mfma_f32_16x16x32_bf16 v[8:11], a[140:143], v[148:151], v[8:11]\n"
    "s_waitcnt vmcnt(15)\n"
    "v_fma_mix_f32 v246, v168, v192, v200 op_sel_hi:[1,0,0]\n"
    "v_fma_mix_f32 v247, v168, v193, v201 op_sel:[1,0,0] op_sel_hi:[1,0,0]\n"
    "v_cvt_pk_bf16_f32 v168, v246, v247\n"
    "v_mfma_f32_16x16x32_bf16 v[12:15], a[136:139], v[152:155], v[12:15]\n"
    "s_waitcnt lgkmcnt(0)\n"
    "s_barrier\n"
    "s_add_u32 m0, s79, 16384\n"
    "v_mfma_f32_16x16x32_bf16 v[12:15], a[140:143], v[156:159], v[12:15]\n"
    "s_mov_b32 s81, 3670016\n"
    "buffer_load_dwordx4 v228, s[48:51], s81 offen offset:0 lds\n"
    "v_mfma_f32_16x16x32_bf16 v[16:19], a[144:147], v[144:147], v[16:19]\n"
    "ds_read_b64_tr_b16 a[128:129], v225 offset:32768\n"
    "ds_read_b64_tr_b16 a[130:131], v225 offset:34816\n"
    "ds_read_b64_tr_b16 a[132:133], v225 offset:40960\n"
    "v_fma_mix_f32 v248, v169, v194, v202 op_sel_hi:[1,0,0]\n"
    "v_mfma_f32_16x16x32_bf16 v[16:19], a[148:151], v[148:151], v[16:19]\n"
    "ds_read_b64_tr_b16 a[134:135], v225 offset:43008\n"
    "v_fma_mix_f32 v249, v169, v195, v203 op_sel:[1,0,0] op_sel_hi:[1,0,0]\n"
    "v_mfma_f32_16x16x32_bf16 v[20:23], a[144:147], v[152:155], v[20:23]\n"
    "ds_read_b64_tr_b16 a[136:137], v225 offset:32776\n"
    "v_cvt_pk_bf16_f32 v169, v248, v249\n"
    "v_mfma_f32_16x16x32_bf16 v[20:23], a[148:151], v[156:159], v[20:23]\n"
    "buffer_load_dwordx4 v229, s[48:51], s81 offen offset:1024 lds\n"
    "v_mfma_f32_16x16x32_bf16 v[24:27], a[152:155], v[144:147], v[24:27]\n"
    "ds_read_b64_tr_b16 a[138:139], v225 offset:34824\n"
    "ds_read_b64_tr_b16 a[140:141], v225 offset:40968\n"
    "ds_read_b128 v[192:195], v226 offset:2816\n"
    "v_mfma_f32_16x16x32_bf16 v[24:27], a[156:159], v[148:151], v[24:27]\n"
    "ds_read_b64_tr_b16 a[142:143], v225 offset:43016\n"
    "ds_read_b128 v[200:203], v226 offset:2848\n"
    "v_mfma_f32_16x16x32_bf16 v[28:31], a[152:155], v[152:155], v[28:31]\n"
    "ds_read_b64_tr_b16 a[144:145], v225 offset:33024\n"
    "ds_read_b64_tr_b16 a[146:147], v225 offset:35072\n"
    "v_fma_mix_f32 v246, v162, v196, v204 op_sel_hi:[1,0,0]\n"
    "v_mfma_f32_16x16x32_bf16 v[28:31], a[156:159], v[156:159], v[28:31]\n"
    "buffer_load_dwordx4 v230, s[48:51], s81 offen offset:2048 lds\n"
    "v_mfma_f32_16x16x32_bf16 v[32:35], a[160:163], v[144:147], v[32:35]\n"
    "ds_read_b64_tr_b16 a[148:149], v225 offset:41216\n"
    "ds_read_b64_tr_b16 a[150:151], v225 offset:43264\n"
    "v_fma_mix_f32 v247, v162, v197, v205 op_sel:[1,0,0] op_sel_hi:[1,0,0]\n"
    "v_mfma_f32_16x16x32_bf16 v[32:35], a[164:167], v[148:151], v[32:35]\n"
    "ds_read_b64_tr_b16 a[152:153], v225 offset:33032\n"
    "v_cvt_pk_bf16_f32 v162, v246, v247\n"
    "v_mfma_f32_16x16x32_bf16 v[36:39], a[160:163], v[152:155], v[36:39]\n"
    "ds_read_b64_tr_b16 a[154:155], v225 offset:35080\n"
    "v_fma_mix_f32 v248, v163, v198, v206 op_sel_hi:[1,0,0]\n"
    "v_mfma_f32_16x16x32_bf16 v[36:39], a[164:167], v[156:159], v[36:39]\n"
    "buffer_load_dwordx4 v231, s[48:51], s81 offen offset:3072 lds\n"
    "v_mfma_f32_16x16x32_bf16 v[40:43], a[168:171], v[144:147], v[40:43]\n"
    "s_add_u32 m0, s79, 65544\n"
    "ds_read_b64_tr_b16 a[156:157], v225 offset:41224\n"
    "ds_read_b64_tr_b16 a[158:159], v225 offset:43272\n"
    "v_fma_mix_f32 v249, v163, v199, v207 op_sel:[1,0,0] op_sel_hi:[1,0,0]\n"
    "v_mfma_f32_16x16x32_bf16 v[40:43], a[172:175], v[148:151], v[40:43]\n"
    "s_mov_b32 s81, 3735552\n"
    "ds_read_b64_tr_b16 a[160:161], v225 offset:33280\n"
    "ds_read_b64_tr_b16 a[162:163], v225 offset:35328\n"
    "v_mfma_f32_16x16x32_bf16 v[44:47], a[168:171], v[152:155], v[44:47]\n"
    "ds_read_b64_tr_b16 a[164:165], v225 offset:41472\n"
    "v_cvt_pk_bf16_f32 v163, v248, v249\n"
    "v_mfma_f32_16x16x32_bf16 v[44:47], a[172:175], v[156:159], v[44:47]\n"
    "buffer_load_dwordx4 v228, s[48:51], s81 offen offset:0 lds\n"
    "v_mfma_f32_16x16x32_bf16 v[48:51], a[176:179], v[144:147], v[48:51]\n"
    "ds_read_b64_tr_b16 a[166:167], v225 offset:43520\n"
    "ds_read_b64_tr_b16 a[168:169], v225 offset:33288\n"
    "v_fma_mix_f32 v246, v170, v196, v204 op_sel_hi:[1,0,0]\n"
    "v_mfma_f32_16x16x32_bf16 v[48:51], a[180:183], v[148:151], v[48:51]\n"
    "ds_read_b64_tr_b16 a[170:171], v225 offset:35336\n"
    "v_fma_mix_f32 v247, v170, v197, v205 op_sel:[1,0,0] op_sel_hi:[1,0,0]\n"
    "v_mfma_f32_16x16x32_bf16 v[52:55], a[176:179], v[152:155], v[52:55]\n"
    "ds_read_b64_tr_b16 a[172:173], v225 offset:41480\n"
    "v_cvt_pk_bf16_f32 v170, v246, v247\n"
    "v_mfma_f32_16x16x32_bf16 v[52:55], a[180:183], v[156:159], v[52:55]\n"
    "buffer_load_dwordx4 v229, s[48:51], s81 offen offset:1024 lds\n"
    "v_mfma_f32_16x16x32_bf16 v[56:59], a[184:187], v[144:147], v[56:59]\n"
    "ds_read_b64_tr_b16 a[174:175], v225 offset:43528\n"
    "ds_read_b64_tr_b16 a[176:177], v225 offset:33536\n"
    "ds_read_b64_tr_b16 a[178:179], v225 offset:35584\n"
    "v_fma_mix_f32 v248, v171, v198, v206 op_sel_hi:[1,0,0]\n"
    "v_mfma_f32_16x16x32_bf16 v[56:59], a[188:191], v[148:151], v[56:59]\n"
    "ds_read_b64_tr_b16 a[180:181], v225 offset:41728\n"
    "v_fma_mix_f32 v249, v171, v199, v207 op_sel:[1,0,0] op_sel_hi:[1,0,0]\n"
    "v_mfma_f32_16x16x32_bf16 v[60:63], a[184:187], v[152:155], v[60:63]\n"
    "ds_read_b64_tr_b16 a[182:183], v225 offset:43776\n"
    "v_cvt_pk_bf16_f32 v171, v248, v249\n"
    "v_mfma_f32_16x16x32_bf16 v[60:63], a[188:191], v[156:159], v[60:63]\n"
    "buffer_load_dwordx4 v230, s[48:51], s81 offen offset:2048 lds\n"
    "v_mfma_f32_16x16x32_bf16 v[64:67], a[192:195], v[144:147], v[64:67]\n"
    "ds_read_b64_tr_b16 a[184:185], v225 offset:33544\n"
    "ds_read_b64_tr_b16 a[186:187], v225 offset:35592\n"
    "ds_read_b128 v[196:199], v226 offset:2832\n"
    "v_mfma_f32_16x16x32_bf16 v[64:67], a[196:199], v[148:151], v[64:67]\n"
    "ds_read_b64_tr_b16 a[188:189], v225 offset:41736\n"
    "ds_read_b128 v[204:207], v226 offset:2864\n"
    "v_mfma_f32_16x16x32_bf16 v[68:71], a[192:195], v[152:155], v[68:71]\n"
    "ds_read_b64_tr_b16 a[190:191], v225 offset:43784\n"
    "s_waitcnt lgkmcnt(6)\n"
    "v_fma_mix_f32 v246, v164, v192, v200 op_sel_hi:[1,0,0]\n"
    "v_mfma_f32_16x16x32_bf16 v[68:71], a[196:199], v[156:159], v[68:71]\n"
    "buffer_load_dwordx4 v231, s[48:51], s81 offen offset:3072 lds\n"
    "v_mfma_f32_16x16x32_bf16 v[72:75], a[200:203], v[144:147], v[72:75]\n"
    "ds_read_b64_tr_b16 a[192:193], v225 offset:33792\n"
    "ds_read_b64_tr_b16 a[194:195], v225 offset:35840\n"
    "ds_read_b64_tr_b16 a[196:197], v225 offset:41984\n"
    "v_fma_mix_f32 v247, v164, v193, v201 op_sel:[1,0,0] op_sel_hi:[1,0,0]\n"
    "v_mfma_f32_16x16x32_bf16 v[72:75], a[204:207], v[148:151], v[72:75]\n"
    "ds_read_b64_tr_b16 a[198:199], v225 offset:44032\n"
    "v_cvt_pk_bf16_f32 v164, v246, v247\n"
    "v_mfma_f32_16x16x32_bf16 v[76:79], a[200:203], v[152:155], v[76:79]\n"
    "ds_read_b64_tr_b16 a[200:201], v225 offset:33800\n"
    "v_fma_mix_f32 v248, v165, v194, v202 op_sel_hi:[1,0,0]\n"
    "v_mfma_f32_16x16x32_bf16 v[76:79], a[204:207], v[156:159], v[76:79]\n"
    "buffer_load_dwordx4 v[136:139], v245, s[56:59], s82 offen offset:0 sc1\n"
    "v_mfma_f32_16x16x32_bf16 v[80:83], a[208:211], v[144:147], v[80:83]\n"
    "ds_read_b64_tr_b16 a[202:203], v225 offset:35848\n"
    "ds_read_b64_tr_b16 a[204:205], v225 offset:41992\n"
    "v_fma_mix_f32 v249, v165, v195, v203 op_sel:[1,0,0] op_sel_hi:[1,0,0]\n"
    "v_mfma_f32_16x16x32_bf16 v[80:83], a[212:215], v[148:151], v[80:83]\n"
    "ds_read_b64_tr_b16 a[206:207], v225 offset:44040\n"
    "v_cvt_pk_bf16_f32 v165, v248, v249\n"
    "v_mfma_f32_16x16x32_bf16 v[84:87], a[208:211], v[152:155], v[84:87]\n"
    "ds_read_b64_tr_b16 a[208:209], v225 offset:34048\n"
    "ds_read_b64_tr_b16 a[210:211], v225 offset:36096\n"
    "s_waitcnt vmcnt(23)\n"
    "v_fma_mix_f32 v246, v172, v192, v200 op_sel_hi:[1,0,0]\n"
    "v_mfma_f32_16x16x32_bf16 v[84:87], a[212:215], v[156:159], v[84:87]\n"
    "buffer_load_dwordx4 v[140:143], v245, s[56:59], s82 offen offset:1024 sc1\n"
    "v_mfma_f32_16x16x32_bf16 v[88:91], a[216:219], v[144:147], v[88:91]\n"
    "ds_read_b64_tr_b16 a[212:213], v225 offset:42240\n"
    "v_fma_mix_f32 v247, v172, v193, v201 op_sel:[1,0,0] op_sel_hi:[1,0,0]\n"
    "v_mfma_f32_16x16x32_bf16 v[88:91], a[220:223], v[148:151], v[88:91]\n"
    "ds_read_b64_tr_b16 a[214:215], v225 offset:44288\n"
    "v_cvt_pk_bf16_f32 v172, v246, v247\n"
    "v_mfma_f32_16x16x32_bf16 v[92:95], a[216:219], v[152:155], v[92:95]\n"
    "ds_read_b64_tr_b16 a[216:217], v225 offset:34056\n"
    "v_fma_mix_f32 v248, v173, v194, v202 op_sel_hi:[1,0,0]\n"
    "v_mfma_f32_16x16x32_bf16 v[92:95], a[220:223], v[156:159], v[92:95]\n"
    "ds_read_b64_tr_b16 a[218:219], v225 offset:36104\n"
    "v_fma_mix_f32 v249, v173, v195, v203 op_sel:[1,0,0] op_sel_hi:[1,0,0]\n"
    "v_mfma_f32_16x16x32_bf16 v[96:99], a[224:227], v[144:147], v[96:99]\n"
    "ds_read_b64_tr_b16 a[220:221], v225 offset:42248\n"
    "v_cvt_pk_bf16_f32 v173, v248, v249\n"
    "v_mfma_f32_16x16x32_bf16 v[96:99], a[228:231], v[148:151], v[96:99]\n"
    "ds_read_b64_tr_b16 a[222:223], v225 offset:44296\n"
    "s_waitcnt lgkmcnt(4)\n"
    "v_fma_mix_f32 v246, v166, v196, v204 op_sel_hi:[1,0,0]\n"
    "v_mfma_f32_16x16x32_bf16 v[100:103], a[224:227], v[152:155], v[100:103]\n"
    "ds_read_b64_tr_b16 a[224:225], v225 offset:34304\n"
    "v_fma_mix_f32 v247, v166, v197, v205 op_sel:[1,0,0] op_sel_hi:[1,0,0]\n"
    "v_mfma_f32_16x16x32_bf16 v[100:103], a[228:231], v[156:159], v[100:103]\n"
    "ds_read_b64_tr_b16 a[226:227], v225 offset:36352\n"
    "v_cvt_pk_bf16_f32 v166, v246, v247\n"
    "v_mfma_f32_16x16x32_bf16 v[104:107], a[232:235], v[144:147], v[104:107]\n"
    "ds_read_b64_tr_b16 a[228:229], v225 offset:42496\n"
    "v_fma_mix_f32 v248, v167, v198, v206 op_sel_hi:[1,0,0]\n"
    "v_mfma_f32_16x16x32_bf16 v[104:107], a[236:239], v[148:151], v[104:107]\n"
    "ds_read_b64_tr_b16 a[230:231], v225 offset:44544\n"
    "v_fma_mix_f32 v249, v167, v199, v207 op_sel:[1,0,0] op_sel_hi:[1,0,0]\n"
    "v_mfma_f32_16x16x32_bf16 v[108:111], a[232:235], v[152:155], v[108:111]\n"
    "ds_read_b64_tr_b16 a[232:233], v225 offset:34312\n"
    "v_cvt_pk_bf16_f32 v167, v248, v249\n"
    "v_mfma_f32_16x16x32_bf16 v[108:111], a[236:239], v[156:159], v[108:111]\n"
    "ds_read_b64_tr_b16 a[234:235], v225 offset:36360\n"
    "v_fma_mix_f32 v246, v174, v196, v204 op_sel_hi:[1,0,0]\n"
    "v_mfma_f32_16x16x32_bf16 v[112:115], a[240:243], v[144:147], v[112:115]\n"
    "ds_read_b64_tr_b16 a[236:237], v225 offset:42504\n"
    "v_fma_mix_f32 v247, v174, v197, v205 op_sel:[1,0,0] op_sel_hi:[1,0,0]\n"
    "v_mfma_f32_16x16x32_bf16 v[112:115], a[244:247], v[148:151], v[112:115]\n"
    "ds_read_b64_tr_b16 a[238:239], v225 offset:44552\n"
    "v_cvt_pk_bf16_f32 v174, v246, v247\n"
    "v_mfma_f32_16x16x32_bf16 v[116:119], a[240:243], v[152:155], v[116:119]\n"
    "ds_read_b64_tr_b16 a[240:241], v225 offset:34560\n"
    "v_fma_mix_f32 v248, v175, v198, v206 op_sel_hi:[1,0,0]\n"
    "v_mfma_f32_16x16x32_bf16 v[116:119], a[244:247], v[156:159], v[116:119]\n"
    "ds_read_b64_tr_b16 a[242:243], v225 offset:36608\n"
    "v_fma_mix_f32 v249, v175, v199, v207 op_sel:[1,0,0] op_sel_hi:[1,0,0]\n"
    "v_mfma_f32_16x16x32_bf16 v[120:123], a[248:251], v[144:147], v[120:123]\n"
    "ds_read_b64_tr_b16 a[244:245], v225 offset:42752\n"
    "v_cvt_pk_bf16_f32 v175, v248, v249\n"
    "v_mfma_f32_16x16x32_bf16 v[120:123], a[252:255], v[148:151], v[120:123]\n"
    "ds_read_b64_tr_b16 a[246:247], v225 offset:44800\n"
    "ds_read_b128 v[192:195], v226 offset:3072\n"
    "ds_read_b128 v[200:203], v226 offset:3104\n"
    "v_mfma_f32_16x16x32_bf16 v[124:127], a[248:251], v[152:155], v[124:127]\n"
    "ds_read_b64_tr_b16 a[248:249], v225 offset:34568\n"
    "ds_read_b64_tr_b16 a[250:251], v225 offset:36616\n"
    "ds_read_b128 v[196:199], v226 offset:3088\n"
    "v_mfma_f32_16x16x32_bf16 v[124:127], a[252:255], v[156:159], v[124:127]\n"
    "ds_read_b64_tr_b16 a[252:253], v225 offset:42760\n"
    "ds_read_b128 v[204:207], v226 offset:3120\n"
    "v_mfma_f32_16x16x32_bf16 v[0:3], a[128:131], v[160:163], v[0:3]\n"
    "s_add_u32 s82, s90, 16384\n"
    "buffer_load_dwordx4 v[144:147], v244, s[56:59], s82 offen offset:0 sc1\n"
    "v_mfma_f32_16x16x32_bf16 v[0:3], a[132:135], v[164:167], v[0:3]\n"
    "ds_read_b64_tr_b16 a[254:255], v225 offset:44808\n"
    "s_waitcnt lgkmcnt(5)\n"
    "s_waitcnt vmcnt(24)\n"
    "v_fma_mix_f32 v246, v176, v192, v200 op_sel_hi:[1,0,0]\n"
    "v_mfma_f32_16x16x32_bf16 v[4:7], a[128:131], v[168:171], v[4:7]\n"
    "v_fma_mix_f32 v247, v176, v193, v201 op_sel:[1,0,0] op_sel_hi:[1,0,0]\n"
    "v_cvt_pk_bf16_f32 v176, v246, v247\n"
    "v_mfma_f32_16x16x32_bf16 v[4:7], a[132:135], v[172:175], v[4:7]\n"
    "buffer_load_dwordx4 v[148:151], v244, s[56:59], s82 offen offset:1024 sc1\n"
    "v_mfma_f32_16x16x32_bf16 v[8:11], a[136:139], v[160:163], v[8:11]\n"
    "v_fma_mix_f32 v248, v177, v194, v202 op_sel_hi:[1,0,0]\n"
    "v_fma_mix_f32 v249, v177, v195, v203 op_sel:[1,0,0] op_sel_hi:[1,0,0]\n"
    "v_cvt_pk_bf16_f32 v177, v248, v249\n"
    "v_mfma_f32_16x16x32_bf16 v[8:11], a[140:143], v[164:167], v[8:11]\n"
    "s_waitcnt vmcnt(15)\n"
    "v_fma_mix_f32 v246, v184, v192, v200 op_sel_hi:[1,0,0]\n"
    "v_fma_mix_f32 v247, v184, v193, v201 op_sel:[1,0,0] op_sel_hi:[1,0,0]\n"
    "v_cvt_pk_bf16_f32 v184, v246, v247\n"
    "v_mfma_f32_16x16x32_bf16 v[12:15], a[136:139], v[168:171], v[12:15]\n"
    "s_waitcnt lgkmcnt(0)\n"
    "s_barrier\n"
    "s_add_u32 m0, s79, 32768\n"
    "v_mfma_f32_16x16x32_bf16 v[12:15], a[140:143], v[172:175], v[12:15]\n"
    "s_mov_b32 s81, 4194304\n"
    "buffer_load_dwordx4 v228, s[48:51], s81 offen offset:0 lds\n"
    "v_mfma_f32_16x16x32_bf16 v[16:19], a[144:147], v[160:163], v[16:19]\n"
    "ds_read_b64_tr_b16 a[128:129], v225 offset:0\n"
    "ds_read_b64_tr_b16 a[130:131], v225 offset:2048\n"
    "ds_read_b64_tr_b16 a[132:133], v225 offset:8192\n"
    "v_fma_mix_f32 v248, v185, v194, v202 op_sel_hi:[1,0,0]\n"
    "v_mfma_f32_16x16x32_bf16 v[16:19], a[148:151], v[164:167], v[16:19]\n"
    "ds_read_b64_tr_b16 a[134:135], v225 offset:10240\n"
    "v_fma_mix_f32 v249, v185, v195, v203 op_sel:[1,0,0] op_sel_hi:[1,0,0]\n"
    "v_mfma_f32_16x16x32_bf16 v[20:23], a[144:147], v[168:171], v[20:23]\n"
    "ds_read_b64_tr_b16 a[136:137], v225 offset:8\n"
    "v_cvt_pk_bf16_f32 v185, v248, v249\n"
    "v_mfma_f32_16x16x32_bf16 v[20:23], a[148:151], v[172:175], v[20:23]\n"
    "buffer_load_dwordx4 v229, s[48:51], s81 offen offset:1024 lds\n"
    "v_mfma_f32_16x16x32_bf16 v[24:27], a[152:155], v[160:163], v[24:27]\n"
    "ds_read_b64_tr_b16 a[138:139], v225 offset:2056\n"
    "ds_read_b64_tr_b16 a[140:141], v225 offset:8200\n"
    "ds_read_b128 v[192:195], v226 offset:3328\n"
    "v_mfma_f32_16x16x32_bf16 v[24:27], a[156:159], v[164:167], v[24:27]\n"
    "ds_read_b64_tr_b16 a[142:143], v225 offset:10248\n"
    "ds_read_b128 v[200:203], v226 offset:3360\n"
    "v_mfma_f32_16x16x32_bf16 v[28:31], a[152:155], v[168:171], v[28:31]\n"
    "ds_read_b64_tr_b16 a[144:145], v225 offset:256\n"
    "ds_read_b64_tr_b16 a[146:147], v225 offset:2304\n"
    "v_fma_mix_f32 v246, v178, v196, v204 op_sel_hi:[1,0,0]\n"
    "v_mfma_f32_16x16x32_bf16 v[28:31], a[156:159], v[172:175], v[28:31]\n"
    "buffer_load_dwordx4 v230, s[48:51], s81 offen offset:2048 lds\n"
    "v_mfma_f32_16x16x32_bf16 v[32:35], a[160:163], v[160:163], v[32:35]\n"
    "ds_read_b64_tr_b16 a[148:149], v225 offset:8448\n"
    "ds_read_b64_tr_b16 a[150:151], v225 offset:10496\n"
    "v_fma_mix_f32 v247, v178, v197, v205 op_sel:[1,0,0] op_sel_hi:[1,0,0]\n"
    "v_mfma_f32_16x16x32_bf16 v[32:35], a[164:167], v[164:167], v[32:35]\n"
    "ds_read_b64_tr_b16 a[152:153], v225 offset:264\n"
    "v_cvt_pk_bf16_f32 v178, v246, v247\n"
    "v_mfma_f32_16x16x32_bf16 v[36:39], a[160:163], v[168:171], v[36:39]\n"
    "ds_read_b64_tr_b16 a[154:155], v225 offset:2312\n"
    "v_fma_mix_f32 v248, v179, v198, v206 op_sel_hi:[1,0,0]\n"
    "v_mfma_f32_16x16x32_bf16 v[36:39], a[164:167], v[172:175], v[36:39]\n"
    "buffer_load_dwordx4 v231, s[48:51], s81 offen offset:3072 lds\n"
    "v_mfma_f32_16x16x32_bf16 v[40:43], a[168:171], v[160:163], v[40:43]\n"
    "s_add_u32 m0, s79, 81928\n"
    "ds_read_b64_tr_b16 a[156:157], v225 offset:8456\n"
    "ds_read_b64_tr_b16 a[158:159], v225 offset:10504\n"
    "v_fma_mix_f32 v249, v179, v199, v207 op_sel:[1,0,0] op_sel_hi:[1,0,0]\n"
    "v_mfma_f32_16x16x32_bf16 v[40:43], a[172:175], v[164:167], v[40:43]\n"
    "s_mov_b32 s81, 4259840\n"
    "ds_read_b64_tr_b16 a[160:161], v225 offset:512\n"
    "ds_read_b64_tr_b16 a[162:163], v225 offset:2560\n"
    "v_mfma_f32_16x16x32_bf16 v[44:47], a[168:171], v[168:171], v[44:47]\n"
    "ds_read_b64_tr_b16 a[164:165], v225 offset:8704\n"
    "v_cvt_pk_bf16_f32 v179, v248, v249\n"
    "v_mfma_f32_16x16x32_bf16 v[44:47], a[172:175], v[172:175], v[44:47]\n"
    "buffer_load_dwordx4 v228, s[48:51], s81 offen offset:0 lds\n"
    "v_mfma_f32_16x16x32_bf16 v[48:51], a[176:179], v[160:163], v[48:51]\n"
    "ds_read_b64_tr_b16 a[166:167], v225 offset:10752\n"
    "ds_read_b64_tr_b16 a[168:169], v225 offset:520\n"
    "v_fma_mix_f32 v246, v186, v196, v204 op_sel_hi:[1,0,0]\n"
    "v_mfma_f32_16x16x32_bf16 v[48:51], a[180:183], v[164:167], v[48:51]\n"
    "ds_read_b64_tr_b16 a[170:171], v225 offset:2568\n"
    "v_fma_mix_f32 v247, v186, v197, v205 op_sel:[1,0,0] op_sel_hi:[1,0,0]\n"
    "v_mfma_f32_16x16x32_bf16 v[52:55], a[176:179], v[168:171], v[52:55]\n"
    "ds_read_b64_tr_b16 a[172:173], v225 offset:8712\n"
    "v_cvt_pk_bf16_f32 v186, v246, v247\n"
    "v_mfma_f32_16x16x32_bf16 v[52:55], a[180:183], v[172:175], v[52:55]\n"
    "buffer_load_dwordx4 v229, s[48:51], s81 offen offset:1024 lds\n"
    "v_mfma_f32_16x16x32_bf16 v[56:59], a[184:187], v[160:163], v[56:59]\n"
    "ds_read_b64_tr_b16 a[174:175], v225 offset:10760\n"
    "ds_read_b64_tr_b16 a[176:177], v225 offset:768\n"
    "ds_read_b64_tr_b16 a[178:179], v225 offset:2816\n"
    "v_fma_mix_f32 v248, v187, v198, v206 op_sel_hi:[1,0,0]\n"
    "v_mfma_f32_16x16x32_bf16 v[56:59], a[188:191], v[164:167], v[56:59]\n"
    "ds_read_b64_tr_b16 a[180:181], v225 offset:8960\n"
    "v_fma_mix_f32 v249, v187, v199, v207 op_sel:[1,0,0] op_sel_hi:[1,0,0]\n"
    "v_mfma_f32_16x16x32_bf16 v[60:63], a[184:187], v[168:171], v[60:63]\n"
    "ds_read_b64_tr_b16 a[182:183], v225 offset:11008\n"
    "v_cvt_pk_bf16_f32 v187, v248, v249\n"
    "v_mfma_f32_16x16x32_bf16 v[60:63], a[188:191], v[172:175], v[60:63]\n"
    "buffer_load_dwordx4 v230, s[48:51], s81 offen offset:2048 lds\n"
    "v_mfma_f32_16x16x32_bf16 v[64:67], a[192:195], v[160:163], v[64:67]\n"
    "ds_read_b64_tr_b16 a[184:185], v225 offset:776\n"
    "ds_read_b64_tr_b16 a[186:187], v225 offset:2824\n"
    "ds_read_b128 v[196:199], v226 offset:3344\n"
    "v_mfma_f32_16x16x32_bf16 v[64:67], a[196:199], v[164:167], v[64:67]\n"
    "ds_read_b64_tr_b16 a[188:189], v225 offset:8968\n"
    "ds_read_b128 v[204:207], v226 offset:3376\n"
    "v_mfma_f32_16x16x32_bf16 v[68:71], a[192:195], v[168:171], v[68:71]\n"
    "ds_read_b64_tr_b16 a[190:191], v225 offset:11016\n"
    "s_waitcnt lgkmcnt(6)\n"
    "v_fma_mix_f32 v246, v180, v192, v200 op_sel_hi:[1,0,0]\n"
    "v_mfma_f32_16x16x32_bf16 v[68:71], a[196:199], v[172:175], v[68:71]\n"
    "buffer_load_dwordx4 v231, s[48:51], s81 offen offset:3072 lds\n"
    "v_mfma_f32_16x16x32_bf16 v[72:75], a[200:203], v[160:163], v[72:75]\n"
    "ds_read_b64_tr_b16 a[192:193], v225 offset:1024\n"
    "ds_read_b64_tr_b16 a[194:195], v225 offset:3072\n"
    "ds_read_b64_tr_b16 a[196:197], v225 offset:9216\n"
    "v_fma_mix_f32 v247, v180, v193, v201 op_sel:[1,0,0] op_sel_hi:[1,0,0]\n"
    "v_mfma_f32_16x16x32_bf16 v[72:75], a[204:207], v[164:167], v[72:75]\n"
    "ds_read_b64_tr_b16 a[198:199], v225 offset:11264\n"
    "v_cvt_pk_bf16_f32 v180, v246, v247\n"
    "v_mfma_f32_16x16x32_bf16 v[76:79], a[200:203], v[168:171], v[76:79]\n"
    "ds_read_b64_tr_b16 a[200:201], v225 offset:1032\n"
    "v_fma_mix_f32 v248, v181, v194, v202 op_sel_hi:[1,0,0]\n"
    "v_mfma_f32_16x16x32_bf16 v[76:79], a[204:207], v[172:175], v[76:79]\n"
    "buffer_load_dwordx4 v[152:155], v245, s[56:59], s82 offen offset:0 sc1\n"
    "v_mfma_f32_16x16x32_bf16 v[80:83], a[208:211], v[160:163], v[80:83]\n"
    "ds_read_b64_tr_b16 a[202:203], v225 offset:3080\n"
    "ds_read_b64_tr_b16 a[204:205], v225 offset:9224\n"
    "v_fma_mix_f32 v249, v181, v195, v203 op_sel:[1,0,0] op_sel_hi:[1,0,0]\n"
    "v_mfma_f32_16x16x32_bf16 v[80:83], a[212:215], v[164:167], v[80:83]\n"
    "ds_read_b64_tr_b16 a[206:207], v225 offset:11272\n"
    "v_cvt_pk_bf16_f32 v181, v248, v249\n"
    "v_mfma_f32_16x16x32_bf16 v[84:87], a[208:211], v[168:171], v[84:87]\n"
    "ds_read_b64_tr_b16 a[208:209], v225 offset:1280\n"
    "s_waitcnt vmcnt(23)\n"
    "v_fma_mix_f32 v246, v188, v192, v200 op_sel_hi:[1,0,0]\n"
    "v_mfma_f32_16x16x32_bf16 v[84:87], a[212:215], v[172:175], v[84:87]\n"
    "buffer_load_dwordx4 v[156:159], v245, s[56:59], s82 offen offset:1024 sc1\n"
    "v_mfma_f32_16x16x32_bf16 v[88:91], a[216:219], v[160:163], v[88:91]\n"
    "ds_read_b64_tr_b16 a[210:211], v225 offset:3328\n"
    "v_fma_mix_f32 v247, v188, v193, v201 op_sel:[1,0,0] op_sel_hi:[1,0,0]\n"
    "v_mfma_f32_16x16x32_bf16 v[88:91], a[220:223], v[164:167], v[88:91]\n"
    "ds_read_b64_tr_b16 a[212:213], v225 offset:9472\n"
    "v_cvt_pk_bf16_f32 v188, v246, v247\n"
    "v_mfma_f32_16x16x32_bf16 v[92:95], a[216:219], v[168:171], v[92:95]\n"
    "ds_read_b64_tr_b16 a[214:215], v225 offset:11520\n"
    "v_fma_mix_f32 v248, v189, v194, v202 op_sel_hi:[1,0,0]\n"
    "v_mfma_f32_16x16x32_bf16 v[92:95], a[220:223], v[172:175], v[92:95]\n"
    "ds_read_b64_tr_b16 a[216:217], v225 offset:1288\n"
    "v_fma_mix_f32 v249, v189, v195, v203 op_sel:[1,0,0] op_sel_hi:[1,0,0]\n"
    "v_mfma_f32_16x16x32_bf16 v[96:99], a[224:227], v[160:163], v[96:99]\n"
    "ds_read_b64_tr_b16 a[218:219], v225 offset:3336\n"
    "v_cvt_pk_bf16_f32 v189, v248, v249\n"
    "v_mfma_f32_16x16x32_bf16 v[96:99], a[228:231], v[164:167], v[96:99]\n"
    "ds_read_b64_tr_b16 a[220:221], v225 offset:9480\n"
    "s_waitcnt lgkmcnt(4)\n"
    "v_fma_mix_f32 v246, v182, v196, v204 op_sel_hi:[1,0,0]\n"
    "v_mfma_f32_16x16x32_bf16 v[100:103], a[224:227], v[168:171], v[100:103]\n"
    "ds_read_b64_tr_b16 a[222:223], v225 offset:11528\n"
    "v_fma_mix_f32 v247, v182, v197, v205 op_sel:[1,0,0] op_sel_hi:[1,0,0]\n"
    "v_mfma_f32_16x16x32_bf16 v[100:103], a[228:231], v[172:175], v[100:103]\n"
    "ds_read_b64_tr_b16 a[224:225], v225 offset:1536\n"
    "v_cvt_pk_bf16_f32 v182, v246, v247\n"
    "v_mfma_f32_16x16x32_bf16 v[104:107], a[232:235], v[160:163], v[104:107]\n"
    "ds_read_b64_tr_b16 a[226:227], v225 offset:3584\n"
    "v_fma_mix_f32 v248, v183, v198, v206 op_sel_hi:[1,0,0]\n"
    "v_mfma_f32_16x16x32_bf16 v[104:107], a[236:239], v[164:167], v[104:107]\n"
    "ds_read_b64_tr_b16 a[228:229], v225 offset:9728\n"
    "v_fma_mix_f32 v249, v183, v199, v207 op_sel:[1,0,0] op_sel_hi:[1,0,0]\n"
    "v_mfma_f32_16x16x32_bf16 v[108:111], a[232:235], v[168:171], v[108:111]\n"
    "ds_read_b64_tr_b16 a[230:231], v225 offset:11776\n"
    "v_cvt_pk_bf16_f32 v183, v248, v249\n"
    "v_mfma_f32_16x16x32_bf16 v[108:111], a[236:239], v[172:175], v[108:111]\n"
    "ds_read_b64_tr_b16 a[232:233], v225 offset:1544\n"
    "v_fma_mix_f32 v246, v190, v196, v204 op_sel_hi:[1,0,0]\n"
    "v_mfma_f32_16x16x32_bf16 v[112:115], a[240:243], v[160:163], v[112:115]\n"
    "ds_read_b64_tr_b16 a[234:235], v225 offset:3592\n"
    "v_fma_mix_f32 v247, v190, v197, v205 op_sel:[1,0,0] op_sel_hi:[1,0,0]\n"
    "v_mfma_f32_16x16x32_bf16 v[112:115], a[244:247], v[164:167], v[112:115]\n"
    "ds_read_b64_tr_b16 a[236:237], v225 offset:9736\n"
    "v_cvt_pk_bf16_f32 v190, v246, v247\n"
    "v_mfma_f32_16x16x32_bf16 v[116:119], a[240:243], v[168:171], v[116:119]\n"
    "ds_read_b64_tr_b16 a[238:239], v225 offset:11784\n"
    "v_fma_mix_f32 v248, v191, v198, v206 op_sel_hi:[1,0,0]\n"
    "v_mfma_f32_16x16x32_bf16 v[116:119], a[244:247], v[172:175], v[116:119]\n"
    "ds_read_b64_tr_b16 a[240:241], v225 offset:1792\n"
    "v_fma_mix_f32 v249, v191, v199, v207 op_sel:[1,0,0] op_sel_hi:[1,0,0]\n"
    "v_mfma_f32_16x16x32_bf16 v[120:123], a[248:251], v[160:163], v[120:123]\n"
    "ds_read_b64_tr_b16 a[242:243], v225 offset:3840\n"
    "v_cvt_pk_bf16_f32 v191, v248, v249\n"
    "v_mfma_f32_16x16x32_bf16 v[120:123], a[252:255], v[164:167], v[120:123]\n"
    "ds_read_b64_tr_b16 a[244:245], v225 offset:9984\n"
    "ds_read_b64_tr_b16 a[246:247], v225 offset:12032\n"
    "v_mfma_f32_16x16x32_bf16 v[124:127], a[248:251], v[168:171], v[124:127]\n"
    "ds_read_b64_tr_b16 a[248:249], v225 offset:1800\n"
    "ds_read_b128 v[192:195], v226 offset:3584\n"
    "v_mfma_f32_16x16x32_bf16 v[124:127], a[252:255], v[172:175], v[124:127]\n"
    "ds_read_b64_tr_b16 a[250:251], v225 offset:3848\n"
    "ds_read_b128 v[200:203], v226 offset:3616\n"
    "v_mfma_f32_16x16x32_bf16 v[0:3], a[128:131], v[176:179], v[0:3]\n"
    "ds_read_b64_tr_b16 a[252:253], v225 offset:9992\n"
    "ds_read_b128 v[196:199], v226 offset:3600\n"
    "v_mfma_f32_16x16x32_bf16 v[0:3], a[132:135], v[180:183], v[0:3]\n"
    "ds_read_b64_tr_b16 a[254:255], v225 offset:12040\n"
    "ds_read_b128 v[204:207], v226 offset:3632\n"
    "v_mfma_f32_16x16x32_bf16 v[4:7], a[128:131], v[184:187], v[4:7]\n"
    "s_waitcnt lgkmcnt(4)\n"
    "s_waitcnt vmcnt(23)\n"
    "v_fma_mix_f32 v246, v128, v192, v200 op_sel_hi:[1,0,0]\n"
    "v_fma_mix_f32 v247, v128, v193, v201 op_sel:[1,0,0] op_sel_hi:[1,0,0]\n"
    "v_mfma_f32_16x16x32_bf16 v[4:7], a[132:135], v[188:191], v[4:7]\n"
    "v_cvt_pk_bf16_f32 v128, v246, v247\n"
    "v_fma_mix_f32 v248, v129, v194, v202 op_sel_hi:[1,0,0]\n"
    "v_mfma_f32_16x16x32_bf16 v[8:11], a[136:139], v[176:179], v[8:11]\n"
    "v_fma_mix_f32 v249, v129, v195, v203 op_sel:[1,0,0] op_sel_hi:[1,0,0]\n"
    "v_cvt_pk_bf16_f32 v129, v248, v249\n"
    "v_mfma_f32_16x16x32_bf16 v[8:11], a[140:143], v[180:183], v[8:11]\n"
    "s_waitcnt vmcnt(13)\n"
    "v_fma_mix_f32 v246, v136, v192, v200 op_sel_hi:[1,0,0]\n"
    "v_fma_mix_f32 v247, v136, v193, v201 op_sel:[1,0,0] op_sel_hi:[1,0,0]\n"
    "v_mfma_f32_16x16x32_bf16 v[12:15], a[136:139], v[184:187], v[12:15]\n"
    "s_waitcnt lgkmcnt(0)\n"
    "s_barrier\n"
    "v_mfma_f32_16x16x32_bf16 v[12:15], a[140:143], v[188:191], v[12:15]\n"
    "s_mov_b32 s72, 0\n"
    "s_mov_b32 m0, s74\n"
    "s_nop 0\n"
    "ds_read_b64_tr_b16 a[128:129], v225 offset:16384\n"
    "ds_read_b64_tr_b16 a[130:131], v225 offset:18432\n"
    "v_cvt_pk_bf16_f32 v136, v246, v247\n"
    "v_mfma_f32_16x16x32_bf16 v[16:19], a[144:147], v[176:179], v[16:19]\n"
    "buffer_load_dwordx4 v214, s[40:43], s72 offen offset:0  lds\n"
    "v_mfma_f32_16x16x32_bf16 v[16:19], a[148:151], v[180:183], v[16:19]\n"
    "ds_read_b64_tr_b16 a[132:133], v225 offset:24576\n"
    "ds_read_b64_tr_b16 a[134:135], v225 offset:26624\n"
    "v_fma_mix_f32 v248, v137, v194, v202 op_sel_hi:[1,0,0]\n"
    "v_mfma_f32_16x16x32_bf16 v[20:23], a[144:147], v[184:187], v[20:23]\n"
    "ds_read_b64_tr_b16 a[136:137], v225 offset:16392\n"
    "v_fma_mix_f32 v249, v137, v195, v203 op_sel:[1,0,0] op_sel_hi:[1,0,0]\n"
    "v_mfma_f32_16x16x32_bf16 v[20:23], a[148:151], v[188:191], v[20:23]\n"
    "ds_read_b64_tr_b16 a[138:139], v225 offset:18440\n"
    "v_cvt_pk_bf16_f32 v137, v248, v249\n"
    "v_mfma_f32_16x16x32_bf16 v[24:27], a[152:155], v[176:179], v[24:27]\n"
    "ds_read_b64_tr_b16 a[140:141], v225 offset:24584\n"
    "ds_read_b128 v[192:195], v226 offset:3840\n"
    "v_mfma_f32_16x16x32_bf16 v[24:27], a[156:159], v[180:183], v[24:27]\n"
    "ds_read_b64_tr_b16 a[142:143], v225 offset:26632\n"
    "ds_read_b128 v[200:203], v226 offset:3872\n"
    "v_mfma_f32_16x16x32_bf16 v[28:31], a[152:155], v[184:187], v[28:31]\n"
    "buffer_load_dwordx4 v215, s[40:43], s72 offen offset:1024  lds\n"
    "v_mfma_f32_16x16x32_bf16 v[28:31], a[156:159], v[188:191], v[28:31]\n"
    "ds_read_b64_tr_b16 a[144:145], v225 offset:16640\n"
    "ds_read_b64_tr_b16 a[146:147], v225 offset:18688\n"
    "ds_read_b64_tr_b16 a[148:149], v225 offset:24832\n"
    "v_fma_mix_f32 v246, v130, v196, v204 op_sel_hi:[1,0,0]\n"
    "v_mfma_f32_16x16x32_bf16 v[32:35], a[160:163], v[176:179], v[32:35]\n"
    "ds_read_b64_tr_b16 a[150:151], v225 offset:26880\n"
    "v_fma_mix_f32 v247, v130, v197, v205 op_sel:[1,0,0] op_sel_hi:[1,0,0]\n"
    "v_mfma_f32_16x16x32_bf16 v[32:35], a[164:167], v[180:183], v[32:35]\n"
    "ds_read_b64_tr_b16 a[152:153], v225 offset:16648\n"
    "v_cvt_pk_bf16_f32 v130, v246, v247\n"
    "v_mfma_f32_16x16x32_bf16 v[36:39], a[160:163], v[184:187], v[36:39]\n"
    "ds_read_b64_tr_b16 a[154:155], v225 offset:18696\n"
    "v_fma_mix_f32 v248, v131, v198, v206 op_sel_hi:[1,0,0]\n"
    "v_mfma_f32_16x16x32_bf16 v[36:39], a[164:167], v[188:191], v[36:39]\n"
    "ds_read_b64_tr_b16 a[156:157], v225 offset:24840\n"
    "v_fma_mix_f32 v249, v131, v199, v207 op_sel:[1,0,0] op_sel_hi:[1,0,0]\n"
    "v_mfma_f32_16x16x32_bf16 v[40:43], a[168:171], v[176:179], v[40:43]\n"
    "ds_read_b64_tr_b16 a[158:159], v225 offset:26888\n"
    "v_cvt_pk_bf16_f32 v131, v248, v249\n"
    "v_mfma_f32_16x16x32_bf16 v[40:43], a[172:175], v[180:183], v[40:43]\n"
    "buffer_load_dwordx4 v216, s[40:43], s72 offen offset:2048  lds\n"
    "v_mfma_f32_16x16x32_bf16 v[44:47], a[168:171], v[184:187], v[44:47]\n"
    "ds_read_b64_tr_b16 a[160:161], v225 offset:16896\n"
    "ds_read_b64_tr_b16 a[162:163], v225 offset:18944\n"
    "ds_read_b64_tr_b16 a[164:165], v225 offset:25088\n"
    "v_fma_mix_f32 v246, v138, v196, v204 op_sel_hi:[1,0,0]\n"
    "v_mfma_f32_16x16x32_bf16 v[44:47], a[172:175], v[188:191], v[44:47]\n"
    "ds_read_b64_tr_b16 a[166:167], v225 offset:27136\n"
    "v_fma_mix_f32 v247, v138, v197, v205 op_sel:[1,0,0] op_sel_hi:[1,0,0]\n"
    "v_mfma_f32_16x16x32_bf16 v[48:51], a[176:179], v[176:179], v[48:51]\n"
    "ds_read_b64_tr_b16 a[168:169], v225 offset:16904\n"
    "v_cvt_pk_bf16_f32 v138, v246, v247\n"
    "v_mfma_f32_16x16x32_bf16 v[48:51], a[180:183], v[180:183], v[48:51]\n"
    "ds_read_b64_tr_b16 a[170:171], v225 offset:18952\n"
    "v_fma_mix_f32 v248, v139, v198, v206 op_sel_hi:[1,0,0]\n"
    "v_mfma_f32_16x16x32_bf16 v[52:55], a[176:179], v[184:187], v[52:55]\n"
    "ds_read_b64_tr_b16 a[172:173], v225 offset:25096\n"
    "v_fma_mix_f32 v249, v139, v199, v207 op_sel:[1,0,0] op_sel_hi:[1,0,0]\n"
    "v_mfma_f32_16x16x32_bf16 v[52:55], a[180:183], v[188:191], v[52:55]\n"
    "buffer_load_dwordx4 v217, s[40:43], s72 offen offset:3072  lds\n"
    "v_mfma_f32_16x16x32_bf16 v[56:59], a[184:187], v[176:179], v[56:59]\n"
    "ds_read_b64_tr_b16 a[174:175], v225 offset:27144\n"
    "ds_read_b64_tr_b16 a[176:177], v225 offset:17152\n"
    "ds_read_b64_tr_b16 a[178:179], v225 offset:19200\n"
    "v_cvt_pk_bf16_f32 v139, v248, v249\n"
    "v_mfma_f32_16x16x32_bf16 v[56:59], a[188:191], v[180:183], v[56:59]\n"
    "ds_read_b64_tr_b16 a[180:181], v225 offset:25344\n"
    "ds_read_b128 v[196:199], v226 offset:3856\n"
    "v_mfma_f32_16x16x32_bf16 v[60:63], a[184:187], v[184:187], v[60:63]\n"
    "s_mov_b32 m0, s76\n"
    "ds_read_b64_tr_b16 a[182:183], v225 offset:27392\n"
    "v_mfma_f32_16x16x32_bf16 v[60:63], a[188:191], v[188:191], v[60:63]\n"
    "ds_read_b64_tr_b16 a[184:185], v225 offset:17160\n"
    "ds_read_b128 v[204:207], v226 offset:3888\n"
    "v_mfma_f32_16x16x32_bf16 v[64:67], a[192:195], v[176:179], v[64:67]\n"
    "s_nop 0\n"
    "ds_read_b64_tr_b16 a[186:187], v225 offset:19208\n"
    "v_mfma_f32_16x16x32_bf16 v[64:67], a[196:199], v[180:183], v[64:67]\n"
    "ds_read_b64_tr_b16 a[188:189], v225 offset:25352\n"
    "s_waitcnt lgkmcnt(5)\n"
    "v_fma_mix_f32 v246, v132, v192, v200 op_sel_hi:[1,0,0]\n"
    "v_mfma_f32_16x16x32_bf16 v[68:71], a[192:195], v[184:187], v[68:71]\n"
    "buffer_load_dwordx4 v218, s[44:47], s72 offen offset:0  lds\n"
    "v_mfma_f32_16x16x32_bf16 v[68:71], a[196:199], v[188:191], v[68:71]\n"
    "ds_read_b64_tr_b16 a[190:191], v225 offset:27400\n"
    "ds_read_b64_tr_b16 a[192:193], v225 offset:17408\n"
    "ds_read_b64_tr_b16 a[194:195], v225 offset:19456\n"
    "v_fma_mix_f32 v247, v132, v193, v201 op_sel:[1,0,0] op_sel_hi:[1,0,0]\n"
    "v_mfma_f32_16x16x32_bf16 v[72:75], a[200:203], v[176:179], v[72:75]\n"
    "ds_read_b64_tr_b16 a[196:197], v225 offset:25600\n"
    "v_cvt_pk_bf16_f32 v132, v246, v247\n"
    "v_mfma_f32_16x16x32_bf16 v[72:75], a[204:207], v[180:183], v[72:75]\n"
    "ds_read_b64_tr_b16 a[198:199], v225 offset:27648\n"
    "v_fma_mix_f32 v248, v133, v194, v202 op_sel_hi:[1,0,0]\n"
    "v_mfma_f32_16x16x32_bf16 v[76:79], a[200:203], v[184:187], v[76:79]\n"
    "ds_read_b64_tr_b16 a[200:201], v225 offset:17416\n"
    "v_fma_mix_f32 v249, v133, v195, v203 op_sel:[1,0,0] op_sel_hi:[1,0,0]\n"
    "v_mfma_f32_16x16x32_bf16 v[76:79], a[204:207], v[188:191], v[76:79]\n"
    "ds_read_b64_tr_b16 a[202:203], v225 offset:19464\n"
    "v_cvt_pk_bf16_f32 v133, v248, v249\n"
    "v_mfma_f32_16x16x32_bf16 v[80:83], a[208:211], v[176:179], v[80:83]\n"
    "buffer_load_dwordx4 v219, s[44:47], s72 offen offset:1024  lds\n"
    "v_mfma_f32_16x16x32_bf16 v[80:83], a[212:215], v[180:183], v[80:83]\n"
    "ds_read_b64_tr_b16 a[204:205], v225 offset:25608\n"
    "ds_read_b64_tr_b16 a[206:207], v225 offset:27656\n"
    "s_waitcnt vmcnt(18)\n"
    "v_fma_mix_f32 v246, v140, v192, v200 op_sel_hi:[1,0,0]\n"
    "v_mfma_f32_16x16x32_bf16 v[84:87], a[208:211], v[184:187], v[84:87]\n"
    "ds_read_b64_tr_b16 a[208:209], v225 offset:17664\n"
    "v_fma_mix_f32 v247, v140, v193, v201 op_sel:[1,0,0] op_sel_hi:[1,0,0]\n"
    "v_mfma_f32_16x16x32_bf16 v[84:87], a[212:215], v[188:191], v[84:87]\n"
    "ds_read_b64_tr_b16 a[210:211], v225 offset:19712\n"
    "v_cvt_pk_bf16_f32 v140, v246, v247\n"
    "v_mfma_f32_16x16x32_bf16 v[88:91], a[216:219], v[176:179], v[88:91]\n"
    "ds_read_b64_tr_b16 a[212:213], v225 offset:25856\n"
    "v_fma_mix_f32 v248, v141, v194, v202 op_sel_hi:[1,0,0]\n"
    "v_mfma_f32_16x16x32_bf16 v[88:91], a[220:223], v[180:183], v[88:91]\n"
    "ds_read_b64_tr_b16 a[214:215], v225 offset:27904\n"
    "v_fma_mix_f32 v249, v141, v195, v203 op_sel:[1,0,0] op_sel_hi:[1,0,0]\n"
    "v_mfma_f32_16x16x32_bf16 v[92:95], a[216:219], v[184:187], v[92:95]\n"
    "ds_read_b64_tr_b16 a[216:217], v225 offset:17672\n"
    "ds_read_b64_tr_b16 a[218:219], v225 offset:19720\n"
    "v_cvt_pk_bf16_f32 v141, v248, v249\n"
    "v_mfma_f32_16x16x32_bf16 v[92:95], a[220:223], v[188:191], v[92:95]\n"
    "buffer_load_dwordx4 v218, s[44:47], s72 offen offset:2048  lds\n"
    "v_mfma_f32_16x16x32_bf16 v[96:99], a[224:227], v[176:179], v[96:99]\n"
    "ds_read_b64_tr_b16 a[220:221], v225 offset:25864\n"
    "s_waitcnt lgkmcnt(4)\n"
    "v_fma_mix_f32 v246, v134, v196, v204 op_sel_hi:[1,0,0]\n"
    "v_mfma_f32_16x16x32_bf16 v[96:99], a[228:231], v[180:183], v[96:99]\n"
    "ds_read_b64_tr_b16 a[222:223], v225 offset:27912\n"
    "v_fma_mix_f32 v247, v134, v197, v205 op_sel:[1,0,0] op_sel_hi:[1,0,0]\n"
    "v_mfma_f32_16x16x32_bf16 v[100:103], a[224:227], v[184:187], v[100:103]\n"
    "ds_read_b64_tr_b16 a[224:225], v225 offset:17920\n"
    "ds_read_b64_tr_b16 a[226:227], v225 offset:19968\n"
    "v_cvt_pk_bf16_f32 v134, v246, v247\n"
    "v_mfma_f32_16x16x32_bf16 v[100:103], a[228:231], v[188:191], v[100:103]\n"
    "ds_read_b64_tr_b16 a[228:229], v225 offset:26112\n"
    "v_fma_mix_f32 v248, v135, v198, v206 op_sel_hi:[1,0,0]\n"
    "v_mfma_f32_16x16x32_bf16 v[104:107], a[232:235], v[176:179], v[104:107]\n"
    "ds_read_b64_tr_b16 a[230:231], v225 offset:28160\n"
    "v_fma_mix_f32 v249, v135, v199, v207 op_sel:[1,0,0] op_sel_hi:[1,0,0]\n"
    "v_mfma_f32_16x16x32_bf16 v[104:107], a[236:239], v[180:183], v[104:107]\n"
    "buffer_load_dwordx4 v219, s[44:47], s72 offen offset:3072  lds\n"
    "v_mfma_f32_16x16x32_bf16 v[108:111], a[232:235], v[184:187], v[108:111]\n"
    "ds_read_b64_tr_b16 a[232:233], v225 offset:17928\n"
    "v_cvt_pk_bf16_f32 v135, v248, v249\n"
    "v_mfma_f32_16x16x32_bf16 v[108:111], a[236:239], v[188:191], v[108:111]\n"
    "ds_read_b64_tr_b16 a[234:235], v225 offset:19976\n"
    "v_fma_mix_f32 v246, v142, v196, v204 op_sel_hi:[1,0,0]\n"
    "v_mfma_f32_16x16x32_bf16 v[112:115], a[240:243], v[176:179], v[112:115]\n"
    "ds_read_b64_tr_b16 a[236:237], v225 offset:26120\n"
    "v_fma_mix_f32 v247, v142, v197, v205 op_sel:[1,0,0] op_sel_hi:[1,0,0]\n"
    "v_mfma_f32_16x16x32_bf16 v[112:115], a[244:247], v[180:183], v[112:115]\n"
    "ds_read_b64_tr_b16 a[238:239], v225 offset:28168\n"
    "v_cvt_pk_bf16_f32 v142, v246, v247\n"
    "v_mfma_f32_16x16x32_bf16 v[116:119], a[240:243], v[184:187], v[116:119]\n"
    "ds_read_b64_tr_b16 a[240:241], v225 offset:18176\n"
    "v_fma_mix_f32 v248, v143, v198, v206 op_sel_hi:[1,0,0]\n"
    "v_mfma_f32_16x16x32_bf16 v[116:119], a[244:247], v[188:191], v[116:119]\n"
    "ds_read_b64_tr_b16 a[242:243], v225 offset:20224\n"
    "v_fma_mix_f32 v249, v143, v199, v207 op_sel:[1,0,0] op_sel_hi:[1,0,0]\n"
    "v_mfma_f32_16x16x32_bf16 v[120:123], a[248:251], v[176:179], v[120:123]\n"
    "ds_read_b64_tr_b16 a[244:245], v225 offset:26368\n"
    "v_cvt_pk_bf16_f32 v143, v248, v249\n"
    "v_mfma_f32_16x16x32_bf16 v[120:123], a[252:255], v[180:183], v[120:123]\n"
    "ds_read_b64_tr_b16 a[246:247], v225 offset:28416\n"
    "ds_read_b128 v[192:195], v226 offset:4096\n"
    "v_mfma_f32_16x16x32_bf16 v[124:127], a[248:251], v[184:187], v[124:127]\n"
    "ds_read_b64_tr_b16 a[248:249], v225 offset:18184\n"
    "ds_read_b128 v[200:203], v226 offset:4128\n"
    "v_mfma_f32_16x16x32_bf16 v[124:127], a[252:255], v[188:191], v[124:127]\n"
    "ds_read_b64_tr_b16 a[250:251], v225 offset:20232\n"
    "ds_read_b128 v[196:199], v226 offset:4112\n"
    "v_mfma_f32_16x16x32_bf16 v[0:3], a[128:131], v[128:131], v[0:3]\n"
    "ds_read_b64_tr_b16 a[252:253], v225 offset:26376\n"
    "ds_read_b128 v[204:207], v226 offset:4144\n"
    "v_mfma_f32_16x16x32_bf16 v[0:3], a[132:135], v[132:135], v[0:3]\n"
    "ds_read_b64_tr_b16 a[254:255], v225 offset:28424\n"
    "s_waitcnt lgkmcnt(5)\n"
    "s_waitcnt vmcnt(19)\n"
    "v_fma_mix_f32 v246, v144, v192, v200 op_sel_hi:[1,0,0]\n"
    "v_mfma_f32_16x16x32_bf16 v[4:7], a[128:131], v[136:139], v[4:7]\n"
    "v_fma_mix_f32 v247, v144, v193, v201 op_sel:[1,0,0] op_sel_hi:[1,0,0]\n"
    "v_cvt_pk_bf16_f32 v144, v246, v247\n"
    "v_mfma_f32_16x16x32_bf16 v[4:7], a[132:135], v[140:143], v[4:7]\n"
    "v_fma_mix_f32 v248, v145, v194, v202 op_sel_hi:[1,0,0]\n"
    "v_fma_mix_f32 v249, v145, v195, v203 op_sel:[1,0,0] op_sel_hi:[1,0,0]\n"
    "v_mfma_f32_16x16x32_bf16 v[8:11], a[136:139], v[128:131], v[8:11]\n"
    "v_cvt_pk_bf16_f32 v145, v248, v249\n"
    "s_waitcnt vmcnt(9)\n"
    "v_fma_mix_f32 v246, v152, v192, v200 op_sel_hi:[1,0,0]\n"
    "v_fma_mix_f32 v247, v152, v193, v201 op_sel:[1,0,0] op_sel_hi:[1,0,0]\n"
    "v_mfma_f32_16x16x32_bf16 v[8:11], a[140:143], v[132:135], v[8:11]\n"
    "v_cvt_pk_bf16_f32 v152, v246, v247\n"
    "v_fma_mix_f32 v248, v153, v194, v202 op_sel_hi:[1,0,0]\n"
    "v_fma_mix_f32 v249, v153, v195, v203 op_sel:[1,0,0] op_sel_hi:[1,0,0]\n"
    "v_mfma_f32_16x16x32_bf16 v[12:15], a[136:139], v[136:139], v[12:15]\n"
    "s_waitcnt lgkmcnt(0)\n"
    "s_barrier\n"
    "v_mfma_f32_16x16x32_bf16 v[12:15], a[140:143], v[140:143], v[12:15]\n"
    "s_mov_b32 s72, 0x4000\n"
    "s_mov_b32 m0, s75\n"
    "s_nop 0\n"
    "ds_read_b64_tr_b16 a[128:129], v225 offset:32768\n"
    "ds_read_b64_tr_b16 a[130:131], v225 offset:34816\n"
    "v_cvt_pk_bf16_f32 v153, v248, v249\n"
    "v_mfma_f32_16x16x32_bf16 v[16:19], a[144:147], v[128:131], v[16:19]\n"
    "buffer_load_dwordx4 v214, s[40:43], s72 offen offset:0  lds\n"
    "v_mfma_f32_16x16x32_bf16 v[16:19], a[148:151], v[132:135], v[16:19]\n"
    "ds_read_b64_tr_b16 a[132:133], v225 offset:40960\n"
    "ds_read_b64_tr_b16 a[134:135], v225 offset:43008\n"
    "ds_read_b128 v[192:195], v226 offset:4352\n"
    "ds_read_b128 v[200:203], v226 offset:4384\n"
    "v_mfma_f32_16x16x32_bf16 v[20:23], a[144:147], v[136:139], v[20:23]\n"
    "ds_read_b64_tr_b16 a[136:137], v225 offset:32776\n"
    "v_fma_mix_f32 v246, v146, v196, v204 op_sel_hi:[1,0,0]\n"
    "v_fma_mix_f32 v247, v146, v197, v205 op_sel:[1,0,0] op_sel_hi:[1,0,0]\n"
    "v_mfma_f32_16x16x32_bf16 v[20:23], a[148:151], v[140:143], v[20:23]\n"
    "ds_read_b64_tr_b16 a[138:139], v225 offset:34824\n"
    "v_cvt_pk_bf16_f32 v146, v246, v247\n"
    "v_fma_mix_f32 v248, v147, v198, v206 op_sel_hi:[1,0,0]\n"
    "v_mfma_f32_16x16x32_bf16 v[24:27], a[152:155], v[128:131], v[24:27]\n"
    "buffer_load_dwordx4 v215, s[40:43], s72 offen offset:1024  lds\n"
    "v_mfma_f32_16x16x32_bf16 v[24:27], a[156:159], v[132:135], v[24:27]\n"
    "ds_read_b64_tr_b16 a[140:141], v225 offset:40968\n"
    "ds_read_b64_tr_b16 a[142:143], v225 offset:43016\n"
    "v_fma_mix_f32 v249, v147, v199, v207 op_sel:[1,0,0] op_sel_hi:[1,0,0]\n"
    "v_cvt_pk_bf16_f32 v147, v248, v249\n"
    "v_mfma_f32_16x16x32_bf16 v[28:31], a[152:155], v[136:139], v[28:31]\n"
    "ds_read_b64_tr_b16 a[144:145], v225 offset:33024\n"
    "ds_read_b64_tr_b16 a[146:147], v225 offset:35072\n"
    "v_fma_mix_f32 v246, v154, v196, v204 op_sel_hi:[1,0,0]\n"
    "v_mfma_f32_16x16x32_bf16 v[28:31], a[156:159], v[140:143], v[28:31]\n"
    "ds_read_b64_tr_b16 a[148:149], v225 offset:41216\n"
    "v_fma_mix_f32 v247, v154, v197, v205 op_sel:[1,0,0] op_sel_hi:[1,0,0]\n"
    "v_cvt_pk_bf16_f32 v154, v246, v247\n"
    "v_mfma_f32_16x16x32_bf16 v[32:35], a[160:163], v[128:131], v[32:35]\n"
    "buffer_load_dwordx4 v216, s[40:43], s72 offen offset:2048  lds\n"
    "v_mfma_f32_16x16x32_bf16 v[32:35], a[164:167], v[132:135], v[32:35]\n"
    "ds_read_b64_tr_b16 a[150:151], v225 offset:43264\n"
    "ds_read_b64_tr_b16 a[152:153], v225 offset:33032\n"
    "v_fma_mix_f32 v248, v155, v198, v206 op_sel_hi:[1,0,0]\n"
    "v_mfma_f32_16x16x32_bf16 v[36:39], a[160:163], v[136:139], v[36:39]\n"
    "ds_read_b64_tr_b16 a[154:155], v225 offset:35080\n"
    "v_fma_mix_f32 v249, v155, v199, v207 op_sel:[1,0,0] op_sel_hi:[1,0,0]\n"
    "v_mfma_f32_16x16x32_bf16 v[36:39], a[164:167], v[140:143], v[36:39]\n"
    "ds_read_b64_tr_b16 a[156:157], v225 offset:41224\n"
    "v_cvt_pk_bf16_f32 v155, v248, v249\n"
    "v_mfma_f32_16x16x32_bf16 v[40:43], a[168:171], v[128:131], v[40:43]\n"
    "buffer_load_dwordx4 v217, s[40:43], s72 offen offset:3072  lds\n"
    "v_mfma_f32_16x16x32_bf16 v[40:43], a[172:175], v[132:135], v[40:43]\n"
    "ds_read_b64_tr_b16 a[158:159], v225 offset:43272\n"
    "ds_read_b64_tr_b16 a[160:161], v225 offset:33280\n"
    "ds_read_b64_tr_b16 a[162:163], v225 offset:35328\n"
    "ds_read_b128 v[196:199], v226 offset:4368\n"
    "v_mfma_f32_16x16x32_bf16 v[44:47], a[168:171], v[136:139], v[44:47]\n"
    "ds_read_b64_tr_b16 a[164:165], v225 offset:41472\n"
    "ds_read_b128 v[204:207], v226 offset:4400\n"
    "v_mfma_f32_16x16x32_bf16 v[44:47], a[172:175], v[140:143], v[44:47]\n"
    "ds_read_b64_tr_b16 a[166:167], v225 offset:43520\n"
    "s_waitcnt lgkmcnt(7)\n"
    "v_fma_mix_f32 v246, v148, v192, v200 op_sel_hi:[1,0,0]\n"
    "v_mfma_f32_16x16x32_bf16 v[48:51], a[176:179], v[128:131], v[48:51]\n"
    "v_accvgpr_write_b32 a0, -1\n"
    "v_accvgpr_write_b32 a1, -1\n"
    "v_accvgpr_write_b32 a2, -1\n"
    "v_accvgpr_write_b32 a3, -1\n"
    "ds_read_b64_tr_b16 a[168:169], v225 offset:33288\n"
    "v_fma_mix_f32 v247, v148, v193, v201 op_sel:[1,0,0] op_sel_hi:[1,0,0]\n"
    "v_mfma_f32_16x16x32_bf16 v[48:51], a[180:183], v[132:135], v[48:51]\n"
    "s_lshl_b32 s93, s89, 4\n"
    "s_add_u32 s93, s93, s88\n"
    "ds_read_b64_tr_b16 a[170:171], v225 offset:35336\n"
    "v_mfma_f32_16x16x32_bf16 v[52:55], a[176:179], v[136:139], v[52:55]\n"
    "s_mul_i32 s81, s93, 5120\n"
    "ds_read_b64_tr_b16 a[172:173], v225 offset:41480\n"
    "v_cvt_pk_bf16_f32 v148, v246, v247\n"
    "v_mfma_f32_16x16x32_bf16 v[52:55], a[180:183], v[140:143], v[52:55]\n"
    "s_mul_i32 s80, s78, 1280\n"
    "s_add_u32 s81, s81, s80\n"
    "ds_read_b64_tr_b16 a[174:175], v225 offset:43528\n"
    "v_mfma_f32_16x16x32_bf16 v[56:59], a[184:187], v[128:131], v[56:59]\n"
    "s_add_u32 s81, s81, 4194304\n"
    "ds_read_b64_tr_b16 a[176:177], v225 offset:33536\n"
    "ds_read_b64_tr_b16 a[178:179], v225 offset:35584\n"
    "v_fma_mix_f32 v248, v149, v194, v202 op_sel_hi:[1,0,0]\n"
    "v_mfma_f32_16x16x32_bf16 v[56:59], a[188:191], v[132:135], v[56:59]\n"
    "buffer_store_dwordx4 a[0:3], v244, s[56:59], s81 offen sc1\n"
    "v_mfma_f32_16x16x32_bf16 v[60:63], a[184:187], v[136:139], v[60:63]\n"
    "ds_read_b64_tr_b16 a[180:181], v225 offset:41728\n"
    "ds_read_b64_tr_b16 a[182:183], v225 offset:43776\n"
    "v_fma_mix_f32 v249, v149, v195, v203 op_sel:[1,0,0] op_sel_hi:[1,0,0]\n"
    "v_mfma_f32_16x16x32_bf16 v[60:63], a[188:191], v[140:143], v[60:63]\n"
    "ds_read_b64_tr_b16 a[184:185], v225 offset:33544\n"
    "v_cvt_pk_bf16_f32 v149, v248, v249\n"
    "v_mfma_f32_16x16x32_bf16 v[64:67], a[192:195], v[128:131], v[64:67]\n"
    "ds_read_b64_tr_b16 a[186:187], v225 offset:35592\n"
    "s_waitcnt vmcnt(13)\n"
    "v_fma_mix_f32 v246, v156, v192, v200 op_sel_hi:[1,0,0]\n"
    "v_mfma_f32_16x16x32_bf16 v[64:67], a[196:199], v[132:135], v[64:67]\n"
    "ds_read_b64_tr_b16 a[188:189], v225 offset:41736\n"
    "v_fma_mix_f32 v247, v156, v193, v201 op_sel:[1,0,0] op_sel_hi:[1,0,0]\n"
    "v_mfma_f32_16x16x32_bf16 v[68:71], a[192:195], v[136:139], v[68:71]\n"
    "ds_read_b64_tr_b16 a[190:191], v225 offset:43784\n"
    "v_cvt_pk_bf16_f32 v156, v246, v247\n"
    "v_mfma_f32_16x16x32_bf16 v[68:71], a[196:199], v[140:143], v[68:71]\n"
    "ds_read_b64_tr_b16 a[192:193], v225 offset:33792\n"
    "ds_read_b64_tr_b16 a[194:195], v225 offset:35840\n"
    "v_fma_mix_f32 v248, v157, v194, v202 op_sel_hi:[1,0,0]\n"
    "v_mfma_f32_16x16x32_bf16 v[72:75], a[200:203], v[128:131], v[72:75]\n"
    "ds_read_b64_tr_b16 a[196:197], v225 offset:41984\n"
    "v_fma_mix_f32 v249, v157, v195, v203 op_sel:[1,0,0] op_sel_hi:[1,0,0]\n"
    "v_mfma_f32_16x16x32_bf16 v[72:75], a[204:207], v[132:135], v[72:75]\n"
    "buffer_store_dwordx4 a[0:3], v244, s[56:59], s81 offen offset:256 sc1\n"
    "ds_read_b64_tr_b16 a[198:199], v225 offset:44032\n"
    "v_mfma_f32_16x16x32_bf16 v[76:79], a[200:203], v[136:139], v[76:79]\n"
    "v_lshrrev_b32 v250, 3, v239\n"
    "v_mul_u32_u24 v250, 18432, v250\n"
    "v_and_b32 v251, 0x70, v244\n"
    "ds_read_b64_tr_b16 a[200:201], v225 offset:33800\n"
    "v_mfma_f32_16x16x32_bf16 v[76:79], a[204:207], v[140:143], v[76:79]\n"
    "v_add_u32 v250, v250, v251\n"
    "ds_read_b64_tr_b16 a[202:203], v225 offset:35848\n"
    "v_cvt_pk_bf16_f32 v157, v248, v249\n"
    "v_mfma_f32_16x16x32_bf16 v[80:83], a[208:211], v[128:131], v[80:83]\n"
    "s_mul_i32 s81, s78, 165888\n"
    "s_lshl_b32 s80, s39, 7\n"
    "ds_read_b64_tr_b16 a[204:205], v225 offset:41992\n"
    "v_mfma_f32_16x16x32_bf16 v[80:83], a[212:215], v[132:135], v[80:83]\n"
    "s_add_u32 s81, s81, s80\n"
    "ds_read_b64_tr_b16 a[206:207], v225 offset:44040\n"
    "s_waitcnt lgkmcnt(3)\n"
    "v_fma_mix_f32 v246, v150, v196, v204 op_sel_hi:[1,0,0]\n"
    "v_mfma_f32_16x16x32_bf16 v[84:87], a[208:211], v[136:139], v[84:87]\n"
    "s_add_u32 s81, s81, 6291456\n"
    "ds_read_b64_tr_b16 a[208:209], v225 offset:34048\n"
    "ds_read_b64_tr_b16 a[210:211], v225 offset:36096\n"
    "v_mfma_f32_16x16x32_bf16 v[84:87], a[212:215], v[140:143], v[84:87]\n"
    "s_cmp_lt_u32 s39, 144\n"
    "v_fma_mix_f32 v247, v150, v197, v205 op_sel:[1,0,0] op_sel_hi:[1,0,0]\n"
    "v_mfma_f32_16x16x32_bf16 v[88:91], a[216:219], v[128:131], v[88:91]\n"
    "s_cselect_b32 s81, s81, 1073741816\n"
    "ds_read_b64_tr_b16 a[212:213], v225 offset:42240\n"
    "v_mfma_f32_16x16x32_bf16 v[88:91], a[220:223], v[132:135], v[88:91]\n"
    "ds_read_b64_tr_b16 a[214:215], v225 offset:44288\n"
    "v_cvt_pk_bf16_f32 v150, v246, v247\n"
    "v_mfma_f32_16x16x32_bf16 v[92:95], a[216:219], v[136:139], v[92:95]\n"
    "buffer_store_dwordx4 a[0:3], v250, s[56:59], s81 offen sc1\n"
    "v_mfma_f32_16x16x32_bf16 v[92:95], a[220:223], v[140:143], v[92:95]\n"
    "ds_read_b64_tr_b16 a[216:217], v225 offset:34056\n"
    "ds_read_b64_tr_b16 a[218:219], v225 offset:36104\n"
    "v_fma_mix_f32 v248, v151, v198, v206 op_sel_hi:[1,0,0]\n"
    "v_mfma_f32_16x16x32_bf16 v[96:99], a[224:227], v[128:131], v[96:99]\n"
    "ds_read_b64_tr_b16 a[220:221], v225 offset:42248\n"
    "v_fma_mix_f32 v249, v151, v199, v207 op_sel:[1,0,0] op_sel_hi:[1,0,0]\n"
    "v_mfma_f32_16x16x32_bf16 v[96:99], a[228:231], v[132:135], v[96:99]\n"
    "ds_read_b64_tr_b16 a[222:223], v225 offset:44296\n"
    "v_cvt_pk_bf16_f32 v151, v248, v249\n"
    "v_mfma_f32_16x16x32_bf16 v[100:103], a[224:227], v[136:139], v[100:103]\n"
    "ds_read_b64_tr_b16 a[224:225], v225 offset:34304\n"
    "v_fma_mix_f32 v246, v158, v196, v204 op_sel_hi:[1,0,0]\n"
    "v_mfma_f32_16x16x32_bf16 v[100:103], a[228:231], v[140:143], v[100:103]\n"
    "ds_read_b64_tr_b16 a[226:227], v225 offset:36352\n"
    "v_fma_mix_f32 v247, v158, v197, v205 op_sel:[1,0,0] op_sel_hi:[1,0,0]\n"
    "v_mfma_f32_16x16x32_bf16 v[104:107], a[232:235], v[128:131], v[104:107]\n"
    "s_add_u32 s81, s81, 18432\n"
    "ds_read_b64_tr_b16 a[228:229], v225 offset:42496\n"
    "v_mfma_f32_16x16x32_bf16 v[104:107], a[236:239], v[132:135], v[104:107]\n"
    "ds_read_b64_tr_b16 a[230:231], v225 offset:44544\n"
    "v_cvt_pk_bf16_f32 v158, v246, v247\n"
    "v_mfma_f32_16x16x32_bf16 v[108:111], a[232:235], v[136:139], v[108:111]\n"
    "buffer_store_dwordx4 a[0:3], v250, s[56:59], s81 offen sc1\n"
    "v_lshrrev_b32 v250, 1, v239\n"
    "v_min_u32 v250, 4, v250\n"
    "v_lshlrev_b32 v250, 9, v250\n"
    "ds_read_b64_tr_b16 a[232:233], v225 offset:34312\n"
    "v_mfma_f32_16x16x32_bf16 v[108:111], a[236:239], v[140:143], v[108:111]\n"
    "v_and_b32 v251, 16, v244\n"
    "v_add_u32 v250, v250, v251\n"
    "ds_read_b64_tr_b16 a[234:235], v225 offset:36360\n"
    "ds_read_b64_tr_b16 a[236:237], v225 offset:42504\n"
    "v_fma_mix_f32 v248, v159, v198, v206 op_sel_hi:[1,0,0]\n"
    "v_mfma_f32_16x16x32_bf16 v[112:115], a[240:243], v[128:131], v[112:115]\n"
    "s_mul_i32 s81, s88, 2560\n"
    "s_lshl_b32 s80, s89, 5\n"
    "ds_read_b64_tr_b16 a[238:239], v225 offset:44552\n"
    "v_mfma_f32_16x16x32_bf16 v[112:115], a[244:247], v[132:135], v[112:115]\n"
    "s_add_u32 s81, s81, s80\n"
    "v_add_u32 v250, s81, v250\n"
    "v_fma_mix_f32 v249, v159, v199, v207 op_sel:[1,0,0] op_sel_hi:[1,0,0]\n"
    "v_mfma_f32_16x16x32_bf16 v[116:119], a[240:243], v[136:139], v[116:119]\n"
    "s_lshl_b32 s81, s88, 7\n"
    "s_add_u32 s81, s81, s89\n"
    "ds_read_b64_tr_b16 a[240:241], v225 offset:34560\n"
    "ds_read_b64_tr_b16 a[242:243], v225 offset:36608\n"
    "v_mfma_f32_16x16x32_bf16 v[116:119], a[244:247], v[140:143], v[116:119]\n"
    "s_lshl_b32 s81, s81, 4\n"
    "s_add_u32 s81, s81, 65536\n"
    "v_cvt_pk_bf16_f32 v159, v248, v249\n"
    "v_mfma_f32_16x16x32_bf16 v[120:123], a[248:251], v[128:131], v[120:123]\n"
    "v_and_b32 v251, 7, v239\n"
    "v_lshl_add_u32 v251, v251, 8, s81\n"
    "ds_read_b64_tr_b16 a[244:245], v225 offset:42752\n"
    "v_mfma_f32_16x16x32_bf16 v[120:123], a[252:255], v[132:135], v[120:123]\n"
    "v_cmp_gt_u32 vcc, 16, v239\n"
    "s_nop 1\n"
    "v_cndmask_b32 v250, v251, v250, vcc\n"
    "v_mfma_f32_16x16x32_bf16 v[124:127], a[248:251], v[136:139], v[124:127]\n"
    "s_mov_b32 s81, 7340032\n"
    "ds_read_b64_tr_b16 a[246:247], v225 offset:44800\n"
    "v_mfma_f32_16x16x32_bf16 v[124:127], a[252:255], v[140:143], v[124:127]\n"
    "buffer_store_dwordx4 a[0:3], v250, s[56:59], s81 offen sc1\n"
    "v_mfma_f32_16x16x32_bf16 v[0:3], a[128:131], v[144:147], v[0:3]\n"
    "ds_read_b64_tr_b16 a[248:249], v225 offset:34568\n"
    "ds_read_b64_tr_b16 a[250:251], v225 offset:36616\n"
    "v_mfma_f32_16x16x32_bf16 v[0:3], a[132:135], v[148:151], v[0:3]\n"
    "ds_read_b64_tr_b16 a[252:253], v225 offset:42760\n"
    "ds_read_b64_tr_b16 a[254:255], v225 offset:44808\n"
    "v_mfma_f32_16x16x32_bf16 v[4:7], a[128:131], v[152:155], v[4:7]\n"
    "v_mov_b32 v192, 0\n"
    "v_mov_b32 v193, 0\n"
    "v_mfma_f32_16x16x32_bf16 v[4:7], a[132:135], v[156:159], v[4:7]\n"
    "v_mov_b32 v194, 0\n"
    "v_mov_b32 v195, 0\n"
    "v_mfma_f32_16x16x32_bf16 v[8:11], a[136:139], v[144:147], v[8:11]\n"
    "v_mov_b32 v196, 0\n"
    "v_mov_b32 v197, 0\n"
    "v_mfma_f32_16x16x32_bf16 v[8:11], a[140:143], v[148:151], v[8:11]\n"
    "v_mov_b32 v198, 0\n"
    "v_mov_b32 v199, 0\n"
    "v_mfma_f32_16x16x32_bf16 v[12:15], a[136:139], v[152:155], v[12:15]\n"
    "s_waitcnt vmcnt(9) lgkmcnt(0)\n"
    "s_barrier\n"
    "v_mfma_f32_16x16x32_bf16 v[12:15], a[140:143], v[156:159], v[12:15]\n"
    "s_mov_b32 m0, s77\n"
    "s_nop 0\n"
    "ds_read_b128 a[64:67], v208 offset:0\n"
    "ds_read_b128 a[68:71], v209 offset:0\n"
    "ds_read_b128 a[72:75], v210 offset:0\n"
    "ds_read_b128 a[76:79], v211 offset:0\n"
    "v_mfma_f32_16x16x32_bf16 v[16:19], a[144:147], v[144:147], v[16:19]\n"
    "buffer_load_dwordx4 v218, s[44:47], s72 offen offset:0  lds\n"
    "v_cvt_pk_bf16_f32 v246, v0, v1\n"
    "v_cvt_pk_bf16_f32 v247, v2, v3\n"
    "v_mfma_f32_16x16x32_bf16 v[16:19], a[148:151], v[148:151], v[16:19]\n"
    "ds_read_b128 a[80:83], v208 offset:4096\n"
    "ds_read_b128 a[84:87], v209 offset:4096\n"
    "v_cvt_pk_bf16_f32 v248, v8, v9\n"
    "v_cvt_pk_bf16_f32 v249, v10, v11\n"
    "v_mfma_f32_16x16x32_bf16 v[20:23], a[144:147], v[152:155], v[20:23]\n"
    "ds_read_b128 a[88:91], v210 offset:4096\n"
    "ds_read_b128 a[92:95], v211 offset:4096\n"
    "v_accvgpr_write_b32 a0, v246\n"
    "v_mfma_f32_16x16x32_bf16 v[20:23], a[148:151], v[156:159], v[20:23]\n"
    "buffer_load_dwordx4 v219, s[44:47], s72 offen offset:1024  lds\n"
    "v_mfma_f32_16x16x32_bf16 v[24:27], a[152:155], v[144:147], v[24:27]\n"
    "ds_read_b128 a[96:99], v208 offset:8192\n"
    "ds_read_b128 a[100:103], v209 offset:8192\n"
    "v_accvgpr_write_b32 a1, v247\n"
    "v_mfma_f32_16x16x32_bf16 v[24:27], a[156:159], v[148:151], v[24:27]\n"
    "ds_read_b128 a[104:107], v210 offset:8192\n"
    "ds_read_b128 a[108:111], v211 offset:8192\n"
    "v_accvgpr_write_b32 a2, v248\n"
    "v_mfma_f32_16x16x32_bf16 v[28:31], a[152:155], v[152:155], v[28:31]\n"
    "ds_read_b128 a[112:115], v208 offset:12288\n"
    "ds_read_b128 a[116:119], v209 offset:12288\n"
    "v_accvgpr_write_b32 a3, v249\n"
    "v_mfma_f32_16x16x32_bf16 v[28:31], a[156:159], v[156:159], v[28:31]\n"
    "buffer_load_dwordx4 v218, s[44:47], s72 offen offset:2048  lds\n"
    "v_mfma_f32_16x16x32_bf16 v[32:35], a[160:163], v[144:147], v[32:35]\n"
    "ds_read_b128 a[120:123], v210 offset:12288\n"
    "v_cvt_pk_bf16_f32 v246, v16, v17\n"
    "v_cvt_pk_bf16_f32 v247, v18, v19\n"
    "v_mfma_f32_16x16x32_bf16 v[32:35], a[164:167], v[148:151], v[32:35]\n"
    "ds_read_b128 a[124:127], v211 offset:12288\n"
    "v_cvt_pk_bf16_f32 v248, v24, v25\n"
    "v_mfma_f32_16x16x32_bf16 v[36:39], a[160:163], v[152:155], v[36:39]\n"
    "buffer_load_dwordx4 v219, s[44:47], s72 offen offset:3072  lds\n"
    "v_mfma_f32_16x16x32_bf16 v[36:39], a[164:167], v[156:159], v[36:39]\n"
    "ds_read_b64_tr_b16 a[128:129], v212 offset:16384\n"
    "v_cvt_pk_bf16_f32 v249, v26, v27\n"
    "v_mfma_f32_16x16x32_bf16 v[40:43], a[168:171], v[144:147], v[40:43]\n"
    "ds_read_b64_tr_b16 a[130:131], v212 offset:20480\n"
    "v_accvgpr_write_b32 a4, v246\n"
    "v_mfma_f32_16x16x32_bf16 v[40:43], a[172:175], v[148:151], v[40:43]\n"
    "ds_read_b64_tr_b16 a[132:133], v212 offset:24576\n"
    "v_accvgpr_write_b32 a5, v247\n"
    "v_mfma_f32_16x16x32_bf16 v[44:47], a[168:171], v[152:155], v[44:47]\n"
    "ds_read_b64_tr_b16 a[134:135], v212 offset:28672\n"
    "v_accvgpr_write_b32 a6, v248\n"
    "v_mfma_f32_16x16x32_bf16 v[44:47], a[172:175], v[156:159], v[44:47]\n"
    "ds_read_b64_tr_b16 a[136:137], v213 offset:16384\n"
    "v_accvgpr_write_b32 a7, v249\n"
    "v_mfma_f32_16x16x32_bf16 v[48:51], a[176:179], v[144:147], v[48:51]\n"
    "s_mul_i32 s81, s78, 36864\n"
    "s_add_u32 s81, s81, s38\n"
    "s_add_u32 m0, s73, 32768\n"
    "v_cvt_pk_bf16_f32 v246, v32, v33\n"
    "v_cvt_pk_bf16_f32 v247, v34, v35\n"
    "v_mfma_f32_16x16x32_bf16 v[48:51], a[180:183], v[148:151], v[48:51]\n"
    "s_nop 0\n"
    "ds_read_b64_tr_b16 a[138:139], v213 offset:20480\n"
    "v_cvt_pk_bf16_f32 v248, v40, v41\n"
    "v_mfma_f32_16x16x32_bf16 v[52:55], a[176:179], v[152:155], v[52:55]\n"
    "buffer_load_dwordx4 v233, s[52:55], s81 offen lds\n"
    "v_mfma_f32_16x16x32_bf16 v[52:55], a[180:183], v[156:159], v[52:55]\n"
    "ds_read_b64_tr_b16 a[140:141], v213 offset:24576\n"
    "v_cvt_pk_bf16_f32 v249, v42, v43\n"
    "v_mfma_f32_16x16x32_bf16 v[56:59], a[184:187], v[144:147], v[56:59]\n"
    "ds_read_b64_tr_b16 a[142:143], v213 offset:28672\n"
    "v_accvgpr_write_b32 a8, v246\n"
    "v_mfma_f32_16x16x32_bf16 v[56:59], a[188:191], v[148:151], v[56:59]\n"
    "ds_read_b64_tr_b16 a[144:145], v212 offset:16640\n"
    "v_accvgpr_write_b32 a9, v247\n"
    "v_mfma_f32_16x16x32_bf16 v[60:63], a[184:187], v[152:155], v[60:63]\n"
    "ds_read_b64_tr_b16 a[146:147], v212 offset:20736\n"
    "v_accvgpr_write_b32 a10, v248\n"
    "v_mfma_f32_16x16x32_bf16 v[60:63], a[188:191], v[156:159], v[60:63]\n"
    "ds_read_b64_tr_b16 a[148:149], v212 offset:24832\n"
    "v_accvgpr_write_b32 a11, v249\n"
    "v_mfma_f32_16x16x32_bf16 v[64:67], a[192:195], v[144:147], v[64:67]\n"
    "ds_read_b64_tr_b16 a[150:151], v212 offset:28928\n"
    "v_cvt_pk_bf16_f32 v246, v48, v49\n"
    "v_cvt_pk_bf16_f32 v247, v50, v51\n"
    "v_mfma_f32_16x16x32_bf16 v[64:67], a[196:199], v[148:151], v[64:67]\n"
    "ds_read_b64_tr_b16 a[152:153], v213 offset:16640\n"
    "v_cvt_pk_bf16_f32 v248, v56, v57\n"
    "v_cvt_pk_bf16_f32 v249, v58, v59\n"
    "v_mfma_f32_16x16x32_bf16 v[68:71], a[192:195], v[152:155], v[68:71]\n"
    "v_accvgpr_write_b32 a12, v246\n"
    "ds_read_b64_tr_b16 a[154:155], v213 offset:20736\n"
    "v_mfma_f32_16x16x32_bf16 v[68:71], a[196:199], v[156:159], v[68:71]\n"
    "v_accvgpr_write_b32 a13, v247\n"
    "v_accvgpr_write_b32 a14, v248\n"
    "v_mfma_f32_16x16x32_bf16 v[72:75], a[200:203], v[144:147], v[72:75]\n"
    "v_accvgpr_write_b32 a15, v249\n"
    "ds_read_b64_tr_b16 a[156:157], v213 offset:24832\n"
    "v_mfma_f32_16x16x32_bf16 v[72:75], a[204:207], v[148:151], v[72:75]\n"
    "v_cvt_pk_bf16_f32 v246, v4, v5\n"
    "v_cvt_pk_bf16_f32 v247, v6, v7\n"
    "v_mfma_f32_16x16x32_bf16 v[76:79], a[200:203], v[152:155], v[76:79]\n"
    "v_cvt_pk_bf16_f32 v248, v12, v13\n"
    "ds_read_b64_tr_b16 a[158:159], v213 offset:28928\n"
    "v_mfma_f32_16x16x32_bf16 v[76:79], a[204:207], v[156:159], v[76:79]\n"
    "v_cvt_pk_bf16_f32 v249, v14, v15\n"
    "v_accvgpr_write_b32 a16, v246\n"
    "v_mfma_f32_16x16x32_bf16 v[80:83], a[208:211], v[144:147], v[80:83]\n"
    "v_accvgpr_write_b32 a17, v247\n"
    "ds_read_b64_tr_b16 a[160:161], v212 offset:16896\n"
    "v_mfma_f32_16x16x32_bf16 v[80:83], a[212:215], v[148:151], v[80:83]\n"
    "v_accvgpr_write_b32 a18, v248\n"
    "v_accvgpr_write_b32 a19, v249\n"
    "v_mfma_f32_16x16x32_bf16 v[84:87], a[208:211], v[152:155], v[84:87]\n"
    "v_cvt_pk_bf16_f32 v246, v20, v21\n"
    "ds_read_b64_tr_b16 a[162:163], v212 offset:20992\n"
    "v_mfma_f32_16x16x32_bf16 v[84:87], a[212:215], v[156:159], v[84:87]\n"
    "v_cvt_pk_bf16_f32 v247, v22, v23\n"
    "v_cvt_pk_bf16_f32 v248, v28, v29\n"
    "v_mfma_f32_16x16x32_bf16 v[88:91], a[216:219], v[144:147], v[88:91]\n"
    "v_cvt_pk_bf16_f32 v249, v30, v31\n"
    "ds_read_b64_tr_b16 a[164:165], v212 offset:25088\n"
    "v_mfma_f32_16x16x32_bf16 v[88:91], a[220:223], v[148:151], v[88:91]\n"
    "v_accvgpr_write_b32 a20, v246\n"
    "v_accvgpr_write_b32 a21, v247\n"
    "v_mfma_f32_16x16x32_bf16 v[92:95], a[216:219], v[152:155], v[92:95]\n"
    "v_accvgpr_write_b32 a22, v248\n"
    "ds_read_b64_tr_b16 a[166:167], v212 offset:29184\n"
    "v_mfma_f32_16x16x32_bf16 v[92:95], a[220:223], v[156:159], v[92:95]\n"
    "v_accvgpr_write_b32 a23, v249\n"
    "v_cvt_pk_bf16_f32 v246, v36, v37\n"
    "s_waitcnt lgkmcnt(2)\n"
    "v_mfma_f32_16x16x32_bf16 v[128:131], a[64:67], a[0:3], 0\n"
    "v_cvt_pk_bf16_f32 v247, v38, v39\n"
    "v_cvt_pk_bf16_f32 v248, v44, v45\n"
    "v_mfma_f32_16x16x32_bf16 v[128:131], a[68:71], a[4:7], v[128:131]\n"
    "ds_read_b64_tr_b16 a[168:169], v213 offset:16896\n"
    "v_cvt_pk_bf16_f32 v249, v46, v47\n"
    "v_accvgpr_write_b32 a24, v246\n"
    "v_mfma_f32_16x16x32_bf16 v[128:131], a[72:75], a[8:11], v[128:131]\n"
    "v_accvgpr_write_b32 a25, v247\n"
    "v_accvgpr_write_b32 a26, v248\n"
    "v_mov_b32 v200, 0\n"
    "v_mfma_f32_16x16x32_bf16 v[128:131], a[76:79], a[12:15], v[128:131]\n"
    "ds_read_b64_tr_b16 a[170:171], v213 offset:20992\n"
    "v_accvgpr_write_b32 a27, v249\n"
    "v_cvt_pk_bf16_f32 v246, v52, v53\n"
    "v_mfma_f32_16x16x32_bf16 v[132:135], a[80:83], a[0:3], 0\n"
    "v_cvt_pk_bf16_f32 v247, v54, v55\n"
    "v_cvt_pk_bf16_f32 v248, v60, v61\n"
    "v_mov_b32 v201, 0\n"
    "v_mfma_f32_16x16x32_bf16 v[132:135], a[84:87], a[4:7], v[132:135]\n"
    "ds_read_b64_tr_b16 a[172:173], v213 offset:25088\n"
    "v_cvt_pk_bf16_f32 v249, v62, v63\n"
    "v_accvgpr_write_b32 a28, v246\n"
    "v_mfma_f32_16x16x32_bf16 v[132:135], a[88:91], a[8:11], v[132:135]\n"
    "v_accvgpr_write_b32 a29, v247\n"
    "v_accvgpr_write_b32 a30, v248\n"
    "v_mov_b32 v202, 0\n"
    "v_mfma_f32_16x16x32_bf16 v[132:135], a[92:95], a[12:15], v[132:135]\n"
    "ds_read_b64_tr_b16 a[174:175], v213 offset:29184\n"
    "v_accvgpr_write_b32 a31, v249\n"
    "v_cvt_pk_bf16_f32 v246, v64, v65\n"
    "v_mfma_f32_16x16x32_bf16 v[136:139], a[96:99], a[0:3], 0\n"
    "s_cmp_lt_u32 s78, 2\n"
    "s_cselect_b64 s[48:49], s[40:41], s[44:45]\n"
    "s_mov_b64 s[50:51], s[42:43]\n"
    "v_cvt_pk_bf16_f32 v247, v66, v67\n"
    "v_mov_b32 v203, 0\n"
    "v_mfma_f32_16x16x32_bf16 v[136:139], a[100:103], a[4:7], v[136:139]\n"
    "s_and_b32 s81, s89, 7\n"
    "ds_read_b64_tr_b16 a[176:177], v212 offset:17152\n"
    "v_cvt_pk_bf16_f32 v248, v72, v73\n"
    "v_mfma_f32_16x16x32_bf16 v[136:139], a[104:107], a[8:11], v[136:139]\n"
    "s_lshl_b32 s81, s81, 1\n"
    "v_cvt_pk_bf16_f32 v249, v74, v75\n"
    "v_mov_b32 v204, 0\n"
    "v_mfma_f32_16x16x32_bf16 v[136:139], a[108:111], a[12:15], v[136:139]\n"
    "s_and_b32 s80, s78, 1\n"
    "ds_read_b64_tr_b16 a[178:179], v212 offset:21248\n"
    "v_accvgpr_write_b32 a32, v246\n"
    "v_mfma_f32_16x16x32_bf16 v[140:143], a[112:115], a[0:3], 0\n"
    "s_add_u32 s81, s81, s80\n"
    "v_accvgpr_write_b32 a33, v247\n"
    "v_mov_b32 v205, 0\n"
    "v_mfma_f32_16x16x32_bf16 v[140:143], a[116:119], a[4:7], v[140:143]\n"
    "ds_read_b64_tr_b16 a[180:181], v212 offset:25344\n"
    "v_accvgpr_write_b32 a34, v248\n"
    "v_accvgpr_write_b32 a35, v249\n"
    "v_mfma_f32_16x16x32_bf16 v[140:143], a[120:123], a[8:11], v[140:143]\n"
    "s_lshl_b32 s81, s81, 10\n"
    "v_cvt_pk_bf16_f32 v246, v68, v69\n"
    "v_mov_b32 v206, 0\n"
    "v_mfma_f32_16x16x32_bf16 v[140:143], a[124:127], a[12:15], v[140:143]\n"
    "ds_read_b64_tr_b16 a[182:183], v212 offset:29440\n"
    "v_cvt_pk_bf16_f32 v247, v70, v71\n"
    "v_cvt_pk_bf16_f32 v248, v76, v77\n"
    "v_mfma_f32_16x16x32_bf16 v[96:99], a[224:227], v[144:147], v[96:99]\n"
    "s_add_u32 s81, s81, 49152\n"
    "v_cvt_pk_bf16_f32 v249, v78, v79\n"
    "v_mov_b32 v207, 0\n"
    "v_mfma_f32_16x16x32_bf16 v[96:99], a[228:231], v[148:151], v[96:99]\n"
    "ds_read_b64_tr_b16 a[184:185], v213 offset:17152\n"
    "v_accvgpr_write_b32 a48, v246\n"
    "v_accvgpr_write_b32 a49, v247\n"
    "v_mfma_f32_16x16x32_bf16 v[100:103], a[224:227], v[152:155], v[100:103]\n"
    "v_and_b32 v228, 7, v239\n"
    "v_accvgpr_write_b32 a50, v248\n"
    "v_max3_f32 v250, v128, v129, v130\n"
    "v_max3_f32 v250, v250, v131, v132\n"
    "v_max3_f32 v250, v250, v133, v134\n"
    "v_max3_f32 v250, v250, v135, v136\n"
    "v_max3_f32 v250, v250, v137, v138\n"
    "v_max3_f32 v250, v250, v139, v140\n"
    "v_max3_f32 v250, v250, v141, v142\n"
    "v_max_f32 v250, v250, v143\n"
    "v_mov_b32 v251, v250\n"
    "s_nop 1\n"
    "v_permlane32_swap_b32 v250, v251\n"
    "v_mfma_f32_16x16x32_bf16 v[100:103], a[228:231], v[156:159], v[100:103]\n"
    "ds_read_b64_tr_b16 a[186:187], v213 offset:21248\n"
    "v_accvgpr_write_b32 a51, v249\n"
    "s_nop 0\n"
    "v_mfma_f32_16x16x32_bf16 v[104:107], a[232:235], v[144:147], v[104:107]\n"
    "v_lshl_add_u32 v228, v228, 7, s81\n"
    "v_cvt_pk_bf16_f32 v246, v80, v81\n"
    "v_max_f32 v250, v250, v251\n"
    "v_mfma_f32_16x16x32_bf16 v[104:107], a[236:239], v[148:151], v[104:107]\n"
    "ds_read_b64_tr_b16 a[188:189], v213 offset:25344\n"
    "v_cvt_pk_bf16_f32 v247, v82, v83\n"
    "v_mov_b32 v251, v250\n"
    "v_mfma_f32_16x16x32_bf16 v[108:111], a[232:235], v[152:155], v[108:111]\n"
    "v_cvt_pk_bf16_f32 v248, v88, v89\n"
    "v_permlane16_swap_b32 v250, v251\n"
    "s_nop 0\n"
    "v_mfma_f32_16x16x32_bf16 v[108:111], a[236:239], v[156:159], v[108:111]\n"
    "ds_read_b64_tr_b16 a[190:191], v213 offset:29440\n"
    "v_max_f32 v250, v250, v251\n"
    "v_add_f32 v250, 0x42cc0000, v250\n"
    "v_mfma_f32_16x16x32_bf16 v[112:115], a[240:243], v[144:147], v[112:115]\n"
    "v_cvt_pk_bf16_f32 v249, v90, v91\n"
    "v_sub_f32 v128, v128, v250\n"
    "v_sub_f32 v129, v129, v250\n"
    "v_mfma_f32_16x16x32_bf16 v[112:115], a[244:247], v[148:151], v[112:115]\n"
    "v_accvgpr_write_b32 a36, v246\n"
    "v_sub_f32 v130, v130, v250\n"
    "v_sub_f32 v131, v131, v250\n"
    "v_mfma_f32_16x16x32_bf16 v[116:119], a[240:243], v[152:155], v[116:119]\n"
    "v_sub_f32 v176, 0, v250\n"
    "v_exp_f32 v128, v128\n"
    "v_mfma_f32_16x16x32_bf16 v[116:119], a[244:247], v[156:159], v[116:119]\n"
    "v_accvgpr_write_b32 a37, v247\n"
    "v_exp_f32 v129, v129\n"
    "v_mfma_f32_16x16x32_bf16 v[120:123], a[248:251], v[144:147], v[120:123]\n"
    "v_exp_f32 v130, v130\n"
    "v_mfma_f32_16x16x32_bf16 v[120:123], a[252:255], v[148:151], v[120:123]\n"
    "v_accvgpr_write_b32 a38, v248\n"
    "v_exp_f32 v131, v131\n"
    "v_cvt_pk_bf16_f32 v160, v128, v129\n"
    "v_mfma_f32_16x16x32_bf16 v[124:127], a[248:251], v[152:155], v[124:127]\n"
    "s_waitcnt vmcnt(1) lgkmcnt(0)\n"
    "s_barrier\n"
    "v_cvt_pk_bf16_f32 v161, v130, v131\n"
    "v_mfma_f32_16x16x32_bf16 v[124:127], a[252:255], v[156:159], v[124:127]\n"
    "s_mov_b32 s72, 0x8000\n"
    "s_mov_b32 m0, s74\n"
    "s_nop 0\n"
    "v_accvgpr_write_b32 a39, v249\n"
    "v_sub_f32 v132, v132, v250\n"
    "v_sub_f32 v133, v133, v250\n"
    "v_mfma_f32_16x16x32_bf16 v[144:147], a[64:67], a[16:19], 0\n"
    "buffer_load_dwordx4 v214, s[40:43], s72 offen offset:0  lds\n"
    "v_sub_f32 v134, v134, v250\n"
    "v_mfma_f32_16x16x32_bf16 v[144:147], a[68:71], a[20:23], v[144:147]\n"
    "v_sub_f32 v135, v135, v250\n"
    "v_sub_f32 v177, 0, v250\n"
    "v_exp_f32 v132, v132\n"
    "v_mfma_f32_16x16x32_bf16 v[144:147], a[72:75], a[24:27], v[144:147]\n"
    "v_cvt_pk_bf16_f32 v246, v84, v85\n"
    "v_exp_f32 v133, v133\n"
    "v_mfma_f32_16x16x32_bf16 v[144:147], a[76:79], a[28:31], v[144:147]\n"
    "v_exp_f32 v134, v134\n"
    "v_exp_f32 v135, v135\n"
    "v_mfma_f32_16x16x32_bf16 v[148:151], a[80:83], a[16:19], 0\n"
    "v_cvt_pk_bf16_f32 v247, v86, v87\n"
    "v_cvt_pk_bf16_f32 v162, v132, v133\n"
    "v_cvt_pk_bf16_f32 v163, v134, v135\n"
    "v_mfma_f32_16x16x32_bf16 v[148:151], a[84:87], a[20:23], v[148:151]\n"
    "buffer_load_dwordx4 v215, s[40:43], s72 offen offset:1024  lds\n"
    "v_sub_f32 v136, v136, v250\n"
    "v_mfma_f32_16x16x32_bf16 v[148:151], a[88:91], a[24:27], v[148:151]\n"
    "v_cvt_pk_bf16_f32 v248, v92, v93\n"
    "v_sub_f32 v137, v137, v250\n"
    "v_sub_f32 v138, v138, v250\n"
    "v_sub_f32 v139, v139, v250\n"
    "v_mfma_f32_16x16x32_bf16 v[148:151], a[92:95], a[28:31], v[148:151]\n"
    "v_cvt_pk_bf16_f32 v249, v94, v95\n"
    "v_sub_f32 v178, 0, v250\n"
    "v_exp_f32 v136, v136\n"
    "v_mfma_f32_16x16x32_bf16 v[152:155], a[96:99], a[16:19], 0\n"
    "v_exp_f32 v137, v137\n"
    "v_exp_f32 v138, v138\n"
    "v_mfma_f32_16x16x32_bf16 v[152:155], a[100:103], a[20:23], v[152:155]\n"
    "v_accvgpr_write_b32 a52, v246\n"
    "v_exp_f32 v139, v139\n"
    "v_cvt_pk_bf16_f32 v164, v136, v137\n"
    "v_mfma_f32_16x16x32_bf16 v[152:155], a[104:107], a[24:27], v[152:155]\n"
    "buffer_load_dwordx4 v216, s[40:43], s72 offen offset:2048  lds\n"
    "v_cvt_pk_bf16_f32 v165, v138, v139\n"
    "v_mfma_f32_16x16x32_bf16 v[152:155], a[108:111], a[28:31], v[152:155]\n"
    "v_accvgpr_write_b32 a53, v247\n"
    "v_sub_f32 v140, v140, v250\n"
    "v_sub_f32 v141, v141, v250\n"
    "v_sub_f32 v142, v142, v250\n"
    "v_mfma_f32_16x16x32_bf16 v[156:159], a[112:115], a[16:19], 0\n"
    "v_accvgpr_write_b32 a54, v248\n"
    "v_sub_f32 v143, v143, v250\n"
    "v_sub_f32 v179, 0, v250\n"
    "v_exp_f32 v140, v140\n"
    "v_mfma_f32_16x16x32_bf16 v[156:159], a[116:119], a[20:23], v[156:159]\n"
    "v_exp_f32 v141, v141\n"
    "v_exp_f32 v142, v142\n"
    "v_mfma_f32_16x16x32_bf16 v[156:159], a[120:123], a[24:27], v[156:159]\n"
    "v_accvgpr_write_b32 a55, v249\n"
    "v_exp_f32 v143, v143\n"
    "v_cvt_pk_bf16_f32 v166, v140, v141\n"
    "v_cvt_pk_bf16_f32 v167, v142, v143\n"
    "v_mfma_f32_16x16x32_bf16 v[156:159], a[124:127], a[28:31], v[156:159]\n"
    "buffer_load_dwordx4 v217, s[40:43], s72 offen offset:3072  lds\n"
    "v_mfma_f32_16x16x32_bf16 v[0:3], a[128:131], v[160:163], 0\n"
    "v_cvt_pk_bf16_f32 v246, v96, v97\n"
    "v_cvt_pk_bf16_f32 v247, v98, v99\n"
    "v_mfma_f32_16x16x32_bf16 v[0:3], a[132:135], v[164:167], v[0:3]\n"
    "v_cvt_pk_bf16_f32 v248, v104, v105\n"
    "v_cvt_pk_bf16_f32 v249, v106, v107\n"
    "v_mfma_f32_16x16x32_bf16 v[4:7], a[136:139], v[160:163], 0\n"
    "v_accvgpr_write_b32 a40, v246\n"
    "v_accvgpr_write_b32 a41, v247\n"
    "v_mfma_f32_16x16x32_bf16 v[4:7], a[140:143], v[164:167], v[4:7]\n"
    "s_mov_b32 m0, s76\n"
    "v_accvgpr_write_b32 a42, v248\n"
    "v_mfma_f32_16x16x32_bf16 v[8:11], a[144:147], v[160:163], 0\n"
    "s_nop 0\n"
    "v_accvgpr_write_b32 a43, v249\n"
    "v_mfma_f32_16x16x32_bf16 v[8:11], a[148:151], v[164:167], v[8:11]\n"
    "buffer_load_dwordx4 v218, s[44:47], s72 offen offset:0  lds\n"
    "v_mfma_f32_16x16x32_bf16 v[12:15], a[152:155], v[160:163], 0\n"
    "v_cvt_pk_bf16_f32 v246, v100, v101\n"
    "v_cvt_pk_bf16_f32 v247, v102, v103\n"
    "v_mfma_f32_16x16x32_bf16 v[12:15], a[156:159], v[164:167], v[12:15]\n"
    "v_cvt_pk_bf16_f32 v248, v108, v109\n"
    "v_cvt_pk_bf16_f32 v249, v110, v111\n"
    "v_mfma_f32_16x16x32_bf16 v[16:19], a[160:163], v[160:163], 0\n"
    "v_accvgpr_write_b32 a56, v246\n"
    "v_accvgpr_write_b32 a57, v247\n"
    "v_mfma_f32_16x16x32_bf16 v[16:19], a[164:167], v[164:167], v[16:19]\n"
    "v_accvgpr_write_b32 a58, v248\n"
    "v_accvgpr_write_b32 a59, v249\n"
    "v_mfma_f32_16x16x32_bf16 v[20:23], a[168:171], v[160:163], 0\n"
    "buffer_load_dwordx4 v219, s[44:47], s72 offen offset:1024  lds\n"
    "v_mfma_f32_16x16x32_bf16 v[20:23], a[172:175], v[164:167], v[20:23]\n"
    "v_cvt_pk_bf16_f32 v246, v112, v113\n"
    "v_cvt_pk_bf16_f32 v247, v114, v115\n"
    "v_mfma_f32_16x16x32_bf16 v[24:27], a[176:179], v[160:163], 0\n"
    "v_cvt_pk_bf16_f32 v248, v120, v121\n"
    "v_cvt_pk_bf16_f32 v249, v122, v123\n"
    "v_mfma_f32_16x16x32_bf16 v[24:27], a[180:183], v[164:167], v[24:27]\n"
    "v_accvgpr_write_b32 a44, v246\n"
    "v_max3_f32 v250, v144, v145, v146\n"
    "v_max3_f32 v250, v250, v147, v148\n"
    "v_max3_f32 v250, v250, v149, v150\n"
    "v_mfma_f32_16x16x32_bf16 v[28:31], a[184:187], v[160:163], 0\n"
    "v_accvgpr_write_b32 a45, v247\n"
    "v_max3_f32 v250, v250, v151, v152\n"
    "v_max3_f32 v250, v250, v153, v154\n"
    "v_max3_f32 v250, v250, v155, v156\n"
    "v_mfma_f32_16x16x32_bf16 v[28:31], a[188:191], v[164:167], v[28:31]\n"
    "buffer_load_dwordx4 v218, s[44:47], s72 offen offset:2048  lds\n"
    "v_max3_f32 v250, v250, v157, v158\n"
    "v_smfmac_f32_16x16x64_bf16 v[192:195], v[220:223], v[160:167], v224\n"
    "v_accvgpr_write_b32 a46, v248\n"
    "v_max_f32 v250, v250, v159\n"
    "v_mov_b32 v251, v250\n"
    "s_nop 1\n"
    "v_permlane32_swap_b32 v250, v251\n"
    "v_mfma_f32_16x16x32_bf16 v[128:131], a[64:67], a[32:35], 0\n"
    "v_accvgpr_write_b32 a47, v249\n"
    "s_nop 0\n"
    "v_max_f32 v250, v250, v251\n"
    "v_mov_b32 v251, v250\n"
    "v_mfma_f32_16x16x32_bf16 v[128:131], a[68:71], a[36:39], v[128:131]\n"
    "s_nop 0\n"
    "v_permlane16_swap_b32 v250, v251\n"
    "s_nop 0\n"
    "v_max_f32 v250, v250, v251\n"
    "v_add_f32 v250, 0x42cc0000, v250\n"
    "v_mfma_f32_16x16x32_bf16 v[128:131], a[72:75], a[40:43], v[128:131]\n"
    "v_sub_f32 v144, v144, v250\n"
    "v_sub_f32 v145, v145, v250\n"
    "v_sub_f32 v146, v146, v250\n"
    "v_sub_f32 v147, v147, v250\n"
    "v_mfma_f32_16x16x32_bf16 v[128:131], a[76:79], a[44:47], v[128:131]\n"
    "buffer_load_dwordx4 v219, s[44:47], s72 offen offset:3072  lds\n"
    "v_sub_f32 v180, 0, v250\n"
    "v_mfma_f32_16x16x32_bf16 v[132:135], a[80:83], a[32:35], 0\n"
    "v_exp_f32 v144, v144\n"
    "v_exp_f32 v145, v145\n"
    "v_mfma_f32_16x16x32_bf16 v[132:135], a[84:87], a[36:39], v[132:135]\n"
    "v_exp_f32 v146, v146\n"
    "v_exp_f32 v147, v147\n"
    "v_cvt_pk_bf16_f32 v168, v144, v145\n"
    "v_mfma_f32_16x16x32_bf16 v[132:135], a[88:91], a[40:43], v[132:135]\n"
    "v_cvt_pk_bf16_f32 v169, v146, v147\n"
    "v_sub_f32 v148, v148, v250\n"
    "v_sub_f32 v149, v149, v250\n"
    "v_sub_f32 v150, v150, v250\n"
    "v_sub_f32 v151, v151, v250\n"
    "v_mfma_f32_16x16x32_bf16 v[132:135], a[92:95], a[44:47], v[132:135]\n"
    "v_sub_f32 v181, 0, v250\n"
    "v_exp_f32 v148, v148\n"
    "v_exp_f32 v149, v149\n"
    "v_mfma_f32_16x16x32_bf16 v[136:139], a[96:99], a[32:35], 0\n"
    "v_exp_f32 v150, v150\n"
    "v_exp_f32 v151, v151\n"
    "v_mfma_f32_16x16x32_bf16 v[136:139], a[100:103], a[36:39], v[136:139]\n"
    "ds_read_b64_tr_b16 a[192:193], v212 offset:0\n"
    "v_cvt_pk_bf16_f32 v170, v148, v149\n"
    "v_cvt_pk_bf16_f32 v171, v150, v151\n"
    "v_sub_f32 v152, v152, v250\n"
    "v_sub_f32 v153, v153, v250\n"
    "v_mfma_f32_16x16x32_bf16 v[136:139], a[104:107], a[40:43], v[136:139]\n"
    "ds_read_b64_tr_b16 a[194:195], v212 offset:4096\n"
    "v_sub_f32 v154, v154, v250\n"
    "v_sub_f32 v155, v155, v250\n"
    "v_sub_f32 v182, 0, v250\n"
    "v_exp_f32 v152, v152\n"
    "v_mfma_f32_16x16x32_bf16 v[136:139], a[108:111], a[44:47], v[136:139]\n"
    "v_exp_f32 v153, v153\n"
    "v_exp_f32 v154, v154\n"
    "v_exp_f32 v155, v155\n"
    "v_mfma_f32_16x16x32_bf16 v[140:143], a[112:115], a[32:35], 0\n"
    "ds_read_b64_tr_b16 a[196:197], v212 offset:8192\n"
    "v_cvt_pk_bf16_f32 v172, v152, v153\n"
    "v_cvt_pk_bf16_f32 v173, v154, v155\n"
    "v_sub_f32 v156, v156, v250\n"
    "v_sub_f32 v157, v157, v250\n"
    "v_mfma_f32_16x16x32_bf16 v[140:143], a[116:119], a[36:39], v[140:143]\n"
    "v_sub_f32 v158, v158, v250\n"
    "v_sub_f32 v159, v159, v250\n"
    "v_sub_f32 v183, 0, v250\n"
    "v_exp_f32 v156, v156\n"
    "v_exp_f32 v157, v157\n"
    "v_mfma_f32_16x16x32_bf16 v[140:143], a[120:123], a[40:43], v[140:143]\n"
    "ds_read_b64_tr_b16 a[198:199], v212 offset:12288\n"
    "v_exp_f32 v158, v158\n"
    "v_exp_f32 v159, v159\n"
    "v_cvt_pk_bf16_f32 v174, v156, v157\n"
    "v_cvt_pk_bf16_f32 v175, v158, v159\n"
    "v_mfma_f32_16x16x32_bf16 v[140:143], a[124:127], a[44:47], v[140:143]\n"
    "ds_read_b64_tr_b16 a[200:201], v213 offset:0\n"
    "ds_read_b64_tr_b16 a[202:203], v213 offset:4096\n"
    "v_mfma_f32_16x16x32_bf16 v[32:35], a[128:131], v[168:171], 0\n"
    "ds_read_b64_tr_b16 a[204:205], v213 offset:8192\n"
    "ds_read_b64_tr_b16 a[206:207], v213 offset:12288\n"
    "v_mfma_f32_16x16x32_bf16 v[32:35], a[132:135], v[172:175], v[32:35]\n"
    "ds_read_b64_tr_b16 a[208:209], v212 offset:256\n"
    "ds_read_b64_tr_b16 a[210:211], v212 offset:4352\n"
    "v_mfma_f32_16x16x32_bf16 v[36:39], a[136:139], v[168:171], 0\n"
    "ds_read_b64_tr_b16 a[212:213], v212 offset:8448\n"
    "ds_read_b64_tr_b16 a[214:215], v212 offset:12544\n"
    "v_mfma_f32_16x16x32_bf16 v[36:39], a[140:143], v[172:175], v[36:39]\n"
    "ds_read_b64_tr_b16 a[216:217], v213 offset:256\n"
    "ds_read_b64_tr_b16 a[218:219], v213 offset:4352\n"
    "v_mfma_f32_16x16x32_bf16 v[40:43], a[144:147], v[168:171], 0\n"
    "ds_read_b64_tr_b16 a[220:221], v213 offset:8448\n"
    "ds_read_b64_tr_b16 a[222:223], v213 offset:12544\n"
    "v_mfma_f32_16x16x32_bf16 v[40:43], a[148:151], v[172:175], v[40:43]\n"
    "ds_read_b64_tr_b16 a[224:225], v212 offset:512\n"
    "ds_read_b64_tr_b16 a[226:227], v212 offset:4608\n"
    "v_mfma_f32_16x16x32_bf16 v[44:47], a[152:155], v[168:171], 0\n"
    "ds_read_b64_tr_b16 a[228:229], v212 offset:8704\n"
    "ds_read_b64_tr_b16 a[230:231], v212 offset:12800\n"
    "v_mfma_f32_16x16x32_bf16 v[44:47], a[156:159], v[172:175], v[44:47]\n"
    "ds_read_b64_tr_b16 a[232:233], v213 offset:512\n"
    "ds_read_b64_tr_b16 a[234:235], v213 offset:4608\n"
    "v_mfma_f32_16x16x32_bf16 v[48:51], a[160:163], v[168:171], 0\n"
    "ds_read_b64_tr_b16 a[236:237], v213 offset:8704\n"
    "ds_read_b64_tr_b16 a[238:239], v213 offset:12800\n"
    "v_mfma_f32_16x16x32_bf16 v[48:51], a[164:167], v[172:175], v[48:51]\n"
    "ds_read_b64_tr_b16 a[240:241], v212 offset:768\n"
    "v_cvt_pk_bf16_f32 v246, v116, v117\n"
    "v_mfma_f32_16x16x32_bf16 v[52:55], a[168:171], v[168:171], 0\n"
    "ds_read_b64_tr_b16 a[242:243], v212 offset:4864\n"
    "v_cvt_pk_bf16_f32 v247, v118, v119\n"
    "v_mfma_f32_16x16x32_bf16 v[52:55], a[172:175], v[172:175], v[52:55]\n"
    "ds_read_b64_tr_b16 a[244:245], v212 offset:8960\n"
    "v_cvt_pk_bf16_f32 v248, v124, v125\n"
    "v_mfma_f32_16x16x32_bf16 v[56:59], a[176:179], v[168:171], 0\n"
    "ds_read_b64_tr_b16 a[246:247], v212 offset:13056\n"
    "v_cvt_pk_bf16_f32 v249, v126, v127\n"
    "v_mfma_f32_16x16x32_bf16 v[56:59], a[180:183], v[172:175], v[56:59]\n"
    "ds_read_b64_tr_b16 a[248:249], v213 offset:768\n"
    "v_accvgpr_write_b32 a60, v246\n"
    "v_mfma_f32_16x16x32_bf16 v[60:63], a[184:187], v[168:171], 0\n"
    "ds_read_b64_tr_b16 a[250:251], v213 offset:4864\n"
    "v_accvgpr_write_b32 a61, v247\n"
    "v_mfma_f32_16x16x32_bf16 v[60:63], a[188:191], v[172:175], v[60:63]\n"
    "ds_read_b64_tr_b16 a[252:253], v213 offset:8960\n"
    "v_accvgpr_write_b32 a62, v248\n"
    "v_smfmac_f32_16x16x64_bf16 v[196:199], v[220:223], v[168:175], v224\n"
    "ds_read_b64_tr_b16 a[254:255], v213 offset:13056\n"
    "v_accvgpr_write_b32 a63, v249\n"
    "v_mfma_f32_16x16x32_bf16 v[144:147], a[64:67], a[48:51], 0\n"
    "v_mov_b32 v96, 0\n"
    "v_mov_b32 v97, 0\n"
    "v_mov_b32 v98, 0\n"
    "v_mov_b32 v99, 0\n"
    "v_mov_b32 v100, 0\n"
    "v_mov_b32 v101, 0\n"
    "v_mov_b32 v102, 0\n"
    "v_mfma_f32_16x16x32_bf16 v[144:147], a[68:71], a[52:55], v[144:147]\n"
    "v_mov_b32 v103, 0\n"
    "v_max3_f32 v250, v128, v129, v130\n"
    "v_max3_f32 v250, v250, v131, v132\n"
    "v_max3_f32 v250, v250, v133, v134\n"
    "v_mfma_f32_16x16x32_bf16 v[144:147], a[72:75], a[56:59], v[144:147]\n"
    "v_mov_b32 v104, 0\n"
    "v_max3_f32 v250, v250, v135, v136\n"
    "v_max3_f32 v250, v250, v137, v138\n"
    "v_max3_f32 v250, v250, v139, v140\n"
    "v_max3_f32 v250, v250, v141, v142\n"
    "v_mfma_f32_16x16x32_bf16 v[144:147], a[76:79], a[60:63], v[144:147]\n"
    "v_mov_b32 v105, 0\n"
    "v_max_f32 v250, v250, v143\n"
    "v_mov_b32 v251, v250\n"
    "s_nop 1\n"
    "v_permlane32_swap_b32 v250, v251\n"
    "s_nop 0\n"
    "v_mfma_f32_16x16x32_bf16 v[148:151], a[80:83], a[48:51], 0\n"
    "v_mov_b32 v106, 0\n"
    "v_max_f32 v250, v250, v251\n"
    "v_mov_b32 v251, v250\n"
    "s_nop 1\n"
    "v_permlane16_swap_b32 v250, v251\n"
    "s_nop 0\n"
    "v_max_f32 v250, v250, v251\n"
    "v_mfma_f32_16x16x32_bf16 v[148:151], a[84:87], a[52:55], v[148:151]\n"
    "v_mov_b32 v107, 0\n"
    "v_add_f32 v250, 0x42cc0000, v250\n"
    "v_sub_f32 v128, v128, v250\n"
    "v_sub_f32 v129, v129, v250\n"
    "v_sub_f32 v130, v130, v250\n"
    "v_sub_f32 v131, v131, v250\n"
    "v_mfma_f32_16x16x32_bf16 v[148:151], a[88:91], a[56:59], v[148:151]\n"
    "ds_read_b128 a[64:67], v208 offset:16384\n"
    "v_sub_f32 v184, 0, v250\n"
    "v_exp_f32 v128, v128\n"
    "v_exp_f32 v129, v129\n"
    "v_mfma_f32_16x16x32_bf16 v[148:151], a[92:95], a[60:63], v[148:151]\n"
    "v_mov_b32 v108, 0\n"
    "v_exp_f32 v130, v130\n"
    "v_exp_f32 v131, v131\n"
    "v_cvt_pk_bf16_f32 v160, v128, v129\n"
    "v_cvt_pk_bf16_f32 v161, v130, v131\n"
    "v_mfma_f32_16x16x32_bf16 v[152:155], a[96:99], a[48:51], 0\n"
    "ds_read_b128 a[68:71], v209 offset:16384\n"
    "v_mov_b32 v109, 0\n"
    "v_sub_f32 v132, v132, v250\n"
    "v_sub_f32 v133, v133, v250\n"
    "v_sub_f32 v134, v134, v250\n"
    "v_sub_f32 v135, v135, v250\n"
    "v_sub_f32 v185, 0, v250\n"
    "v_mfma_f32_16x16x32_bf16 v[152:155], a[100:103], a[52:55], v[152:155]\n"
    "v_mov_b32 v110, 0\n"
    "v_exp_f32 v132, v132\n"
    "v_exp_f32 v133, v133\n"
    "v_exp_f32 v134, v134\n"
    "v_mfma_f32_16x16x32_bf16 v[152:155], a[104:107], a[56:59], v[152:155]\n"
    "ds_read_b128 a[72:75], v210 offset:16384\n"
    "v_mov_b32 v111, 0\n"
    "v_exp_f32 v135, v135\n"
    "v_cvt_pk_bf16_f32 v162, v132, v133\n"
    "v_cvt_pk_bf16_f32 v163, v134, v135\n"
    "v_sub_f32 v136, v136, v250\n"
    "v_sub_f32 v137, v137, v250\n"
    "v_mfma_f32_16x16x32_bf16 v[152:155], a[108:111], a[60:63], v[152:155]\n"
    "s_mov_b32 s72, 0xc000\n"
    "v_sub_f32 v138, v138, v250\n"
    "v_sub_f32 v139, v139, v250\n"
    "v_sub_f32 v186, 0, v250\n"
    "v_exp_f32 v136, v136\n"
    "v_exp_f32 v137, v137\n"
    "v_mfma_f32_16x16x32_bf16 v[156:159], a[112:115], a[48:51], 0\n"
    "ds_read_b128 a[76:79], v211 offset:16384\n"
    "v_mov_b32 v112, 0\n"
    "v_exp_f32 v138, v138\n"
    "v_exp_f32 v139, v139\n"
    "v_cvt_pk_bf16_f32 v164, v136, v137\n"
    "v_cvt_pk_bf16_f32 v165, v138, v139\n"
    "v_mfma_f32_16x16x32_bf16 v[156:159], a[116:119], a[52:55], v[156:159]\n"
    "v_sub_f32 v140, v140, v250\n"
    "v_sub_f32 v141, v141, v250\n"
    "v_sub_f32 v142, v142, v250\n"
    "v_sub_f32 v143, v143, v250\n"
    "v_sub_f32 v187, 0, v250\n"
    "v_exp_f32 v140, v140\n"
    "v_exp_f32 v141, v141\n"
    "v_mfma_f32_16x16x32_bf16 v[156:159], a[120:123], a[56:59], v[156:159]\n"
    "ds_read_b128 a[80:83], v208 offset:20480\n"
    "v_mov_b32 v113, 0\n"
    "v_exp_f32 v142, v142\n"
    "v_exp_f32 v143, v143\n"
    "v_cvt_pk_bf16_f32 v166, v140, v141\n"
    "v_cvt_pk_bf16_f32 v167, v142, v143\n"
    "v_mfma_f32_16x16x32_bf16 v[156:159], a[124:127], a[60:63], v[156:159]\n"
    "ds_read_b128 a[84:87], v209 offset:20480\n"
    "v_mov_b32 v114, 0\n"
    "v_mfma_f32_16x16x32_bf16 v[64:67], a[128:131], v[160:163], 0\n"
    "ds_read_b128 a[88:91], v210 offset:20480\n"
    "v_mov_b32 v115, 0\n"
    "v_mfma_f32_16x16x32_bf16 v[64:67], a[132:135], v[164:167], v[64:67]\n"
    "ds_read_b128 a[92:95], v211 offset:20480\n"
    "v_mov_b32 v116, 0\n"
    "v_mfma_f32_16x16x32_bf16 v[68:71], a[136:139], v[160:163], 0\n"
    "ds_read_b128 a[96:99], v208 offset:24576\n"
    "v_mov_b32 v117, 0\n"
    "v_mfma_f32_16x16x32_bf16 v[68:71], a[140:143], v[164:167], v[68:71]\n"
    "ds_read_b128 a[100:103], v209 offset:24576\n"
    "v_mov_b32 v118, 0\n"
    "v_mfma_f32_16x16x32_bf16 v[72:75], a[144:147], v[160:163], 0\n"
    "ds_read_b128 a[104:107], v210 offset:24576\n"
    "v_mov_b32 v119, 0\n"
    "v_mfma_f32_16x16x32_bf16 v[72:75], a[148:151], v[164:167], v[72:75]\n"
    "ds_read_b128 a[108:111], v211 offset:24576\n"
    "v_mov_b32 v120, 0\n"
    "v_mfma_f32_16x16x32_bf16 v[76:79], a[152:155], v[160:163], 0\n"
    "ds_read_b128 a[112:115], v208 offset:28672\n"
    "v_mov_b32 v121, 0\n"
    "v_mfma_f32_16x16x32_bf16 v[76:79], a[156:159], v[164:167], v[76:79]\n"
    "ds_read_b128 a[116:119], v209 offset:28672\n"
    "v_mov_b32 v122, 0\n"
    "v_mfma_f32_16x16x32_bf16 v[80:83], a[160:163], v[160:163], 0\n"
    "ds_read_b128 a[120:123], v210 offset:28672\n"
    "v_mov_b32 v123, 0\n"
    "v_mfma_f32_16x16x32_bf16 v[80:83], a[164:167], v[164:167], v[80:83]\n"
    "v_mov_b32 v124, 0\n"
    "v_max3_f32 v250, v144, v145, v146\n"
    "v_mfma_f32_16x16x32_bf16 v[84:87], a[168:171], v[160:163], 0\n"
    "ds_read_b128 a[124:127], v211 offset:28672\n"
    "v_max3_f32 v250, v250, v147, v148\n"
    "v_max3_f32 v250, v250, v149, v150\n"
    "v_max3_f32 v250, v250, v151, v152\n"
    "v_max3_f32 v250, v250, v153, v154\n"
    "v_max3_f32 v250, v250, v155, v156\n"
    "v_max3_f32 v250, v250, v157, v158\n"
    "v_max_f32 v250, v250, v159\n"
    "v_mfma_f32_16x16x32_bf16 v[84:87], a[172:175], v[164:167], v[84:87]\n"
    "v_mov_b32 v125, 0\n"
    "v_mov_b32 v251, v250\n"
    "s_nop 1\n"
    "v_permlane32_swap_b32 v250, v251\n"
    "s_nop 0\n"
    "v_max_f32 v250, v250, v251\n"
    "v_mov_b32 v251, v250\n"
    "s_nop 1\n"
    "v_permlane16_swap_b32 v250, v251\n"
    "s_nop 0\n"
    "v_max_f32 v250, v250, v251\n"
    "v_mfma_f32_16x16x32_bf16 v[88:91], a[176:179], v[160:163], 0\n"
    "v_add_f32 v250, 0x42cc0000, v250\n"
    "v_sub_f32 v144, v144, v250\n"
    "v_sub_f32 v145, v145, v250\n"
    "v_sub_f32 v146, v146, v250\n"
    "v_sub_f32 v147, v147, v250\n"
    "v_sub_f32 v188, 0, v250\n"
    "v_exp_f32 v144, v144\n"
    "v_exp_f32 v145, v145\n"
    "v_mfma_f32_16x16x32_bf16 v[88:91], a[180:183], v[164:167], v[88:91]\n"
    "v_mov_b32 v126, 0\n"
    "v_exp_f32 v146, v146\n"
    "v_exp_f32 v147, v147\n"
    "v_cvt_pk_bf16_f32 v168, v144, v145\n"
    "v_cvt_pk_bf16_f32 v169, v146, v147\n"
    "v_sub_f32 v148, v148, v250\n"
    "v_sub_f32 v149, v149, v250\n"
    "v_sub_f32 v150, v150, v250\n"
    "v_mfma_f32_16x16x32_bf16 v[92:95], a[184:187], v[160:163], 0\n"
    "v_mov_b32 v127, 0\n"
    "v_sub_f32 v151, v151, v250\n"
    "v_sub_f32 v189, 0, v250\n"
    "v_exp_f32 v148, v148\n"
    "v_exp_f32 v149, v149\n"
    "v_exp_f32 v150, v150\n"
    "v_exp_f32 v151, v151\n"
    "v_mfma_f32_16x16x32_bf16 v[92:95], a[188:191], v[164:167], v[92:95]\n"
    "v_cvt_pk_bf16_f32 v170, v148, v149\n"
    "v_cvt_pk_bf16_f32 v171, v150, v151\n"
    "v_sub_f32 v152, v152, v250\n"
    "v_sub_f32 v153, v153, v250\n"
    "v_sub_f32 v154, v154, v250\n"
    "v_sub_f32 v155, v155, v250\n"
    "v_sub_f32 v190, 0, v250\n"
    "v_exp_f32 v152, v152\n"
    "v_exp_f32 v153, v153\n"
    "v_smfmac_f32_16x16x64_bf16 v[200:203], v[220:223], v[160:167], v224\n"
    "v_exp_f32 v154, v154\n"
    "v_exp_f32 v155, v155\n"
    "v_cvt_pk_bf16_f32 v172, v152, v153\n"
    "v_cvt_pk_bf16_f32 v173, v154, v155\n"
    "v_sub_f32 v156, v156, v250\n"
    "v_sub_f32 v157, v157, v250\n"
    "v_sub_f32 v158, v158, v250\n"
    "v_sub_f32 v159, v159, v250\n"
    "v_sub_f32 v191, 0, v250\n"
    "s_waitcnt vmcnt(0) lgkmcnt(0)\n"
    "s_barrier\n"
    "s_branch BODY1%=\n"
    ".p2align 6\n"
    "s_nop 0\n"
    "s_nop 0\n"
    "BODY0%=:\n"
    "v_mfma_f32_16x16x32_bf16 v[128:131], a[64:67], a[0:3], v[176:179]\n"
    "v_exp_f32 v156, v156\n"
    "v_mfma_f32_16x16x32_bf16 v[128:131], a[68:71], a[4:7], v[128:131]\n"
    "s_mov_b32 m0, s74\n"
    "v_mfma_f32_16x16x32_bf16 v[128:131], a[72:75], a[8:11], v[128:131]\n"
    "v_exp_f32 v157, v157\n"
    "v_mfma_f32_16x16x32_bf16 v[128:131], a[76:79], a[12:15], v[128:131]\n"
    "buffer_load_dwordx4 v214, s[40:43], s72 offen offset:0 nt lds\n"
    "v_mfma_f32_16x16x32_bf16 v[132:135], a[80:83], a[0:3], v[176:179]\n"
    "v_exp_f32 v158, v158\n"
    "v_mfma_f32_16x16x32_bf16 v[132:135], a[84:87], a[4:7], v[132:135]\n"
    "v_cvt_pk_bf16_f32 v174, v156, v157\n"
    "v_mfma_f32_16x16x32_bf16 v[132:135], a[88:91], a[8:11], v[132:135]\n"
    "v_exp_f32 v159, v159\n"
    "v_mfma_f32_16x16x32_bf16 v[132:135], a[92:95], a[12:15], v[132:135]\n"
    "v_cvt_pk_bf16_f32 v175, v158, v159\n"
    "v_mfma_f32_16x16x32_bf16 v[136:139], a[96:99], a[0:3], v[176:179]\n"
    "v_exp_f32 v128, v128\n"
    "v_mfma_f32_16x16x32_bf16 v[136:139], a[100:103], a[4:7], v[136:139]\n"
    "v_mfma_f32_16x16x32_bf16 v[136:139], a[104:107], a[8:11], v[136:139]\n"
    "v_exp_f32 v129, v129\n"
    "v_mfma_f32_16x16x32_bf16 v[136:139], a[108:111], a[12:15], v[136:139]\n"
    "buffer_load_dwordx4 v215, s[40:43], s72 offen offset:1024 nt lds\n"
    "v_mfma_f32_16x16x32_bf16 v[140:143], a[112:115], a[0:3], v[176:179]\n"
    "v_exp_f32 v130, v130\n"
    "v_mfma_f32_16x16x32_bf16 v[140:143], a[116:119], a[4:7], v[140:143]\n"
    "v_cvt_pk_bf16_f32 v160, v128, v129\n"
    "v_mfma_f32_16x16x32_bf16 v[140:143], a[120:123], a[8:11], v[140:143]\n"
    "v_exp_f32 v131, v131\n"
    "v_mfma_f32_16x16x32_bf16 v[140:143], a[124:127], a[12:15], v[140:143]\n"
    "v_cvt_pk_bf16_f32 v161, v130, v131\n"
    "v_mfma_f32_16x16x32_bf16 v[96:99], a[192:195], v[168:171], v[96:99]\n"
    "v_exp_f32 v132, v132\n"
    "v_mfma_f32_16x16x32_bf16 v[96:99], a[196:199], v[172:175], v[96:99]\n"
    "buffer_load_dwordx4 v216, s[40:43], s72 offen offset:2048 nt lds\n"
    "v_mfma_f32_16x16x32_bf16 v[100:103], a[200:203], v[168:171], v[100:103]\n"
    "v_exp_f32 v133, v133\n"
    "v_mfma_f32_16x16x32_bf16 v[100:103], a[204:207], v[172:175], v[100:103]\n"
    "v_cvt_pk_bf16_f32 v162, v132, v133\n"
    "v_mfma_f32_16x16x32_bf16 v[104:107], a[208:211], v[168:171], v[104:107]\n"
    "v_exp_f32 v134, v134\n"
    "v_mfma_f32_16x16x32_bf16 v[104:107], a[212:215], v[172:175], v[104:107]\n"
    "v_mfma_f32_16x16x32_bf16 v[108:111], a[216:219], v[168:171], v[108:111]\n"
    "v_exp_f32 v135, v135\n"
    "v_mfma_f32_16x16x32_bf16 v[108:111], a[220:223], v[172:175], v[108:111]\n"
    "v_cvt_pk_bf16_f32 v163, v134, v135\n"
    "v_mfma_f32_16x16x32_bf16 v[112:115], a[224:227], v[168:171], v[112:115]\n"
    "v_exp_f32 v136, v136\n"
    "v_mfma_f32_16x16x32_bf16 v[112:115], a[228:231], v[172:175], v[112:115]\n"
    "buffer_load_dwordx4 v217, s[40:43], s72 offen offset:3072 nt lds\n"
    "v_mfma_f32_16x16x32_bf16 v[116:119], a[232:235], v[168:171], v[116:119]\n"
    "v_exp_f32 v137, v137\n"
    "v_mfma_f32_16x16x32_bf16 v[116:119], a[236:239], v[172:175], v[116:119]\n"
    "v_cvt_pk_bf16_f32 v164, v136, v137\n"
    "v_mfma_f32_16x16x32_bf16 v[120:123], a[240:243], v[168:171], v[120:123]\n"
    "v_exp_f32 v138, v138\n"
    "v_mfma_f32_16x16x32_bf16 v[120:123], a[244:247], v[172:175], v[120:123]\n"
    "s_mov_b32 m0, s76\n"
    "v_mfma_f32_16x16x32_bf16 v[124:127], a[248:251], v[168:171], v[124:127]\n"
    "v_exp_f32 v139, v139\n"
    "v_mfma_f32_16x16x32_bf16 v[124:127], a[252:255], v[172:175], v[124:127]\n"
    "buffer_load_dwordx4 v218, s[44:47], s72 offen offset:0 nt lds\n"
    "v_smfmac_f32_16x16x64_bf16 v[204:207], v[220:223], v[168:175], v224\n"
    "v_exp_f32 v140, v140\n"
    "v_mfma_f32_16x16x32_bf16 v[144:147], a[64:67], a[16:19], v[180:183]\n"
    "ds_read_b64_tr_b16 a[192:193], v212 offset:0\n"
    "v_cvt_pk_bf16_f32 v165, v138, v139\n"
    "v_mfma_f32_16x16x32_bf16 v[144:147], a[68:71], a[20:23], v[144:147]\n"
    "v_exp_f32 v141, v141\n"
    "v_mfma_f32_16x16x32_bf16 v[144:147], a[72:75], a[24:27], v[144:147]\n"
    "ds_read_b64_tr_b16 a[194:195], v212 offset:4096\n"
    "v_cvt_pk_bf16_f32 v166, v140, v141\n"
    "v_mfma_f32_16x16x32_bf16 v[144:147], a[76:79], a[28:31], v[144:147]\n"
    "v_exp_f32 v142, v142\n"
    "v_mfma_f32_16x16x32_bf16 v[148:151], a[80:83], a[16:19], v[180:183]\n"
    "ds_read_b64_tr_b16 a[196:197], v212 offset:8192\n"
    "v_mfma_f32_16x16x32_bf16 v[148:151], a[84:87], a[20:23], v[148:151]\n"
    "v_exp_f32 v143, v143\n"
    "v_mfma_f32_16x16x32_bf16 v[148:151], a[88:91], a[24:27], v[148:151]\n"
    "buffer_load_dwordx4 v219, s[44:47], s72 offen offset:1024 nt lds\n"
    "v_mfma_f32_16x16x32_bf16 v[148:151], a[92:95], a[28:31], v[148:151]\n"
    "ds_read_b64_tr_b16 a[198:199], v212 offset:12288\n"
    "v_cvt_pk_bf16_f32 v167, v142, v143\n"
    "v_mfma_f32_16x16x32_bf16 v[152:155], a[96:99], a[16:19], v[180:183]\n"
    "v_exp_f32 v144, v144\n"
    "v_mfma_f32_16x16x32_bf16 v[152:155], a[100:103], a[20:23], v[152:155]\n"
    "ds_read_b64_tr_b16 a[200:201], v213 offset:0\n"
    "v_mfma_f32_16x16x32_bf16 v[152:155], a[104:107], a[24:27], v[152:155]\n"
    "v_exp_f32 v145, v145\n"
    "v_mfma_f32_16x16x32_bf16 v[152:155], a[108:111], a[28:31], v[152:155]\n"
    "ds_read_b64_tr_b16 a[202:203], v213 offset:4096\n"
    "v_cvt_pk_bf16_f32 v168, v144, v145\n"
    "v_mfma_f32_16x16x32_bf16 v[156:159], a[112:115], a[16:19], v[180:183]\n"
    "v_exp_f32 v146, v146\n"
    "v_mfma_f32_16x16x32_bf16 v[156:159], a[116:119], a[20:23], v[156:159]\n"
    "buffer_load_dwordx4 v218, s[44:47], s72 offen offset:2048 nt lds\n"
    "v_mfma_f32_16x16x32_bf16 v[156:159], a[120:123], a[24:27], v[156:159]\n"
    "v_exp_f32 v147, v147\n"
    "v_mfma_f32_16x16x32_bf16 v[156:159], a[124:127], a[28:31], v[156:159]\n"
    "ds_read_b64_tr_b16 a[204:205], v213 offset:8192\n"
    "v_cvt_pk_bf16_f32 v169, v146, v147\n"
    "v_mfma_f32_16x16x32_bf16 v[0:3], a[128:131], v[160:163], v[0:3]\n"
    "v_exp_f32 v148, v148\n"
    "v_mfma_f32_16x16x32_bf16 v[0:3], a[132:135], v[164:167], v[0:3]\n"
    "ds_read_b64_tr_b16 a[206:207], v213 offset:12288\n"
    "ds_read_b64_tr_b16 a[208:209], v212 offset:256\n"
    "v_mfma_f32_16x16x32_bf16 v[4:7], a[136:139], v[160:163], v[4:7]\n"
    "v_exp_f32 v149, v149\n"
    "v_mfma_f32_16x16x32_bf16 v[4:7], a[140:143], v[164:167], v[4:7]\n"
    "buffer_load_dwordx4 v219, s[44:47], s72 offen offset:3072 nt lds\n"
    "v_mfma_f32_16x16x32_bf16 v[8:11], a[144:147], v[160:163], v[8:11]\n"
    "v_exp_f32 v150, v150\n"
    "v_mfma_f32_16x16x32_bf16 v[8:11], a[148:151], v[164:167], v[8:11]\n"
    "ds_read_b64_tr_b16 a[210:211], v212 offset:4352\n"
    "v_cvt_pk_bf16_f32 v170, v148, v149\n"
    "v_mfma_f32_16x16x32_bf16 v[12:15], a[152:155], v[160:163], v[12:15]\n"
    "v_exp_f32 v151, v151\n"
    "v_mfma_f32_16x16x32_bf16 v[12:15], a[156:159], v[164:167], v[12:15]\n"
    "ds_read_b64_tr_b16 a[212:213], v212 offset:8448\n"
    "v_cvt_pk_bf16_f32 v171, v150, v151\n"
    "v_mfma_f32_16x16x32_bf16 v[16:19], a[160:163], v[160:163], v[16:19]\n"
    "v_exp_f32 v152, v152\n"
    "v_mfma_f32_16x16x32_bf16 v[16:19], a[164:167], v[164:167], v[16:19]\n"
    "ds_read_b64_tr_b16 a[214:215], v212 offset:12544\n"
    "s_mov_b32 m0, s73\n"
    "v_mfma_f32_16x16x32_bf16 v[20:23], a[168:171], v[160:163], v[20:23]\n"
    "v_exp_f32 v153, v153\n"
    "v_mfma_f32_16x16x32_bf16 v[20:23], a[172:175], v[164:167], v[20:23]\n"
    "buffer_load_dwordx4 v227, s[68:71], s92 offen lds\n"
    "v_mfma_f32_16x16x32_bf16 v[24:27], a[176:179], v[160:163], v[24:27]\n"
    "v_exp_f32 v154, v154\n"
    "v_mfma_f32_16x16x32_bf16 v[24:27], a[180:183], v[164:167], v[24:27]\n"
    "ds_read_b64_tr_b16 a[216:217], v213 offset:256\n"
    "v_cvt_pk_bf16_f32 v172, v152, v153\n"
    "v_mfma_f32_16x16x32_bf16 v[28:31], a[184:187], v[160:163], v[28:31]\n"
    "v_exp_f32 v155, v155\n"
    "v_mfma_f32_16x16x32_bf16 v[28:31], a[188:191], v[164:167], v[28:31]\n"
    "ds_read_b64_tr_b16 a[218:219], v213 offset:4352\n"
    "v_cvt_pk_bf16_f32 v173, v154, v155\n"
    "v_smfmac_f32_16x16x64_bf16 v[192:195], v[220:223], v[160:167], v224\n"
    "v_exp_f32 v156, v156\n"
    "v_mfma_f32_16x16x32_bf16 v[128:131], a[64:67], a[32:35], v[184:187]\n"
    "buffer_load_dword v254, v228, s[48:51], s72 offen\n"
    "v_mfma_f32_16x16x32_bf16 v[128:131], a[68:71], a[36:39], v[128:131]\n"
    "v_exp_f32 v157, v157\n"
    "v_mfma_f32_16x16x32_bf16 v[128:131], a[72:75], a[40:43], v[128:131]\n"
    "ds_read_b64_tr_b16 a[220:221], v213 offset:8448\n"
    "v_cvt_pk_bf16_f32 v174, v156, v157\n"
    "v_mfma_f32_16x16x32_bf16 v[128:131], a[76:79], a[44:47], v[128:131]\n"
    "v_exp_f32 v158, v158\n"
    "v_mfma_f32_16x16x32_bf16 v[132:135], a[80:83], a[32:35], v[184:187]\n"
    "ds_read_b64_tr_b16 a[222:223], v213 offset:12544\n"
    "ds_read_b64_tr_b16 a[224:225], v212 offset:512\n"
    "v_mfma_f32_16x16x32_bf16 v[132:135], a[84:87], a[36:39], v[132:135]\n"
    "v_exp_f32 v159, v159\n"
    "v_mfma_f32_16x16x32_bf16 v[132:135], a[88:91], a[40:43], v[132:135]\n"
    "ds_read_b64_tr_b16 a[226:227], v212 offset:4608\n"
    "v_cvt_pk_bf16_f32 v175, v158, v159\n"
    "v_mfma_f32_16x16x32_bf16 v[132:135], a[92:95], a[44:47], v[132:135]\n"
    "ds_read_b64_tr_b16 a[228:229], v212 offset:8704\n"
    "ds_read_b64_tr_b16 a[230:231], v212 offset:12800\n"
    "v_mfma_f32_16x16x32_bf16 v[136:139], a[96:99], a[32:35], v[184:187]\n"
    "v_exp_f32 v128, v128\n"
    "v_mfma_f32_16x16x32_bf16 v[136:139], a[100:103], a[36:39], v[136:139]\n"
    "ds_read_b64_tr_b16 a[232:233], v213 offset:512\n"
    "v_mfma_f32_16x16x32_bf16 v[136:139], a[104:107], a[40:43], v[136:139]\n"
    "v_exp_f32 v129, v129\n"
    "v_mfma_f32_16x16x32_bf16 v[136:139], a[108:111], a[44:47], v[136:139]\n"
    "ds_read_b64_tr_b16 a[234:235], v213 offset:4608\n"
    "v_cvt_pk_bf16_f32 v160, v128, v129\n"
    "v_mfma_f32_16x16x32_bf16 v[140:143], a[112:115], a[32:35], v[184:187]\n"
    "v_exp_f32 v130, v130\n"
    "v_mfma_f32_16x16x32_bf16 v[140:143], a[116:119], a[36:39], v[140:143]\n"
    "ds_read_b64_tr_b16 a[236:237], v213 offset:8704\n"
    "v_mfma_f32_16x16x32_bf16 v[140:143], a[120:123], a[40:43], v[140:143]\n"
    "v_exp_f32 v131, v131\n"
    "v_mfma_f32_16x16x32_bf16 v[140:143], a[124:127], a[44:47], v[140:143]\n"
    "ds_read_b64_tr_b16 a[238:239], v213 offset:12800\n"
    "v_cvt_pk_bf16_f32 v161, v130, v131\n"
    "v_mfma_f32_16x16x32_bf16 v[32:35], a[128:131], v[168:171], v[32:35]\n"
    "v_exp_f32 v132, v132\n"
    "v_mfma_f32_16x16x32_bf16 v[32:35], a[132:135], v[172:175], v[32:35]\n"
    "ds_read_b64_tr_b16 a[240:241], v212 offset:768\n"
    "v_mfma_f32_16x16x32_bf16 v[36:39], a[136:139], v[168:171], v[36:39]\n"
    "v_exp_f32 v133, v133\n"
    "v_mfma_f32_16x16x32_bf16 v[36:39], a[140:143], v[172:175], v[36:39]\n"
    "ds_read_b64_tr_b16 a[242:243], v212 offset:4864\n"
    "v_cvt_pk_bf16_f32 v162, v132, v133\n"
    "v_mfma_f32_16x16x32_bf16 v[40:43], a[144:147], v[168:171], v[40:43]\n"
    "v_exp_f32 v134, v134\n"
    "v_mfma_f32_16x16x32_bf16 v[40:43], a[148:151], v[172:175], v[40:43]\n"
    "ds_read_b64_tr_b16 a[244:245], v212 offset:8960\n"
    "v_mfma_f32_16x16x32_bf16 v[44:47], a[152:155], v[168:171], v[44:47]\n"
    "v_exp_f32 v135, v135\n"
    "v_mfma_f32_16x16x32_bf16 v[44:47], a[156:159], v[172:175], v[44:47]\n"
    "ds_read_b64_tr_b16 a[246:247], v212 offset:13056\n"
    "v_cvt_pk_bf16_f32 v163, v134, v135\n"
    "v_mfma_f32_16x16x32_bf16 v[48:51], a[160:163], v[168:171], v[48:51]\n"
    "v_exp_f32 v136, v136\n"
    "v_mfma_f32_16x16x32_bf16 v[48:51], a[164:167], v[172:175], v[48:51]\n"
    "ds_read_b64_tr_b16 a[248:249], v213 offset:768\n"
    "v_mfma_f32_16x16x32_bf16 v[52:55], a[168:171], v[168:171], v[52:55]\n"
    "v_exp_f32 v137, v137\n"
    "v_mfma_f32_16x16x32_bf16 v[52:55], a[172:175], v[172:175], v[52:55]\n"
    "ds_read_b64_tr_b16 a[250:251], v213 offset:4864\n"
    "v_cvt_pk_bf16_f32 v164, v136, v137\n"
    "v_mfma_f32_16x16x32_bf16 v[56:59], a[176:179], v[168:171], v[56:59]\n"
    "v_exp_f32 v138, v138\n"
    "v_mfma_f32_16x16x32_bf16 v[56:59], a[180:183], v[172:175], v[56:59]\n"
    "ds_read_b64_tr_b16 a[252:253], v213 offset:8960\n"
    "v_mfma_f32_16x16x32_bf16 v[60:63], a[184:187], v[168:171], v[60:63]\n"
    "v_exp_f32 v139, v139\n"
    "v_mfma_f32_16x16x32_bf16 v[60:63], a[188:191], v[172:175], v[60:63]\n"
    "ds_read_b64_tr_b16 a[254:255], v213 offset:13056\n"
    "v_cvt_pk_bf16_f32 v165, v138, v139\n"
    "v_smfmac_f32_16x16x64_bf16 v[196:199], v[220:223], v[168:175], v224\n"
    "v_exp_f32 v140, v140\n"
    "v_mfma_f32_16x16x32_bf16 v[144:147], a[64:67], a[48:51], v[188:191]\n"
    "v_mfma_f32_16x16x32_bf16 v[144:147], a[68:71], a[52:55], v[144:147]\n"
    "v_exp_f32 v141, v141\n"
    "v_mfma_f32_16x16x32_bf16 v[144:147], a[72:75], a[56:59], v[144:147]\n"
    "ds_read_b128 a[64:67], v208 offset:16384\n"
    "v_cvt_pk_bf16_f32 v166, v140, v141\n"
    "v_mfma_f32_16x16x32_bf16 v[144:147], a[76:79], a[60:63], v[144:147]\n"
    "v_exp_f32 v142, v142\n"
    "v_mfma_f32_16x16x32_bf16 v[148:151], a[80:83], a[48:51], v[188:191]\n"
    "ds_read_b128 a[68:71], v209 offset:16384\n"
    "ds_read_b128 a[72:75], v210 offset:16384\n"
    "v_mfma_f32_16x16x32_bf16 v[148:151], a[84:87], a[52:55], v[148:151]\n"
    "v_exp_f32 v143, v143\n"
    "v_mfma_f32_16x16x32_bf16 v[148:151], a[88:91], a[56:59], v[148:151]\n"
    "ds_read_b128 a[76:79], v211 offset:16384\n"
    "v_cvt_pk_bf16_f32 v167, v142, v143\n"
    "v_mfma_f32_16x16x32_bf16 v[148:151], a[92:95], a[60:63], v[148:151]\n"
    "ds_read_b128 a[80:83], v208 offset:20480\n"
    "ds_read_b128 a[84:87], v209 offset:20480\n"
    "v_mfma_f32_16x16x32_bf16 v[152:155], a[96:99], a[48:51], v[188:191]\n"
    "v_exp_f32 v144, v144\n"
    "v_mfma_f32_16x16x32_bf16 v[152:155], a[100:103], a[52:55], v[152:155]\n"
    "ds_read_b128 a[88:91], v210 offset:20480\n"
    "ds_read_b128 a[92:95], v211 offset:20480\n"
    "v_mfma_f32_16x16x32_bf16 v[152:155], a[104:107], a[56:59], v[152:155]\n"
    "v_exp_f32 v145, v145\n"
    "v_mfma_f32_16x16x32_bf16 v[152:155], a[108:111], a[60:63], v[152:155]\n"
    "ds_read_b128 a[96:99], v208 offset:24576\n"
    "v_cvt_pk_bf16_f32 v168, v144, v145\n"
    "v_mfma_f32_16x16x32_bf16 v[156:159], a[112:115], a[48:51], v[188:191]\n"
    "v_exp_f32 v146, v146\n"
    "v_mfma_f32_16x16x32_bf16 v[156:159], a[116:119], a[52:55], v[156:159]\n"
    "ds_read_b128 a[100:103], v209 offset:24576\n"
    "ds_read_b128 a[104:107], v210 offset:24576\n"
    "v_mfma_f32_16x16x32_bf16 v[156:159], a[120:123], a[56:59], v[156:159]\n"
    "v_exp_f32 v147, v147\n"
    "v_mfma_f32_16x16x32_bf16 v[156:159], a[124:127], a[60:63], v[156:159]\n"
    "ds_read_b128 a[108:111], v211 offset:24576\n"
    "v_cvt_pk_bf16_f32 v169, v146, v147\n"
    "v_mfma_f32_16x16x32_bf16 v[64:67], a[128:131], v[160:163], v[64:67]\n"
    "v_exp_f32 v148, v148\n"
    "v_mfma_f32_16x16x32_bf16 v[64:67], a[132:135], v[164:167], v[64:67]\n"
    "ds_read_b128 a[112:115], v208 offset:28672\n"
    "ds_read_b128 a[116:119], v209 offset:28672\n"
    "v_mfma_f32_16x16x32_bf16 v[68:71], a[136:139], v[160:163], v[68:71]\n"
    "v_exp_f32 v149, v149\n"
    "v_mfma_f32_16x16x32_bf16 v[68:71], a[140:143], v[164:167], v[68:71]\n"
    "ds_read_b128 a[120:123], v210 offset:28672\n"
    "v_cvt_pk_bf16_f32 v170, v148, v149\n"
    "v_mfma_f32_16x16x32_bf16 v[72:75], a[144:147], v[160:163], v[72:75]\n"
    "v_exp_f32 v150, v150\n"
    "v_mfma_f32_16x16x32_bf16 v[72:75], a[148:151], v[164:167], v[72:75]\n"
    "ds_read_b128 a[124:127], v211 offset:28672\n"
    "s_add_u32 s72, s72, 0x4000\n"
    "v_mfma_f32_16x16x32_bf16 v[76:79], a[152:155], v[160:163], v[76:79]\n"
    "v_exp_f32 v151, v151\n"
    "v_mfma_f32_16x16x32_bf16 v[76:79], a[156:159], v[164:167], v[76:79]\n"
    "v_cvt_pk_bf16_f32 v171, v150, v151\n"
    "s_add_u32 s73, s73, s37\n"
    "v_mfma_f32_16x16x32_bf16 v[80:83], a[160:163], v[160:163], v[80:83]\n"
    "v_exp_f32 v152, v152\n"
    "v_mfma_f32_16x16x32_bf16 v[80:83], a[164:167], v[164:167], v[80:83]\n"
    "s_add_u32 s92, s92, s36\n"
    "s_cmp_eq_u32 s72, s66\n"
    "s_cbranch_scc1 EV0%=\n"
    "NOSW%=:\n"
    "v_mfma_f32_16x16x32_bf16 v[84:87], a[168:171], v[160:163], v[84:87]\n"
    "v_exp_f32 v153, v153\n"
    "v_mfma_f32_16x16x32_bf16 v[84:87], a[172:175], v[164:167], v[84:87]\n"
    "v_cvt_pk_bf16_f32 v172, v152, v153\n"
    "v_mfma_f32_16x16x32_bf16 v[88:91], a[176:179], v[160:163], v[88:91]\n"
    "v_exp_f32 v154, v154\n"
    "v_mfma_f32_16x16x32_bf16 v[88:91], a[180:183], v[164:167], v[88:91]\n"
    "s_waitcnt vmcnt(2) lgkmcnt(0)\n"
    "s_barrier\n"
    "v_mfma_f32_16x16x32_bf16 v[92:95], a[184:187], v[160:163], v[92:95]\n"
    "v_exp_f32 v155, v155\n"
    "v_mfma_f32_16x16x32_bf16 v[92:95], a[188:191], v[164:167], v[92:95]\n"
    "v_cvt_pk_bf16_f32 v173, v154, v155\n"
    "v_smfmac_f32_16x16x64_bf16 v[200:203], v[220:223], v[160:167], v224\n"
    "s_cbranch_scc1 SPEC%=\n"
    "BODY1%=:\n"
    "v_mfma_f32_16x16x32_bf16 v[128:131], a[64:67], a[0:3], v[176:179]\n"
    "v_exp_f32 v156, v156\n"
    "v_mfma_f32_16x16x32_bf16 v[128:131], a[68:71], a[4:7], v[128:131]\n"
    "s_mov_b32 m0, s75\n"
    "v_mfma_f32_16x16x32_bf16 v[128:131], a[72:75], a[8:11], v[128:131]\n"
    "v_exp_f32 v157, v157\n"
    "v_mfma_f32_16x16x32_bf16 v[128:131], a[76:79], a[12:15], v[128:131]\n"
    "buffer_load_dwordx4 v214, s[40:43], s72 offen offset:0 nt lds\n"
    "v_mfma_f32_16x16x32_bf16 v[132:135], a[80:83], a[0:3], v[176:179]\n"
    "v_exp_f32 v158, v158\n"
    "v_mfma_f32_16x16x32_bf16 v[132:135], a[84:87], a[4:7], v[132:135]\n"
    "v_cvt_pk_bf16_f32 v174, v156, v157\n"
    "v_mfma_f32_16x16x32_bf16 v[132:135], a[88:91], a[8:11], v[132:135]\n"
    "v_exp_f32 v159, v159\n"
    "v_mfma_f32_16x16x32_bf16 v[132:135], a[92:95], a[12:15], v[132:135]\n"
    "v_cvt_pk_bf16_f32 v175, v158, v159\n"
    "v_mfma_f32_16x16x32_bf16 v[136:139], a[96:99], a[0:3], v[176:179]\n"
    "v_exp_f32 v128, v128\n"
    "v_mfma_f32_16x16x32_bf16 v[136:139], a[100:103], a[4:7], v[136:139]\n"
    "v_mfma_f32_16x16x32_bf16 v[136:139], a[104:107], a[8:11], v[136:139]\n"
    "v_exp_f32 v129, v129\n"
    "v_mfma_f32_16x16x32_bf16 v[136:139], a[108:111], a[12:15], v[136:139]\n"
    "buffer_load_dwordx4 v215, s[40:43], s72 offen offset:1024 nt lds\n"
    "v_mfma_f32_16x16x32_bf16 v[140:143], a[112:115], a[0:3], v[176:179]\n"
    "v_exp_f32 v130, v130\n"
    "v_mfma_f32_16x16x32_bf16 v[140:143], a[116:119], a[4:7], v[140:143]\n"
    "v_cvt_pk_bf16_f32 v160, v128, v129\n"
    "v_mfma_f32_16x16x32_bf16 v[140:143], a[120:123], a[8:11], v[140:143]\n"
    "v_exp_f32 v131, v131\n"
    "v_mfma_f32_16x16x32_bf16 v[140:143], a[124:127], a[12:15], v[140:143]\n"
    "v_cvt_pk_bf16_f32 v161, v130, v131\n"
    "v_mfma_f32_16x16x32_bf16 v[96:99], a[128:131], v[168:171], v[96:99]\n"
    "v_exp_f32 v132, v132\n"
    "v_mfma_f32_16x16x32_bf16 v[96:99], a[132:135], v[172:175], v[96:99]\n"
    "buffer_load_dwordx4 v216, s[40:43], s72 offen offset:2048 nt lds\n"
    "v_mfma_f32_16x16x32_bf16 v[100:103], a[136:139], v[168:171], v[100:103]\n"
    "v_exp_f32 v133, v133\n"
    "v_mfma_f32_16x16x32_bf16 v[100:103], a[140:143], v[172:175], v[100:103]\n"
    "v_cvt_pk_bf16_f32 v162, v132, v133\n"
    "v_mfma_f32_16x16x32_bf16 v[104:107], a[144:147], v[168:171], v[104:107]\n"
    "v_exp_f32 v134, v134\n"
    "v_mfma_f32_16x16x32_bf16 v[104:107], a[148:151], v[172:175], v[104:107]\n"
    "v_mfma_f32_16x16x32_bf16 v[108:111], a[152:155], v[168:171], v[108:111]\n"
    "v_exp_f32 v135, v135\n"
    "v_mfma_f32_16x16x32_bf16 v[108:111], a[156:159], v[172:175], v[108:111]\n"
    "v_cvt_pk_bf16_f32 v163, v134, v135\n"
    "v_mfma_f32_16x16x32_bf16 v[112:115], a[160:163], v[168:171], v[112:115]\n"
    "v_exp_f32 v136, v136\n"
    "v_mfma_f32_16x16x32_bf16 v[112:115], a[164:167], v[172:175], v[112:115]\n"
    "buffer_load_dwordx4 v217, s[40:43], s72 offen offset:3072 nt lds\n"
    "v_mfma_f32_16x16x32_bf16 v[116:119], a[168:171], v[168:171], v[116:119]\n"
    "v_exp_f32 v137, v137\n"
    "v_mfma_f32_16x16x32_bf16 v[116:119], a[172:175], v[172:175], v[116:119]\n"
    "v_cvt_pk_bf16_f32 v164, v136, v137\n"
    "v_mfma_f32_16x16x32_bf16 v[120:123], a[176:179], v[168:171], v[120:123]\n"
    "v_exp_f32 v138, v138\n"
    "v_mfma_f32_16x16x32_bf16 v[120:123], a[180:183], v[172:175], v[120:123]\n"
    "s_mov_b32 m0, s77\n"
    "v_mfma_f32_16x16x32_bf16 v[124:127], a[184:187], v[168:171], v[124:127]\n"
    "v_exp_f32 v139, v139\n"
    "v_mfma_f32_16x16x32_bf16 v[124:127], a[188:191], v[172:175], v[124:127]\n"
    "buffer_load_dwordx4 v218, s[44:47], s72 offen offset:0 nt lds\n"
    "v_smfmac_f32_16x16x64_bf16 v[204:207], v[220:223], v[168:175], v224\n"
    "v_exp_f32 v140, v140\n"
    "v_mfma_f32_16x16x32_bf16 v[144:147], a[64:67], a[16:19], v[180:183]\n"
    "ds_read_b64_tr_b16 a[128:129], v212 offset:16384\n"
    "v_cvt_pk_bf16_f32 v165, v138, v139\n"
    "v_mfma_f32_16x16x32_bf16 v[144:147], a[68:71], a[20:23], v[144:147]\n"
    "v_exp_f32 v141, v141\n"
    "v_mfma_f32_16x16x32_bf16 v[144:147], a[72:75], a[24:27], v[144:147]\n"
    "ds_read_b64_tr_b16 a[130:131], v212 offset:20480\n"
    "v_cvt_pk_bf16_f32 v166, v140, v141\n"
    "v_mfma_f32_16x16x32_bf16 v[144:147], a[76:79], a[28:31], v[144:147]\n"
    "v_exp_f32 v142, v142\n"
    "v_mfma_f32_16x16x32_bf16 v[148:151], a[80:83], a[16:19], v[180:183]\n"
    "ds_read_b64_tr_b16 a[132:133], v212 offset:24576\n"
    "v_mfma_f32_16x16x32_bf16 v[148:151], a[84:87], a[20:23], v[148:151]\n"
    "v_exp_f32 v143, v143\n"
    "v_mfma_f32_16x16x32_bf16 v[148:151], a[88:91], a[24:27], v[148:151]\n"
    "buffer_load_dwordx4 v219, s[44:47], s72 offen offset:1024 nt lds\n"
    "v_mfma_f32_16x16x32_bf16 v[148:151], a[92:95], a[28:31], v[148:151]\n"
    "ds_read_b64_tr_b16 a[134:135], v212 offset:28672\n"
    "v_cvt_pk_bf16_f32 v167, v142, v143\n"
    "v_mfma_f32_16x16x32_bf16 v[152:155], a[96:99], a[16:19], v[180:183]\n"
    "v_exp_f32 v144, v144\n"
    "v_mfma_f32_16x16x32_bf16 v[152:155], a[100:103], a[20:23], v[152:155]\n"
    "ds_read_b64_tr_b16 a[136:137], v213 offset:16384\n"
    "v_mfma_f32_16x16x32_bf16 v[152:155], a[104:107], a[24:27], v[152:155]\n"
    "v_exp_f32 v145, v145\n"
    "v_mfma_f32_16x16x32_bf16 v[152:155], a[108:111], a[28:31], v[152:155]\n"
    "ds_read_b64_tr_b16 a[138:139], v213 offset:20480\n"
    "v_cvt_pk_bf16_f32 v168, v144, v145\n"
    "v_mfma_f32_16x16x32_bf16 v[156:159], a[112:115], a[16:19], v[180:183]\n"
    "v_exp_f32 v146, v146\n"
    "v_mfma_f32_16x16x32_bf16 v[156:159], a[116:119], a[20:23], v[156:159]\n"
    "buffer_load_dwordx4 v218, s[44:47], s72 offen offset:2048 nt lds\n"
    "v_mfma_f32_16x16x32_bf16 v[156:159], a[120:123], a[24:27], v[156:159]\n"
    "v_exp_f32 v147, v147\n"
    "v_mfma_f32_16x16x32_bf16 v[156:159], a[124:127], a[28:31], v[156:159]\n"
    "ds_read_b64_tr_b16 a[140:141], v213 offset:24576\n"
    "v_cvt_pk_bf16_f32 v169, v146, v147\n"
    "v_mfma_f32_16x16x32_bf16 v[0:3], a[192:195], v[160:163], v[0:3]\n"
    "v_exp_f32 v148, v148\n"
    "v_mfma_f32_16x16x32_bf16 v[0:3], a[196:199], v[164:167], v[0:3]\n"
    "ds_read_b64_tr_b16 a[142:143], v213 offset:28672\n"
    "ds_read_b64_tr_b16 a[144:145], v212 offset:16640\n"
    "v_mfma_f32_16x16x32_bf16 v[4:7], a[200:203], v[160:163], v[4:7]\n"
    "v_exp_f32 v149, v149\n"
    "v_mfma_f32_16x16x32_bf16 v[4:7], a[204:207], v[164:167], v[4:7]\n"
    "buffer_load_dwordx4 v219, s[44:47], s72 offen offset:3072 nt lds\n"
    "v_mfma_f32_16x16x32_bf16 v[8:11], a[208:211], v[160:163], v[8:11]\n"
    "v_exp_f32 v150, v150\n"
    "v_mfma_f32_16x16x32_bf16 v[8:11], a[212:215], v[164:167], v[8:11]\n"
    "ds_read_b64_tr_b16 a[146:147], v212 offset:20736\n"
    "v_cvt_pk_bf16_f32 v170, v148, v149\n"
    "v_mfma_f32_16x16x32_bf16 v[12:15], a[216:219], v[160:163], v[12:15]\n"
    "v_exp_f32 v151, v151\n"
    "v_mfma_f32_16x16x32_bf16 v[12:15], a[220:223], v[164:167], v[12:15]\n"
    "ds_read_b64_tr_b16 a[148:149], v212 offset:24832\n"
    "v_cvt_pk_bf16_f32 v171, v150, v151\n"
    "v_mfma_f32_16x16x32_bf16 v[16:19], a[224:227], v[160:163], v[16:19]\n"
    "v_exp_f32 v152, v152\n"
    "v_mfma_f32_16x16x32_bf16 v[16:19], a[228:231], v[164:167], v[16:19]\n"
    "ds_read_b64_tr_b16 a[150:151], v212 offset:28928\n"
    "s_mov_b32 m0, s73\n"
    "v_mfma_f32_16x16x32_bf16 v[20:23], a[232:235], v[160:163], v[20:23]\n"
    "v_exp_f32 v153, v153\n"
    "v_mfma_f32_16x16x32_bf16 v[20:23], a[236:239], v[164:167], v[20:23]\n"
    "buffer_load_dwordx4 v227, s[68:71], s92 offen lds\n"
    "v_mfma_f32_16x16x32_bf16 v[24:27], a[240:243], v[160:163], v[24:27]\n"
    "v_exp_f32 v154, v154\n"
    "v_mfma_f32_16x16x32_bf16 v[24:27], a[244:247], v[164:167], v[24:27]\n"
    "ds_read_b64_tr_b16 a[152:153], v213 offset:16640\n"
    "v_cvt_pk_bf16_f32 v172, v152, v153\n"
    "v_mfma_f32_16x16x32_bf16 v[28:31], a[248:251], v[160:163], v[28:31]\n"
    "v_exp_f32 v155, v155\n"
    "v_mfma_f32_16x16x32_bf16 v[28:31], a[252:255], v[164:167], v[28:31]\n"
    "ds_read_b64_tr_b16 a[154:155], v213 offset:20736\n"
    "v_cvt_pk_bf16_f32 v173, v154, v155\n"
    "v_smfmac_f32_16x16x64_bf16 v[192:195], v[220:223], v[160:167], v224\n"
    "v_exp_f32 v156, v156\n"
    "v_mfma_f32_16x16x32_bf16 v[128:131], a[64:67], a[32:35], v[184:187]\n"
    "buffer_load_dword v254, v228, s[48:51], s72 offen\n"
    "v_mfma_f32_16x16x32_bf16 v[128:131], a[68:71], a[36:39], v[128:131]\n"
    "v_exp_f32 v157, v157\n"
    "v_mfma_f32_16x16x32_bf16 v[128:131], a[72:75], a[40:43], v[128:131]\n"
    "ds_read_b64_tr_b16 a[156:157], v213 offset:24832\n"
    "v_cvt_pk_bf16_f32 v174, v156, v157\n"
    "v_mfma_f32_16x16x32_bf16 v[128:131], a[76:79], a[44:47], v[128:131]\n"
    "v_exp_f32 v158, v158\n"
    "v_mfma_f32_16x16x32_bf16 v[132:135], a[80:83], a[32:35], v[184:187]\n"
    "ds_read_b64_tr_b16 a[158:159], v213 offset:28928\n"
    "ds_read_b64_tr_b16 a[160:161], v212 offset:16896\n"
    "v_mfma_f32_16x16x32_bf16 v[132:135], a[84:87], a[36:39], v[132:135]\n"
    "v_exp_f32 v159, v159\n"
    "v_mfma_f32_16x16x32_bf16 v[132:135], a[88:91], a[40:43], v[132:135]\n"
    "ds_read_b64_tr_b16 a[162:163], v212 offset:20992\n"
    "v_cvt_pk_bf16_f32 v175, v158, v159\n"
    "v_mfma_f32_16x16x32_bf16 v[132:135], a[92:95], a[44:47], v[132:135]\n"
    "ds_read_b64_tr_b16 a[164:165], v212 offset:25088\n"
    "ds_read_b64_tr_b16 a[166:167], v212 offset:29184\n"
    "v_mfma_f32_16x16x32_bf16 v[136:139], a[96:99], a[32:35], v[184:187]\n"
    "v_exp_f32 v128, v128\n"
    "v_mfma_f32_16x16x32_bf16 v[136:139], a[100:103], a[36:39], v[136:139]\n"
    "ds_read_b64_tr_b16 a[168:169], v213 offset:16896\n"
    "v_mfma_f32_16x16x32_bf16 v[136:139], a[104:107], a[40:43], v[136:139]\n"
    "v_exp_f32 v129, v129\n"
    "v_mfma_f32_16x16x32_bf16 v[136:139], a[108:111], a[44:47], v[136:139]\n"
    "ds_read_b64_tr_b16 a[170:171], v213 offset:20992\n"
    "v_cvt_pk_bf16_f32 v160, v128, v129\n"
    "v_mfma_f32_16x16x32_bf16 v[140:143], a[112:115], a[32:35], v[184:187]\n"
    "v_exp_f32 v130, v130\n"
    "v_mfma_f32_16x16x32_bf16 v[140:143], a[116:119], a[36:39], v[140:143]\n"
    "ds_read_b64_tr_b16 a[172:173], v213 offset:25088\n"
    "v_mfma_f32_16x16x32_bf16 v[140:143], a[120:123], a[40:43], v[140:143]\n"
    "v_exp_f32 v131, v131\n"
    "v_mfma_f32_16x16x32_bf16 v[140:143], a[124:127], a[44:47], v[140:143]\n"
    "ds_read_b64_tr_b16 a[174:175], v213 offset:29184\n"
    "v_cvt_pk_bf16_f32 v161, v130, v131\n"
    "v_mfma_f32_16x16x32_bf16 v[32:35], a[192:195], v[168:171], v[32:35]\n"
    "v_exp_f32 v132, v132\n"
    "v_mfma_f32_16x16x32_bf16 v[32:35], a[196:199], v[172:175], v[32:35]\n"
    "ds_read_b64_tr_b16 a[176:177], v212 offset:17152\n"
    "v_mfma_f32_16x16x32_bf16 v[36:39], a[200:203], v[168:171], v[36:39]\n"
    "v_exp_f32 v133, v133\n"
    "v_mfma_f32_16x16x32_bf16 v[36:39], a[204:207], v[172:175], v[36:39]\n"
    "ds_read_b64_tr_b16 a[178:179], v212 offset:21248\n"
    "v_cvt_pk_bf16_f32 v162, v132, v133\n"
    "v_mfma_f32_16x16x32_bf16 v[40:43], a[208:211], v[168:171], v[40:43]\n"
    "v_exp_f32 v134, v134\n"
    "v_mfma_f32_16x16x32_bf16 v[40:43], a[212:215], v[172:175], v[40:43]\n"
    "ds_read_b64_tr_b16 a[180:181], v212 offset:25344\n"
    "v_mfma_f32_16x16x32_bf16 v[44:47], a[216:219], v[168:171], v[44:47]\n"
    "v_exp_f32 v135, v135\n"
    "v_mfma_f32_16x16x32_bf16 v[44:47], a[220:223], v[172:175], v[44:47]\n"
    "ds_read_b64_tr_b16 a[182:183], v212 offset:29440\n"
    "v_cvt_pk_bf16_f32 v163, v134, v135\n"
    "v_mfma_f32_16x16x32_bf16 v[48:51], a[224:227], v[168:171], v[48:51]\n"
    "v_exp_f32 v136, v136\n"
    "v_mfma_f32_16x16x32_bf16 v[48:51], a[228:231], v[172:175], v[48:51]\n"
    "ds_read_b64_tr_b16 a[184:185], v213 offset:17152\n"
    "v_mfma_f32_16x16x32_bf16 v[52:55], a[232:235], v[168:171], v[52:55]\n"
    "v_exp_f32 v137, v137\n"
    "v_mfma_f32_16x16x32_bf16 v[52:55], a[236:239], v[172:175], v[52:55]\n"
    "ds_read_b64_tr_b16 a[186:187], v213 offset:21248\n"
    "v_cvt_pk_bf16_f32 v164, v136, v137\n"
    "v_mfma_f32_16x16x32_bf16 v[56:59], a[240:243], v[168:171], v[56:59]\n"
    "v_exp_f32 v138, v138\n"
    "v_mfma_f32_16x16x32_bf16 v[56:59], a[244:247], v[172:175], v[56:59]\n"
    "ds_read_b64_tr_b16 a[188:189], v213 offset:25344\n"
    "v_mfma_f32_16x16x32_bf16 v[60:63], a[248:251], v[168:171], v[60:63]\n"
    "v_exp_f32 v139, v139\n"
    "v_mfma_f32_16x16x32_bf16 v[60:63], a[252:255], v[172:175], v[60:63]\n"
    "ds_read_b64_tr_b16 a[190:191], v213 offset:29440\n"
    "v_cvt_pk_bf16_f32 v165, v138, v139\n"
    "v_smfmac_f32_16x16x64_bf16 v[196:199], v[220:223], v[168:175], v224\n"
    "v_exp_f32 v140, v140\n"
    "v_mfma_f32_16x16x32_bf16 v[144:147], a[64:67], a[48:51], v[188:191]\n"
    "v_mfma_f32_16x16x32_bf16 v[144:147], a[68:71], a[52:55], v[144:147]\n"
    "v_exp_f32 v141, v141\n"
    "v_mfma_f32_16x16x32_bf16 v[144:147], a[72:75], a[56:59], v[144:147]\n"
    "ds_read_b128 a[64:67], v208 offset:0\n"
    "v_cvt_pk_bf16_f32 v166, v140, v141\n"
    "v_mfma_f32_16x16x32_bf16 v[144:147], a[76:79], a[60:63], v[144:147]\n"
    "v_exp_f32 v142, v142\n"
    "v_mfma_f32_16x16x32_bf16 v[148:151], a[80:83], a[48:51], v[188:191]\n"
    "ds_read_b128 a[68:71], v209 offset:0\n"
    "ds_read_b128 a[72:75], v210 offset:0\n"
    "v_mfma_f32_16x16x32_bf16 v[148:151], a[84:87], a[52:55], v[148:151]\n"
    "v_exp_f32 v143, v143\n"
    "v_mfma_f32_16x16x32_bf16 v[148:151], a[88:91], a[56:59], v[148:151]\n"
    "ds_read_b128 a[76:79], v211 offset:0\n"
    "v_cvt_pk_bf16_f32 v167, v142, v143\n"
    "v_mfma_f32_16x16x32_bf16 v[148:151], a[92:95], a[60:63], v[148:151]\n"
    "ds_read_b128 a[80:83], v208 offset:4096\n"
    "ds_read_b128 a[84:87], v209 offset:4096\n"
    "v_mfma_f32_16x16x32_bf16 v[152:155], a[96:99], a[48:51], v[188:191]\n"
    "v_exp_f32 v144, v144\n"
    "v_mfma_f32_16x16x32_bf16 v[152:155], a[100:103], a[52:55], v[152:155]\n"
    "ds_read_b128 a[88:91], v210 offset:4096\n"
    "ds_read_b128 a[92:95], v211 offset:4096\n"
    "v_mfma_f32_16x16x32_bf16 v[152:155], a[104:107], a[56:59], v[152:155]\n"
    "v_exp_f32 v145, v145\n"
    "v_mfma_f32_16x16x32_bf16 v[152:155], a[108:111], a[60:63], v[152:155]\n"
    "ds_read_b128 a[96:99], v208 offset:8192\n"
    "v_cvt_pk_bf16_f32 v168, v144, v145\n"
    "v_mfma_f32_16x16x32_bf16 v[156:159], a[112:115], a[48:51], v[188:191]\n"
    "v_exp_f32 v146, v146\n"
    "v_mfma_f32_16x16x32_bf16 v[156:159], a[116:119], a[52:55], v[156:159]\n"
    "ds_read_b128 a[100:103], v209 offset:8192\n"
    "ds_read_b128 a[104:107], v210 offset:8192\n"
    "v_mfma_f32_16x16x32_bf16 v[156:159], a[120:123], a[56:59], v[156:159]\n"
    "v_exp_f32 v147, v147\n"
    "v_mfma_f32_16x16x32_bf16 v[156:159], a[124:127], a[60:63], v[156:159]\n"
    "ds_read_b128 a[108:111], v211 offset:8192\n"
    "v_cvt_pk_bf16_f32 v169, v146, v147\n"
    "v_mfma_f32_16x16x32_bf16 v[64:67], a[192:195], v[160:163], v[64:67]\n"
    "v_exp_f32 v148, v148\n"
    "v_mfma_f32_16x16x32_bf16 v[64:67], a[196:199], v[164:167], v[64:67]\n"
    "ds_read_b128 a[112:115], v208 offset:12288\n"
    "ds_read_b128 a[116:119], v209 offset:12288\n"
    "v_mfma_f32_16x16x32_bf16 v[68:71], a[200:203], v[160:163], v[68:71]\n"
    "v_exp_f32 v149, v149\n"
    "v_mfma_f32_16x16x32_bf16 v[68:71], a[204:207], v[164:167], v[68:71]\n"
    "ds_read_b128 a[120:123], v210 offset:12288\n"
    "v_cvt_pk_bf16_f32 v170, v148, v149\n"
    "v_mfma_f32_16x16x32_bf16 v[72:75], a[208:211], v[160:163], v[72:75]\n"
    "v_exp_f32 v150, v150\n"
    "v_mfma_f32_16x16x32_bf16 v[72:75], a[212:215], v[164:167], v[72:75]\n"
    "ds_read_b128 a[124:127], v211 offset:12288\n"
    "s_add_u32 s72, s72, 0x4000\n"
    "v_mfma_f32_16x16x32_bf16 v[76:79], a[216:219], v[160:163], v[76:79]\n"
    "v_exp_f32 v151, v151\n"
    "v_mfma_f32_16x16x32_bf16 v[76:79], a[220:223], v[164:167], v[76:79]\n"
    "v_cvt_pk_bf16_f32 v171, v150, v151\n"
    "s_add_u32 s73, s73, s37\n"
    "v_mfma_f32_16x16x32_bf16 v[80:83], a[224:227], v[160:163], v[80:83]\n"
    "v_exp_f32 v152, v152\n"
    "v_mfma_f32_16x16x32_bf16 v[80:83], a[228:231], v[164:167], v[80:83]\n"
    "s_add_u32 s92, s92, s36\n"
    "s_cmp_lt_u32 s72, 524288\n"
    "v_mfma_f32_16x16x32_bf16 v[84:87], a[232:235], v[160:163], v[84:87]\n"
    "v_exp_f32 v153, v153\n"
    "v_mfma_f32_16x16x32_bf16 v[84:87], a[236:239], v[164:167], v[84:87]\n"
    "v_cvt_pk_bf16_f32 v172, v152, v153\n"
    "v_mfma_f32_16x16x32_bf16 v[88:91], a[240:243], v[160:163], v[88:91]\n"
    "v_exp_f32 v154, v154\n"
    "v_mfma_f32_16x16x32_bf16 v[88:91], a[244:247], v[164:167], v[88:91]\n"
    "s_waitcnt vmcnt(2) lgkmcnt(0)\n"
    "s_barrier\n"
    "v_mfma_f32_16x16x32_bf16 v[92:95], a[248:251], v[160:163], v[92:95]\n"
    "v_exp_f32 v155, v155\n"
    "v_mfma_f32_16x16x32_bf16 v[92:95], a[252:255], v[164:167], v[92:95]\n"
    "v_cvt_pk_bf16_f32 v173, v154, v155\n"
    "v_smfmac_f32_16x16x64_bf16 v[200:203], v[220:223], v[160:167], v224\n"
    "s_cbranch_scc1 BODY0%=\n"
    "SSB%=:\n"
    "v_mfma_f32_16x16x32_bf16 v[128:131], a[64:67], a[0:3], v[176:179]\n"
    "s_lshl_b32 s83, s78, 10\n"
    "v_exp_f32 v156, v156\n"
    "v_mfma_f32_16x16x32_bf16 v[128:131], a[68:71], a[4:7], v[128:131]\n"
    "s_add_u32 s92, s88, 1\n"
    "v_add_u32 v227, 4096, v244\n"
    "v_mov_b32 v225, v233\n"
    "v_mfma_f32_16x16x32_bf16 v[128:131], a[72:75], a[8:11], v[128:131]\n"
    "s_lshl_b32 s73, s78, 13\n"
    "v_exp_f32 v157, v157\n"
    "v_mfma_f32_16x16x32_bf16 v[128:131], a[76:79], a[12:15], v[128:131]\n"
    "v_lshlrev_b32 v246, 3, v239\n"
    "v_add_u32 v226, 32768, v246\n"
    "v_add_u32 v245, 155648, v246\n"
    "v_mfma_f32_16x16x32_bf16 v[132:135], a[80:83], a[0:3], v[176:179]\n"
    "v_lshl_add_u32 v246, v239, 2, s78\n"
    "v_exp_f32 v158, v158\n"
    "v_mfma_f32_16x16x32_bf16 v[132:135], a[84:87], a[4:7], v[132:135]\n"
    "v_mul_u32_u24 v247, 43, v246\n"
    "v_lshrrev_b32 v247, 7, v247\n"
    "v_cvt_pk_bf16_f32 v174, v156, v157\n"
    "v_mfma_f32_16x16x32_bf16 v[132:135], a[88:91], a[8:11], v[132:135]\n"
    "v_add3_u32 v246, v246, v247, s92\n"
    "v_exp_f32 v159, v159\n"
    "v_mfma_f32_16x16x32_bf16 v[132:135], a[92:95], a[12:15], v[132:135]\n"
    "v_and_b32 v246, 3, v246\n"
    "v_lshl_add_u32 v246, v246, 3, v247\n"
    "v_cvt_pk_bf16_f32 v175, v158, v159\n"
    "v_mfma_f32_16x16x32_bf16 v[136:139], a[96:99], a[0:3], v[176:179]\n"
    "v_mul_u32_u24 v248, 36864, v246\n"
    "v_exp_f32 v128, v128\n"
    "v_mfma_f32_16x16x32_bf16 v[136:139], a[100:103], a[4:7], v[136:139]\n"
    "s_add_u32 s81, s78, 4\n"
    "v_exp_f32 v129, v129\n"
    "v_mfma_f32_16x16x32_bf16 v[136:139], a[104:107], a[8:11], v[136:139]\n"
    "s_mul_i32 s81, s81, 36864\n"
    "v_cvt_pk_bf16_f32 v160, v128, v129\n"
    "v_mfma_f32_16x16x32_bf16 v[136:139], a[108:111], a[12:15], v[136:139]\n"
    "s_add_u32 s81, s81, s38\n"
    "v_exp_f32 v130, v130\n"
    "v_mfma_f32_16x16x32_bf16 v[140:143], a[112:115], a[0:3], v[176:179]\n"
    "v_writelane_b32 v248, s81, 7\n"
    "s_lshl_b32 s81, s93, 2\n"
    "s_add_u32 s81, s81, s78\n"
    "v_mfma_f32_16x16x32_bf16 v[140:143], a[116:119], a[4:7], v[140:143]\n"
    "s_lshl_b32 s81, s81, 14\n"
    "v_exp_f32 v131, v131\n"
    "v_mfma_f32_16x16x32_bf16 v[140:143], a[120:123], a[8:11], v[140:143]\n"
    "s_add_u32 s81, s81, 25165824\n"
    "v_lshl_add_u32 v231, v239, 7, s81\n"
    "v_cvt_pk_bf16_f32 v161, v130, v131\n"
    "v_mfma_f32_16x16x32_bf16 v[140:143], a[124:127], a[12:15], v[140:143]\n"
    "v_add_u32 v252, 8192, v231\n"
    "v_exp_f32 v132, v132\n"
    "v_mfma_f32_16x16x32_bf16 v[96:99], a[192:195], v[168:171], v[96:99]\n"
    "s_lshr_b32 s85, s93, 2\n"
    "v_exp_f32 v133, v133\n"
    "v_mfma_f32_16x16x32_bf16 v[96:99], a[196:199], v[172:175], v[96:99]\n"
    "s_lshl_b32 s80, s85, 18\n"
    "s_lshl_b32 s85, s85, 6\n"
    "v_cvt_pk_bf16_f32 v162, v132, v133\n"
    "v_mfma_f32_16x16x32_bf16 v[100:103], a[200:203], v[168:171], v[100:103]\n"
    "s_add_u32 s85, s85, 69632\n"
    "v_exp_f32 v134, v134\n"
    "v_mfma_f32_16x16x32_bf16 v[100:103], a[204:207], v[172:175], v[100:103]\n"
    "s_and_b32 s86, s88, 3\n"
    "v_exp_f32 v135, v135\n"
    "v_mfma_f32_16x16x32_bf16 v[104:107], a[208:211], v[168:171], v[104:107]\n"
    "s_lshl_b32 s86, s86, 2\n"
    "s_add_u32 s86, s86, s85\n"
    "v_cvt_pk_bf16_f32 v163, v134, v135\n"
    "v_mfma_f32_16x16x32_bf16 v[104:107], a[212:215], v[172:175], v[104:107]\n"
    "v_or_b32 v246, s78, v239\n"
    "v_exp_f32 v136, v136\n"
    "v_mfma_f32_16x16x32_bf16 v[108:111], a[216:219], v[168:171], v[108:111]\n"
    "v_exp_f32 v137, v137\n"
    "v_mfma_f32_16x16x32_bf16 v[108:111], a[220:223], v[172:175], v[108:111]\n"
    "v_mov_b32 v247, 2147483632\n"
    "v_cvt_pk_bf16_f32 v164, v136, v137\n"
    "v_mfma_f32_16x16x32_bf16 v[112:115], a[224:227], v[168:171], v[112:115]\n"
    "v_cmp_eq_u32 vcc, 0, v246\n"
    "s_nop 1\n"
    "v_cndmask_b32 v234, v247, 0, vcc\n"
    "v_mfma_f32_16x16x32_bf16 v[112:115], a[228:231], v[172:175], v[112:115]\n"
    "v_and_b32 v229, 3, v239\n"
    "v_exp_f32 v138, v138\n"
    "v_mfma_f32_16x16x32_bf16 v[116:119], a[232:235], v[168:171], v[116:119]\n"
    "v_exp_f32 v139, v139\n"
    "v_mfma_f32_16x16x32_bf16 v[116:119], a[236:239], v[172:175], v[116:119]\n"
    "v_lshlrev_b32 v229, 2, v229\n"
    "v_add_u32 v230, 89600, v229\n"
    "v_mfma_f32_16x16x32_bf16 v[120:123], a[240:243], v[168:171], v[120:123]\n"
    "v_mov_b32 v253, 1\n"
    "v_cvt_pk_bf16_f32 v165, v138, v139\n"
    "v_mfma_f32_16x16x32_bf16 v[120:123], a[244:247], v[172:175], v[120:123]\n"
    "v_exp_f32 v140, v140\n"
    "v_mfma_f32_16x16x32_bf16 v[124:127], a[248:251], v[168:171], v[124:127]\n"
    "s_lshl_b32 s81, s78, 14\n"
    "s_add_u32 s80, s80, s81\n"
    "v_mfma_f32_16x16x32_bf16 v[124:127], a[252:255], v[172:175], v[124:127]\n"
    "v_exp_f32 v141, v141\n"
    "v_smfmac_f32_16x16x64_bf16 v[204:207], v[220:223], v[168:175], v224\n"
    "s_add_u32 s80, s80, 25165824\n"
    "s_add_u32 s81, s88, 1\n"
    "v_mfma_f32_16x16x32_bf16 v[144:147], a[64:67], a[16:19], v[180:183]\n"
    "s_and_b32 s81, s81, 3\n"
    "v_cvt_pk_bf16_f32 v166, v140, v141\n"
    "v_mfma_f32_16x16x32_bf16 v[144:147], a[68:71], a[20:23], v[144:147]\n"
    "s_lshl_b32 s81, s81, 16\n"
    "ds_read_b128 a[64:67], v208 offset:16384\n"
    "v_mfma_f32_16x16x32_bf16 v[144:147], a[72:75], a[24:27], v[144:147]\n"
    "v_exp_f32 v142, v142\n"
    "v_mfma_f32_16x16x32_bf16 v[144:147], a[76:79], a[28:31], v[144:147]\n"
    "s_add_u32 s72, s80, s81\n"
    "ds_read_b128 a[68:71], v209 offset:16384\n"
    "ds_read_b128 a[72:75], v210 offset:16384\n"
    "v_mfma_f32_16x16x32_bf16 v[148:151], a[80:83], a[16:19], v[180:183]\n"
    "v_exp_f32 v143, v143\n"
    "v_mfma_f32_16x16x32_bf16 v[148:151], a[84:87], a[20:23], v[148:151]\n"
    "s_add_u32 s76, s72, 8192\n"
    "ds_read_b128 a[76:79], v211 offset:16384\n"
    "ds_read_b128 a[80:83], v208 offset:20480\n"
    "v_mfma_f32_16x16x32_bf16 v[148:151], a[88:91], a[24:27], v[148:151]\n"
    "v_cvt_pk_bf16_f32 v167, v142, v143\n"
    "ds_read_b128 a[84:87], v209 offset:20480\n"
    "v_mfma_f32_16x16x32_bf16 v[148:151], a[92:95], a[28:31], v[148:151]\n"
    "s_add_u32 s81, s88, 2\n"
    "ds_read_b128 a[88:91], v210 offset:20480\n"
    "v_mfma_f32_16x16x32_bf16 v[152:155], a[96:99], a[16:19], v[180:183]\n"
    "v_exp_f32 v144, v144\n"
    "ds_read_b128 a[92:95], v211 offset:20480\n"
    "v_mfma_f32_16x16x32_bf16 v[152:155], a[100:103], a[20:23], v[152:155]\n"
    "s_and_b32 s81, s81, 3\n"
    "v_exp_f32 v145, v145\n"
    "v_mfma_f32_16x16x32_bf16 v[152:155], a[104:107], a[24:27], v[152:155]\n"
    "v_cvt_pk_bf16_f32 v168, v144, v145\n"
    "ds_read_b128 a[96:99], v208 offset:24576\n"
    "ds_read_b128 a[100:103], v209 offset:24576\n"
    "v_mfma_f32_16x16x32_bf16 v[152:155], a[108:111], a[28:31], v[152:155]\n"
    "s_lshl_b32 s81, s81, 16\n"
    "v_exp_f32 v146, v146\n"
    "v_mfma_f32_16x16x32_bf16 v[156:159], a[112:115], a[16:19], v[180:183]\n"
    "v_exp_f32 v147, v147\n"
    "ds_read_b128 a[104:107], v210 offset:24576\n"
    "ds_read_b128 a[108:111], v211 offset:24576\n"
    "v_mfma_f32_16x16x32_bf16 v[156:159], a[116:119], a[20:23], v[156:159]\n"
    "s_add_u32 s74, s80, s81\n"
    "v_cvt_pk_bf16_f32 v169, v146, v147\n"
    "ds_read_b128 a[112:115], v208 offset:28672\n"
    "v_mfma_f32_16x16x32_bf16 v[156:159], a[120:123], a[24:27], v[156:159]\n"
    "v_exp_f32 v148, v148\n"
    "ds_read_b128 a[116:119], v209 offset:28672\n"
    "v_mfma_f32_16x16x32_bf16 v[156:159], a[124:127], a[28:31], v[156:159]\n"
    "s_add_u32 s77, s74, 8192\n"
    "v_exp_f32 v149, v149\n"
    "ds_read_b128 a[120:123], v210 offset:28672\n"
    "v_mfma_f32_16x16x32_bf16 v[0:3], a[128:131], v[160:163], v[0:3]\n"
    "v_cvt_pk_bf16_f32 v170, v148, v149\n"
    "ds_read_b128 a[124:127], v211 offset:28672\n"
    "ds_read_b64_tr_b16 a[192:193], v212 offset:0\n"
    "ds_read_b64_tr_b16 a[194:195], v212 offset:4096\n"
    "v_mfma_f32_16x16x32_bf16 v[0:3], a[132:135], v[164:167], v[0:3]\n"
    "s_add_u32 s81, s88, 3\n"
    "ds_read_b64_tr_b16 a[196:197], v212 offset:8192\n"
    "ds_read_b64_tr_b16 a[198:199], v212 offset:12288\n"
    "v_mfma_f32_16x16x32_bf16 v[4:7], a[136:139], v[160:163], v[4:7]\n"
    "v_exp_f32 v150, v150\n"
    "ds_read_b64_tr_b16 a[200:201], v213 offset:0\n"
    "v_mfma_f32_16x16x32_bf16 v[4:7], a[140:143], v[164:167], v[4:7]\n"
    "s_and_b32 s81, s81, 3\n"
    "v_exp_f32 v151, v151\n"
    "v_mfma_f32_16x16x32_bf16 v[8:11], a[144:147], v[160:163], v[8:11]\n"
    "v_cvt_pk_bf16_f32 v171, v150, v151\n"
    "ds_read_b64_tr_b16 a[202:203], v213 offset:4096\n"
    "ds_read_b64_tr_b16 a[204:205], v213 offset:8192\n"
    "v_mfma_f32_16x16x32_bf16 v[8:11], a[148:151], v[164:167], v[8:11]\n"
    "s_lshl_b32 s81, s81, 16\n"
    "ds_read_b64_tr_b16 a[206:207], v213 offset:12288\n"
    "ds_read_b64_tr_b16 a[208:209], v212 offset:256\n"
    "v_mfma_f32_16x16x32_bf16 v[12:15], a[152:155], v[160:163], v[12:15]\n"
    "v_exp_f32 v152, v152\n"
    "ds_read_b64_tr_b16 a[210:211], v212 offset:4352\n"
    "v_mfma_f32_16x16x32_bf16 v[12:15], a[156:159], v[164:167], v[12:15]\n"
    "s_add_u32 s75, s80, s81\n"
    "ds_read_b64_tr_b16 a[212:213], v212 offset:8448\n"
    "ds_read_b64_tr_b16 a[214:215], v212 offset:12544\n"
    "v_mfma_f32_16x16x32_bf16 v[16:19], a[160:163], v[160:163], v[16:19]\n"
    "v_exp_f32 v153, v153\n"
    "v_mfma_f32_16x16x32_bf16 v[16:19], a[164:167], v[164:167], v[16:19]\n"
    "s_add_u32 s36, s75, 8192\n"
    "ds_read_b64_tr_b16 a[216:217], v213 offset:256\n"
    "v_mfma_f32_16x16x32_bf16 v[20:23], a[168:171], v[160:163], v[20:23]\n"
    "v_cvt_pk_bf16_f32 v172, v152, v153\n"
    "ds_read_b64_tr_b16 a[218:219], v213 offset:4352\n"
    "v_mfma_f32_16x16x32_bf16 v[20:23], a[172:175], v[164:167], v[20:23]\n"
    "v_exp_f32 v154, v154\n"
    "v_mfma_f32_16x16x32_bf16 v[24:27], a[176:179], v[160:163], v[24:27]\n"
    "ds_read_b64_tr_b16 a[220:221], v213 offset:8448\n"
    "ds_read_b64_tr_b16 a[222:223], v213 offset:12544\n"
    "v_mfma_f32_16x16x32_bf16 v[24:27], a[180:183], v[164:167], v[24:27]\n"
    "v_exp_f32 v155, v155\n"
    "v_mfma_f32_16x16x32_bf16 v[28:31], a[184:187], v[160:163], v[28:31]\n"
    "ds_read_b64_tr_b16 a[224:225], v212 offset:512\n"
    "ds_read_b64_tr_b16 a[226:227], v212 offset:4608\n"
    "v_mfma_f32_16x16x32_bf16 v[28:31], a[188:191], v[164:167], v[28:31]\n"
    "v_cvt_pk_bf16_f32 v173, v154, v155\n"
    "ds_read_b64_tr_b16 a[228:229], v212 offset:8704\n"
    "v_smfmac_f32_16x16x64_bf16 v[192:195], v[220:223], v[160:167], v224\n"
    "v_exp_f32 v156, v156\n"
    "s_waitcnt lgkmcnt(3)\n"
    "v_mfma_f32_16x16x32_bf16 v[128:131], a[64:67], a[0:3], v[176:179]\n"
    "ds_read_b64_tr_b16 a[230:231], v212 offset:12800\n"
    "ds_read_b64_tr_b16 a[232:233], v213 offset:512\n"
    "v_mfma_f32_16x16x32_bf16 v[128:131], a[68:71], a[4:7], v[128:131]\n"
    "v_exp_f32 v157, v157\n"
    "v_mfma_f32_16x16x32_bf16 v[128:131], a[72:75], a[8:11], v[128:131]\n"
    "ds_read_b64_tr_b16 a[234:235], v213 offset:4608\n"
    "ds_read_b64_tr_b16 a[236:237], v213 offset:8704\n"
    "v_mfma_f32_16x16x32_bf16 v[128:131], a[76:79], a[12:15], v[128:131]\n"
    "v_cvt_pk_bf16_f32 v174, v156, v157\n"
    "ds_read_b64_tr_b16 a[238:239], v213 offset:12800\n"
    "v_mfma_f32_16x16x32_bf16 v[132:135], a[80:83], a[0:3], v[176:179]\n"
    "v_exp_f32 v158, v158\n"
    "v_mfma_f32_16x16x32_bf16 v[132:135], a[84:87], a[4:7], v[132:135]\n"
    "ds_read_b64_tr_b16 a[240:241], v212 offset:768\n"
    "ds_read_b64_tr_b16 a[242:243], v212 offset:4864\n"
    "v_mfma_f32_16x16x32_bf16 v[132:135], a[88:91], a[8:11], v[132:135]\n"
    "v_exp_f32 v159, v159\n"
    "v_mfma_f32_16x16x32_bf16 v[132:135], a[92:95], a[12:15], v[132:135]\n"
    "ds_read_b64_tr_b16 a[244:245], v212 offset:8960\n"
    "ds_read_b64_tr_b16 a[246:247], v212 offset:13056\n"
    "v_mfma_f32_16x16x32_bf16 v[136:139], a[96:99], a[0:3], v[176:179]\n"
    "v_cvt_pk_bf16_f32 v175, v158, v159\n"
    "ds_read_b64_tr_b16 a[248:249], v213 offset:768\n"
    "v_mfma_f32_16x16x32_bf16 v[136:139], a[100:103], a[4:7], v[136:139]\n"
    "v_exp_f32 v128, v128\n"
    "v_mfma_f32_16x16x32_bf16 v[136:139], a[104:107], a[8:11], v[136:139]\n"
    "ds_read_b64_tr_b16 a[250:251], v213 offset:4864\n"
    "ds_read_b64_tr_b16 a[252:253], v213 offset:8960\n"
    "v_mfma_f32_16x16x32_bf16 v[136:139], a[108:111], a[12:15], v[136:139]\n"
    "v_exp_f32 v129, v129\n"
    "v_mfma_f32_16x16x32_bf16 v[140:143], a[112:115], a[0:3], v[176:179]\n"
    "v_cvt_pk_bf16_f32 v160, v128, v129\n"
    "ds_read_b64_tr_b16 a[254:255], v213 offset:13056\n"
    "v_mfma_f32_16x16x32_bf16 v[140:143], a[116:119], a[4:7], v[140:143]\n"
    "v_exp_f32 v130, v130\n"
    "v_mfma_f32_16x16x32_bf16 v[140:143], a[120:123], a[8:11], v[140:143]\n"
    "v_exp_f32 v131, v131\n"
    "v_mfma_f32_16x16x32_bf16 v[140:143], a[124:127], a[12:15], v[140:143]\n"
    "v_readlane_b32 s81, v248, 7\n"
    "v_cvt_pk_bf16_f32 v161, v130, v131\n"
    "v_mfma_f32_16x16x32_bf16 v[32:35], a[128:131], v[168:171], v[32:35]\n"
    "s_add_u32 m0, s83, 155648\n"
    "s_lshl_b32 s82, s93, 16\n"
    "v_mfma_f32_16x16x32_bf16 v[32:35], a[132:135], v[172:175], v[32:35]\n"
    "v_exp_f32 v132, v132\n"
    "v_mfma_f32_16x16x32_bf16 v[36:39], a[136:139], v[168:171], v[36:39]\n"
    "s_waitcnt vmcnt(0) lgkmcnt(0)\n"
    "s_barrier\n"
    "v_mfma_f32_16x16x32_bf16 v[36:39], a[140:143], v[172:175], v[36:39]\n"
    "s_nop 0\n"
    "buffer_load_dwordx4 v225, s[52:55], s81 offen lds\n"
    "v_mfma_f32_16x16x32_bf16 v[40:43], a[144:147], v[168:171], v[40:43]\n"
    "v_exp_f32 v133, v133\n"
    "v_mfma_f32_16x16x32_bf16 v[40:43], a[148:151], v[172:175], v[40:43]\n"
    "s_add_u32 m0, s73, 90112\n"
    "s_nop 0\n"
    "v_mfma_f32_16x16x32_bf16 v[44:47], a[152:155], v[168:171], v[44:47]\n"
    "v_cvt_pk_bf16_f32 v162, v132, v133\n"
    "s_lshl_b32 s84, s78, 14\n"
    "v_mfma_f32_16x16x32_bf16 v[44:47], a[156:159], v[172:175], v[44:47]\n"
    "v_exp_f32 v134, v134\n"
    "v_mfma_f32_16x16x32_bf16 v[48:51], a[160:163], v[168:171], v[48:51]\n"
    "buffer_load_dwordx4 v240, s[52:55], s81 offen offset:0  lds\n"
    "v_mfma_f32_16x16x32_bf16 v[48:51], a[164:167], v[172:175], v[48:51]\n"
    "v_exp_f32 v135, v135\n"
    "v_mfma_f32_16x16x32_bf16 v[52:55], a[168:171], v[168:171], v[52:55]\n"
    "v_cvt_pk_bf16_f32 v163, v134, v135\n"
    "s_add_u32 s82, s82, s84\n"
    "v_mfma_f32_16x16x32_bf16 v[52:55], a[172:175], v[172:175], v[52:55]\n"
    "v_exp_f32 v136, v136\n"
    "v_mfma_f32_16x16x32_bf16 v[56:59], a[176:179], v[168:171], v[56:59]\n"
    "v_exp_f32 v137, v137\n"
    "v_mfma_f32_16x16x32_bf16 v[56:59], a[180:183], v[172:175], v[56:59]\n"
    "buffer_load_dwordx4 v241, s[52:55], s81 offen offset:1024  lds\n"
    "v_mfma_f32_16x16x32_bf16 v[60:63], a[184:187], v[168:171], v[60:63]\n"
    "v_cvt_pk_bf16_f32 v164, v136, v137\n"
    "s_add_u32 s82, s82, 25165824\n"
    "v_mfma_f32_16x16x32_bf16 v[60:63], a[188:191], v[172:175], v[60:63]\n"
    "v_exp_f32 v138, v138\n"
    "v_smfmac_f32_16x16x64_bf16 v[196:199], v[220:223], v[168:175], v224\n"
    "v_exp_f32 v139, v139\n"
    "v_mfma_f32_16x16x32_bf16 v[144:147], a[64:67], a[16:19], v[180:183]\n"
    "v_cvt_pk_bf16_f32 v165, v138, v139\n"
    "v_mfma_f32_16x16x32_bf16 v[144:147], a[68:71], a[20:23], v[144:147]\n"
    "buffer_load_dwordx4 v242, s[52:55], s81 offen offset:2048  lds\n"
    "v_mfma_f32_16x16x32_bf16 v[144:147], a[72:75], a[24:27], v[144:147]\n"
    "v_exp_f32 v140, v140\n"
    "v_mfma_f32_16x16x32_bf16 v[144:147], a[76:79], a[28:31], v[144:147]\n"
    "v_exp_f32 v141, v141\n"
    "v_mfma_f32_16x16x32_bf16 v[148:151], a[80:83], a[16:19], v[180:183]\n"
    "v_cvt_pk_bf16_f32 v166, v140, v141\n"
    "v_mfma_f32_16x16x32_bf16 v[148:151], a[84:87], a[20:23], v[148:151]\n"
    "v_exp_f32 v142, v142\n"
    "v_mfma_f32_16x16x32_bf16 v[148:151], a[88:91], a[24:27], v[148:151]\n"
    "buffer_load_dwordx4 v243, s[52:55], s81 offen offset:3072  lds\n"
    "v_mfma_f32_16x16x32_bf16 v[148:151], a[92:95], a[28:31], v[148:151]\n"
    "v_exp_f32 v143, v143\n"
    "v_mfma_f32_16x16x32_bf16 v[152:155], a[96:99], a[16:19], v[180:183]\n"
    "s_add_u32 m0, s73, 94208\n"
    "s_add_u32 s81, s81, 18432\n"
    "v_mfma_f32_16x16x32_bf16 v[152:155], a[100:103], a[20:23], v[152:155]\n"
    "v_cvt_pk_bf16_f32 v167, v142, v143\n"
    "v_mfma_f32_16x16x32_bf16 v[152:155], a[104:107], a[24:27], v[152:155]\n"
    "v_exp_f32 v144, v144\n"
    "v_mfma_f32_16x16x32_bf16 v[152:155], a[108:111], a[28:31], v[152:155]\n"
    "buffer_load_dwordx4 v240, s[52:55], s81 offen offset:0  lds\n"
    "v_mfma_f32_16x16x32_bf16 v[156:159], a[112:115], a[16:19], v[180:183]\n"
    "v_exp_f32 v145, v145\n"
    "v_mfma_f32_16x16x32_bf16 v[156:159], a[116:119], a[20:23], v[156:159]\n"
    "v_cvt_pk_bf16_f32 v168, v144, v145\n"
    "v_mfma_f32_16x16x32_bf16 v[156:159], a[120:123], a[24:27], v[156:159]\n"
    "v_exp_f32 v146, v146\n"
    "v_mfma_f32_16x16x32_bf16 v[156:159], a[124:127], a[28:31], v[156:159]\n"
    "v_exp_f32 v147, v147\n"
    "v_mfma_f32_16x16x32_bf16 v[0:3], a[192:195], v[160:163], v[0:3]\n"
    "buffer_load_dwordx4 v241, s[52:55], s81 offen offset:1024  lds\n"
    "v_mfma_f32_16x16x32_bf16 v[0:3], a[196:199], v[164:167], v[0:3]\n"
    "v_cvt_pk_bf16_f32 v169, v146, v147\n"
    "v_mfma_f32_16x16x32_bf16 v[4:7], a[200:203], v[160:163], v[4:7]\n"
    "v_exp_f32 v148, v148\n"
    "v_mfma_f32_16x16x32_bf16 v[4:7], a[204:207], v[164:167], v[4:7]\n"
    "v_exp_f32 v149, v149\n"
    "v_mfma_f32_16x16x32_bf16 v[8:11], a[208:211], v[160:163], v[8:11]\n"
    "v_cvt_pk_bf16_f32 v170, v148, v149\n"
    "v_mfma_f32_16x16x32_bf16 v[8:11], a[212:215], v[164:167], v[8:11]\n"
    "buffer_load_dwordx4 v242, s[52:55], s81 offen offset:2048  lds\n"
    "v_mfma_f32_16x16x32_bf16 v[12:15], a[216:219], v[160:163], v[12:15]\n"
    "v_exp_f32 v150, v150\n"
    "v_mfma_f32_16x16x32_bf16 v[12:15], a[220:223], v[164:167], v[12:15]\n"
    "v_exp_f32 v151, v151\n"
    "v_mfma_f32_16x16x32_bf16 v[16:19], a[224:227], v[160:163], v[16:19]\n"
    "v_cvt_pk_bf16_f32 v171, v150, v151\n"
    "v_mfma_f32_16x16x32_bf16 v[16:19], a[228:231], v[164:167], v[16:19]\n"
    "v_exp_f32 v152, v152\n"
    "v_mfma_f32_16x16x32_bf16 v[20:23], a[232:235], v[160:163], v[20:23]\n"
    "buffer_load_dwordx4 v243, s[52:55], s81 offen offset:3072  lds\n"
    "v_mfma_f32_16x16x32_bf16 v[20:23], a[236:239], v[164:167], v[20:23]\n"
    "v_exp_f32 v153, v153\n"
    "v_mfma_f32_16x16x32_bf16 v[24:27], a[240:243], v[160:163], v[24:27]\n"
    "v_cvt_pk_bf16_f32 v172, v152, v153\n"
    "v_mfma_f32_16x16x32_bf16 v[24:27], a[244:247], v[164:167], v[24:27]\n"
    "v_exp_f32 v154, v154\n"
    "v_mfma_f32_16x16x32_bf16 v[28:31], a[248:251], v[160:163], v[28:31]\n"
    "v_exp_f32 v155, v155\n"
    "v_mfma_f32_16x16x32_bf16 v[28:31], a[252:255], v[164:167], v[28:31]\n"
    "v_cvt_pk_bf16_f32 v173, v154, v155\n"
    "v_smfmac_f32_16x16x64_bf16 v[192:195], v[220:223], v[160:167], v224\n"
    "v_exp_f32 v156, v156\n"
    "v_mfma_f32_16x16x32_bf16 v[128:131], a[64:67], a[32:35], v[184:187]\n"
    "v_exp_f32 v157, v157\n"
    "v_mfma_f32_16x16x32_bf16 v[128:131], a[68:71], a[36:39], v[128:131]\n"
    "v_cvt_pk_bf16_f32 v174, v156, v157\n"
    "v_mfma_f32_16x16x32_bf16 v[128:131], a[72:75], a[40:43], v[128:131]\n"
    "v_exp_f32 v158, v158\n"
    "v_mfma_f32_16x16x32_bf16 v[128:131], a[76:79], a[44:47], v[128:131]\n"
    "s_nop 3\n"
    "v_add_f32 v192, v192, v193\n"
    "v_rcp_f32 v232, v192\n"
    "v_mfma_f32_16x16x32_bf16 v[132:135], a[80:83], a[32:35], v[184:187]\n"
    "v_cmp_ngt_f32 vcc, 1.0, v192\n"
    "s_or_b64 s[94:95], s[94:95], vcc\n"
    "v_mul_f32 v0, v0, v232\n"
    "v_mul_f32 v1, v1, v232\n"
    "v_mfma_f32_16x16x32_bf16 v[132:135], a[84:87], a[36:39], v[132:135]\n"
    "v_exp_f32 v159, v159\n"
    "v_mul_f32 v2, v2, v232\n"
    "v_mfma_f32_16x16x32_bf16 v[132:135], a[88:91], a[40:43], v[132:135]\n"
    "v_mul_f32 v3, v3, v232\n"
    "v_mul_f32 v4, v4, v232\n"
    "v_mul_f32 v5, v5, v232\n"
    "v_mfma_f32_16x16x32_bf16 v[132:135], a[92:95], a[44:47], v[132:135]\n"
    "v_cvt_pk_bf16_f32 v175, v158, v159\n"
    "v_mul_f32 v6, v6, v232\n"
    "v_mul_f32 v7, v7, v232\n"
    "v_mfma_f32_16x16x32_bf16 v[136:139], a[96:99], a[32:35], v[184:187]\n"
    "v_exp_f32 v128, v128\n"
    "v_max3_f32 v194, |v0|, |v3|, |v6|\n"
    "v_mfma_f32_16x16x32_bf16 v[136:139], a[100:103], a[36:39], v[136:139]\n"
    "v_exp_f32 v129, v129\n"
    "v_cmp_gt_f32 vcc, 0x33000000, v194\n"
    "s_or_b64 s[94:95], s[94:95], vcc\n"
    "v_mfma_f32_16x16x32_bf16 v[136:139], a[104:107], a[40:43], v[136:139]\n"
    "v_cvt_pk_bf16_f32 v160, v128, v129\n"
    "v_exp_f32 v130, v130\n"
    "v_cvt_pk_bf16_f32 v0, v0, v1\n"
    "v_mfma_f32_16x16x32_bf16 v[136:139], a[108:111], a[44:47], v[136:139]\n"
    "v_exp_f32 v131, v131\n"
    "v_cvt_pk_bf16_f32 v1, v2, v3\n"
    "v_mfma_f32_16x16x32_bf16 v[140:143], a[112:115], a[32:35], v[184:187]\n"
    "v_cvt_pk_bf16_f32 v161, v130, v131\n"
    "v_cvt_pk_bf16_f32 v2, v4, v5\n"
    "v_cvt_pk_bf16_f32 v3, v6, v7\n"
    "v_mfma_f32_16x16x32_bf16 v[140:143], a[116:119], a[36:39], v[140:143]\n"
    "buffer_store_dwordx4 v[0:3], v244, s[56:59], s82 offen offset:0 sc1\n"
    "v_mul_f32 v8, v8, v232\n"
    "v_mul_f32 v9, v9, v232\n"
    "v_mul_f32 v10, v10, v232\n"
    "v_mfma_f32_16x16x32_bf16 v[140:143], a[120:123], a[40:43], v[140:143]\n"
    "v_exp_f32 v132, v132\n"
    "v_mul_f32 v11, v11, v232\n"
    "v_mul_f32 v12, v12, v232\n"
    "v_mfma_f32_16x16x32_bf16 v[140:143], a[124:127], a[44:47], v[140:143]\n"
    "v_exp_f32 v133, v133\n"
    "v_mul_f32 v13, v13, v232\n"
    "v_mfma_f32_16x16x32_bf16 v[32:35], a[192:195], v[168:171], v[32:35]\n"
    "v_cvt_pk_bf16_f32 v162, v132, v133\n"
    "v_mul_f32 v14, v14, v232\n"
    "v_mul_f32 v15, v15, v232\n"
    "v_mfma_f32_16x16x32_bf16 v[32:35], a[196:199], v[172:175], v[32:35]\n"
    "v_exp_f32 v134, v134\n"
    "v_cvt_pk_bf16_f32 v8, v8, v9\n"
    "v_mfma_f32_16x16x32_bf16 v[36:39], a[200:203], v[168:171], v[36:39]\n"
    "v_exp_f32 v135, v135\n"
    "v_cvt_pk_bf16_f32 v9, v10, v11\n"
    "v_mfma_f32_16x16x32_bf16 v[36:39], a[204:207], v[172:175], v[36:39]\n"
    "v_cvt_pk_bf16_f32 v163, v134, v135\n"
    "v_cvt_pk_bf16_f32 v10, v12, v13\n"
    "v_cvt_pk_bf16_f32 v11, v14, v15\n"
    "v_mfma_f32_16x16x32_bf16 v[40:43], a[208:211], v[168:171], v[40:43]\n"
    "buffer_store_dwordx4 v[8:11], v244, s[56:59], s82 offen offset:1024 sc1\n"
    "v_mfma_f32_16x16x32_bf16 v[40:43], a[212:215], v[172:175], v[40:43]\n"
    "v_exp_f32 v136, v136\n"
    "v_mul_f32 v16, v16, v232\n"
    "v_mul_f32 v17, v17, v232\n"
    "v_mfma_f32_16x16x32_bf16 v[44:47], a[216:219], v[168:171], v[44:47]\n"
    "v_mul_f32 v18, v18, v232\n"
    "v_mul_f32 v19, v19, v232\n"
    "v_mul_f32 v20, v20, v232\n"
    "v_mfma_f32_16x16x32_bf16 v[44:47], a[220:223], v[172:175], v[44:47]\n"
    "v_exp_f32 v137, v137\n"
    "v_mul_f32 v21, v21, v232\n"
    "v_mfma_f32_16x16x32_bf16 v[48:51], a[224:227], v[168:171], v[48:51]\n"
    "v_mul_f32 v22, v22, v232\n"
    "v_mul_f32 v23, v23, v232\n"
    "v_cvt_pk_bf16_f32 v16, v16, v17\n"
    "v_mfma_f32_16x16x32_bf16 v[48:51], a[228:231], v[172:175], v[48:51]\n"
    "v_cvt_pk_bf16_f32 v164, v136, v137\n"
    "v_cvt_pk_bf16_f32 v17, v18, v19\n"
    "v_cvt_pk_bf16_f32 v18, v20, v21\n"
    "v_mfma_f32_16x16x32_bf16 v[52:55], a[232:235], v[168:171], v[52:55]\n"
    "v_exp_f32 v138, v138\n"
    "v_cvt_pk_bf16_f32 v19, v22, v23\n"
    "v_mfma_f32_16x16x32_bf16 v[52:55], a[236:239], v[172:175], v[52:55]\n"
    "buffer_store_dwordx4 v[16:19], v244, s[56:59], s82 offen offset:2048 sc1\n"
    "v_mfma_f32_16x16x32_bf16 v[56:59], a[240:243], v[168:171], v[56:59]\n"
    "v_exp_f32 v139, v139\n"
    "v_mfma_f32_16x16x32_bf16 v[56:59], a[244:247], v[172:175], v[56:59]\n"
    "v_mul_f32 v24, v24, v232\n"
    "v_mul_f32 v25, v25, v232\n"
    "v_mul_f32 v26, v26, v232\n"
    "v_mfma_f32_16x16x32_bf16 v[60:63], a[248:251], v[168:171], v[60:63]\n"
    "v_cvt_pk_bf16_f32 v165, v138, v139\n"
    "v_mul_f32 v27, v27, v232\n"
    "v_mul_f32 v28, v28, v232\n"
    "v_mfma_f32_16x16x32_bf16 v[60:63], a[252:255], v[172:175], v[60:63]\n"
    "v_exp_f32 v140, v140\n"
    "v_mul_f32 v29, v29, v232\n"
    "v_smfmac_f32_16x16x64_bf16 v[196:199], v[220:223], v[168:175], v224\n"
    "v_mul_f32 v30, v30, v232\n"
    "v_mul_f32 v31, v31, v232\n"
    "v_cvt_pk_bf16_f32 v24, v24, v25\n"
    "v_cvt_pk_bf16_f32 v25, v26, v27\n"
    "v_mfma_f32_16x16x32_bf16 v[144:147], a[64:67], a[48:51], v[188:191]\n"
    "v_exp_f32 v141, v141\n"
    "v_cvt_pk_bf16_f32 v26, v28, v29\n"
    "v_cvt_pk_bf16_f32 v27, v30, v31\n"
    "v_mfma_f32_16x16x32_bf16 v[144:147], a[68:71], a[52:55], v[144:147]\n"
    "buffer_store_dwordx4 v[24:27], v244, s[56:59], s82 offen offset:3072 sc1\n"
    "v_mfma_f32_16x16x32_bf16 v[144:147], a[72:75], a[56:59], v[144:147]\n"
    "v_cvt_pk_bf16_f32 v166, v140, v141\n"
    "s_add_u32 s82, s82, 4096\n"
    "v_mfma_f32_16x16x32_bf16 v[144:147], a[76:79], a[60:63], v[144:147]\n"
    "v_exp_f32 v142, v142\n"
    "v_mfma_f32_16x16x32_bf16 v[148:151], a[80:83], a[48:51], v[188:191]\n"
    "v_add_f32 v196, v196, v197\n"
    "v_rcp_f32 v233, v196\n"
    "v_mfma_f32_16x16x32_bf16 v[148:151], a[84:87], a[52:55], v[148:151]\n"
    "v_exp_f32 v143, v143\n"
    "v_cmp_ngt_f32 vcc, 1.0, v196\n"
    "s_or_b64 s[94:95], s[94:95], vcc\n"
    "v_mfma_f32_16x16x32_bf16 v[148:151], a[88:91], a[56:59], v[148:151]\n"
    "v_mul_f32 v32, v32, v233\n"
    "v_mul_f32 v33, v33, v233\n"
    "v_mul_f32 v34, v34, v233\n"
    "v_mul_f32 v35, v35, v233\n"
    "v_mfma_f32_16x16x32_bf16 v[148:151], a[92:95], a[60:63], v[148:151]\n"
    "v_cvt_pk_bf16_f32 v167, v142, v143\n"
    "v_mul_f32 v36, v36, v233\n"
    "v_mul_f32 v37, v37, v233\n"
    "v_mul_f32 v38, v38, v233\n"
    "v_mfma_f32_16x16x32_bf16 v[152:155], a[96:99], a[48:51], v[188:191]\n"
    "v_exp_f32 v144, v144\n"
    "v_mul_f32 v39, v39, v233\n"
    "v_max3_f32 v198, |v32|, |v35|, |v38|\n"
    "v_mfma_f32_16x16x32_bf16 v[152:155], a[100:103], a[52:55], v[152:155]\n"
    "v_exp_f32 v145, v145\n"
    "v_cmp_gt_f32 vcc, 0x33000000, v198\n"
    "s_or_b64 s[94:95], s[94:95], vcc\n"
    "v_mfma_f32_16x16x32_bf16 v[152:155], a[104:107], a[56:59], v[152:155]\n"
    "v_cvt_pk_bf16_f32 v168, v144, v145\n"
    "v_exp_f32 v146, v146\n"
    "v_cvt_pk_bf16_f32 v32, v32, v33\n"
    "ds_read_b128 a[64:67], v208 offset:0\n"
    "v_mfma_f32_16x16x32_bf16 v[152:155], a[108:111], a[60:63], v[152:155]\n"
    "v_exp_f32 v147, v147\n"
    "v_cvt_pk_bf16_f32 v33, v34, v35\n"
    "v_cvt_pk_bf16_f32 v34, v36, v37\n"
    "v_mfma_f32_16x16x32_bf16 v[156:159], a[112:115], a[48:51], v[188:191]\n"
    "v_cvt_pk_bf16_f32 v169, v146, v147\n"
    "v_exp_f32 v148, v148\n"
    "v_cvt_pk_bf16_f32 v35, v38, v39\n"
    "ds_read_b128 a[68:71], v209 offset:0\n"
    "v_mfma_f32_16x16x32_bf16 v[156:159], a[116:119], a[52:55], v[156:159]\n"
    "buffer_store_dwordx4 v[32:35], v244, s[56:59], s82 offen offset:0 sc1\n"
    "v_mfma_f32_16x16x32_bf16 v[156:159], a[120:123], a[56:59], v[156:159]\n"
    "v_exp_f32 v149, v149\n"
    "v_mfma_f32_16x16x32_bf16 v[156:159], a[124:127], a[60:63], v[156:159]\n"
    "v_mul_f32 v40, v40, v233\n"
    "v_mul_f32 v41, v41, v233\n"
    "v_mul_f32 v42, v42, v233\n"
    "ds_read_b128 a[72:75], v210 offset:0\n"
    "v_mfma_f32_16x16x32_bf16 v[64:67], a[192:195], v[160:163], v[64:67]\n"
    "v_cvt_pk_bf16_f32 v170, v148, v149\n"
    "v_mul_f32 v43, v43, v233\n"
    "v_mul_f32 v44, v44, v233\n"
    "v_mul_f32 v45, v45, v233\n"
    "v_mfma_f32_16x16x32_bf16 v[64:67], a[196:199], v[164:167], v[64:67]\n"
    "v_mul_f32 v46, v46, v233\n"
    "v_mul_f32 v47, v47, v233\n"
    "v_cvt_pk_bf16_f32 v40, v40, v41\n"
    "ds_read_b128 a[76:79], v211 offset:0\n"
    "v_mfma_f32_16x16x32_bf16 v[68:71], a[200:203], v[160:163], v[68:71]\n"
    "v_exp_f32 v150, v150\n"
    "v_cvt_pk_bf16_f32 v41, v42, v43\n"
    "v_cvt_pk_bf16_f32 v42, v44, v45\n"
    "v_mfma_f32_16x16x32_bf16 v[68:71], a[204:207], v[164:167], v[68:71]\n"
    "v_exp_f32 v151, v151\n"
    "v_cvt_pk_bf16_f32 v43, v46, v47\n"
    "ds_read_b128 a[80:83], v208 offset:4096\n"
    "v_mfma_f32_16x16x32_bf16 v[72:75], a[208:211], v[160:163], v[72:75]\n"
    "buffer_store_dwordx4 v[40:43], v244, s[56:59], s82 offen offset:1024 sc1\n"
    "v_mfma_f32_16x16x32_bf16 v[72:75], a[212:215], v[164:167], v[72:75]\n"
    "v_cvt_pk_bf16_f32 v171, v150, v151\n"
    "v_exp_f32 v152, v152\n"
    "v_mfma_f32_16x16x32_bf16 v[76:79], a[216:219], v[160:163], v[76:79]\n"
    "v_exp_f32 v153, v153\n"
    "ds_read_b128 a[84:87], v209 offset:4096\n"
    "v_mfma_f32_16x16x32_bf16 v[76:79], a[220:223], v[164:167], v[76:79]\n"
    "v_cvt_pk_bf16_f32 v172, v152, v153\n"
    "v_mul_f32 v48, v48, v233\n"
    "v_mul_f32 v49, v49, v233\n"
    "v_mul_f32 v50, v50, v233\n"
    "v_mfma_f32_16x16x32_bf16 v[80:83], a[224:227], v[160:163], v[80:83]\n"
    "v_mul_f32 v51, v51, v233\n"
    "v_mul_f32 v52, v52, v233\n"
    "v_mul_f32 v53, v53, v233\n"
    "ds_read_b128 a[88:91], v210 offset:4096\n"
    "v_mfma_f32_16x16x32_bf16 v[80:83], a[228:231], v[164:167], v[80:83]\n"
    "v_exp_f32 v154, v154\n"
    "v_mul_f32 v54, v54, v233\n"
    "v_mul_f32 v55, v55, v233\n"
    "v_mfma_f32_16x16x32_bf16 v[84:87], a[232:235], v[160:163], v[84:87]\n"
    "v_cvt_pk_bf16_f32 v48, v48, v49\n"
    "v_cvt_pk_bf16_f32 v49, v50, v51\n"
    "v_cvt_pk_bf16_f32 v50, v52, v53\n"
    "v_cvt_pk_bf16_f32 v51, v54, v55\n"
    "ds_read_b128 a[92:95], v211 offset:4096\n"
    "v_mfma_f32_16x16x32_bf16 v[84:87], a[236:239], v[164:167], v[84:87]\n"
    "buffer_store_dwordx4 v[48:51], v244, s[56:59], s82 offen offset:2048 sc1\n"
    "v_mfma_f32_16x16x32_bf16 v[88:91], a[240:243], v[160:163], v[88:91]\n"
    "v_exp_f32 v155, v155\n"
    "v_mfma_f32_16x16x32_bf16 v[88:91], a[244:247], v[164:167], v[88:91]\n"
    "ds_read_b128 a[96:99], v208 offset:8192\n"
    "ds_read_b128 a[100:103], v209 offset:8192\n"
    "v_mfma_f32_16x16x32_bf16 v[92:95], a[248:251], v[160:163], v[92:95]\n"
    "v_cvt_pk_bf16_f32 v173, v154, v155\n"
    "v_mul_f32 v56, v56, v233\n"
    "v_mfma_f32_16x16x32_bf16 v[92:95], a[252:255], v[164:167], v[92:95]\n"
    "v_mul_f32 v57, v57, v233\n"
    "v_mul_f32 v58, v58, v233\n"
    "v_mul_f32 v59, v59, v233\n"
    "v_mul_f32 v60, v60, v233\n"
    "ds_read_b128 a[104:107], v210 offset:8192\n"
    "v_smfmac_f32_16x16x64_bf16 v[200:203], v[220:223], v[160:167], v224\n"
    "v_exp_f32 v156, v156\n"
    "v_mul_f32 v61, v61, v233\n"
    "v_mul_f32 v62, v62, v233\n"
    "v_mul_f32 v63, v63, v233\n"
    "s_waitcnt lgkmcnt(1)\n"
    "v_mfma_f32_16x16x32_bf16 v[128:131], a[64:67], a[32:35], v[184:187]\n"
    "v_cvt_pk_bf16_f32 v56, v56, v57\n"
    "v_cvt_pk_bf16_f32 v57, v58, v59\n"
    "v_cvt_pk_bf16_f32 v58, v60, v61\n"
    "v_cvt_pk_bf16_f32 v59, v62, v63\n"
    "ds_read_b128 a[108:111], v211 offset:8192\n"
    "v_mfma_f32_16x16x32_bf16 v[128:131], a[68:71], a[36:39], v[128:131]\n"
    "buffer_store_dwordx4 v[56:59], v244, s[56:59], s82 offen offset:3072 sc1\n"
    "v_mfma_f32_16x16x32_bf16 v[128:131], a[72:75], a[40:43], v[128:131]\n"
    "v_exp_f32 v157, v157\n"
    "v_mfma_f32_16x16x32_bf16 v[128:131], a[76:79], a[44:47], v[128:131]\n"
    "v_cvt_pk_bf16_f32 v174, v156, v157\n"
    "ds_read_b128 a[112:115], v208 offset:12288\n"
    "v_mfma_f32_16x16x32_bf16 v[132:135], a[80:83], a[32:35], v[184:187]\n"
    "v_exp_f32 v158, v158\n"
    "v_mfma_f32_16x16x32_bf16 v[132:135], a[84:87], a[36:39], v[132:135]\n"
    "ds_read_b128 a[116:119], v209 offset:12288\n"
    "ds_read_b128 a[120:123], v210 offset:12288\n"
    "v_mfma_f32_16x16x32_bf16 v[132:135], a[88:91], a[40:43], v[132:135]\n"
    "v_exp_f32 v159, v159\n"
    "v_mfma_f32_16x16x32_bf16 v[132:135], a[92:95], a[44:47], v[132:135]\n"
    "v_cvt_pk_bf16_f32 v175, v158, v159\n"
    "ds_read_b128 a[124:127], v211 offset:12288\n"
    "v_mfma_f32_16x16x32_bf16 v[136:139], a[96:99], a[32:35], v[184:187]\n"
    "v_exp_f32 v128, v128\n"
    "v_mfma_f32_16x16x32_bf16 v[136:139], a[100:103], a[36:39], v[136:139]\n"
    "v_exp_f32 v129, v129\n"
    "s_waitcnt lgkmcnt(2)\n"
    "v_mfma_f32_16x16x32_bf16 v[136:139], a[104:107], a[40:43], v[136:139]\n"
    "v_cvt_pk_bf16_f32 v160, v128, v129\n"
    "s_add_u32 s82, s82, 4096\n"
    "v_mfma_f32_16x16x32_bf16 v[136:139], a[108:111], a[44:47], v[136:139]\n"
    "v_exp_f32 v130, v130\n"
    "v_mfma_f32_16x16x32_bf16 v[140:143], a[112:115], a[32:35], v[184:187]\n"
    "v_exp_f32 v131, v131\n"
    "v_mfma_f32_16x16x32_bf16 v[140:143], a[116:119], a[36:39], v[140:143]\n"
    "v_cvt_pk_bf16_f32 v161, v130, v131\n"
    "s_or_b32 s84, s94, s95\n"
    "s_waitcnt lgkmcnt(0)\n"
    "v_mfma_f32_16x16x32_bf16 v[140:143], a[120:123], a[40:43], v[140:143]\n"
    "v_exp_f32 v132, v132\n"
    "v_mfma_f32_16x16x32_bf16 v[140:143], a[124:127], a[44:47], v[140:143]\n"
    "v_exp_f32 v133, v133\n"
    "v_mfma_f32_16x16x32_bf16 v[96:99], a[192:195], v[168:171], v[96:99]\n"
    "v_mov_b32 v20, s84\n"
    "v_cvt_pk_bf16_f32 v162, v132, v133\n"
    "v_mfma_f32_16x16x32_bf16 v[96:99], a[196:199], v[172:175], v[96:99]\n"
    "s_lshl_b32 s84, s78, 2\n"
    "s_add_u32 s84, s84, 89600\n"
    "v_mfma_f32_16x16x32_bf16 v[100:103], a[200:203], v[168:171], v[100:103]\n"
    "v_exp_f32 v134, v134\n"
    "v_mov_b32 v21, s84\n"
    "ds_write_b32 v21, v20\n"
    "v_mfma_f32_16x16x32_bf16 v[100:103], a[204:207], v[172:175], v[100:103]\n"
    "v_readlane_b32 s81, v248, 0\n"
    "v_exp_f32 v135, v135\n"
    "v_mfma_f32_16x16x32_bf16 v[104:107], a[208:211], v[168:171], v[104:107]\n"
    "s_add_u32 m0, s83, 159744\n"
    "v_cvt_pk_bf16_f32 v163, v134, v135\n"
    "v_mfma_f32_16x16x32_bf16 v[104:107], a[212:215], v[172:175], v[104:107]\n"
    "v_exp_f32 v136, v136\n"
    "v_mfma_f32_16x16x32_bf16 v[108:111], a[216:219], v[168:171], v[108:111]\n"
    "s_waitcnt lgkmcnt(0)\n"
    "s_barrier\n"
    "ds_read_b32 v250, v230\n"
    "v_mfma_f32_16x16x32_bf16 v[108:111], a[220:223], v[172:175], v[108:111]\n"
    "s_nop 0\n"
    "buffer_load_dwordx4 v225, s[52:55], s81 offen lds\n"
    "v_mfma_f32_16x16x32_bf16 v[112:115], a[224:227], v[168:171], v[112:115]\n"
    "v_exp_f32 v137, v137\n"
    "v_mfma_f32_16x16x32_bf16 v[112:115], a[228:231], v[172:175], v[112:115]\n"
    "s_add_u32 m0, s73, 122880\n"
    "s_nop 0\n"
    "v_mfma_f32_16x16x32_bf16 v[116:119], a[232:235], v[168:171], v[116:119]\n"
    "v_cvt_pk_bf16_f32 v164, v136, v137\n"
    "v_mfma_f32_16x16x32_bf16 v[116:119], a[236:239], v[172:175], v[116:119]\n"
    "buffer_load_dwordx4 v240, s[52:55], s81 offen offset:0  lds\n"
    "v_mfma_f32_16x16x32_bf16 v[120:123], a[240:243], v[168:171], v[120:123]\n"
    "v_exp_f32 v138, v138\n"
    "v_mfma_f32_16x16x32_bf16 v[120:123], a[244:247], v[172:175], v[120:123]\n"
    "v_exp_f32 v139, v139\n"
    "v_mfma_f32_16x16x32_bf16 v[124:127], a[248:251], v[168:171], v[124:127]\n"
    "s_waitcnt lgkmcnt(0)\n"
    "v_cmp_lt_u32 vcc, s65, v250\n"
    "s_nop 1\n"
    "s_cmp_lg_u64 vcc, 0\n"
    "s_cbranch_scc1 FALLBACK%=\n"
    "v_mfma_f32_16x16x32_bf16 v[124:127], a[252:255], v[172:175], v[124:127]\n"
    "buffer_load_dwordx4 v241, s[52:55], s81 offen offset:1024  lds\n"
    "v_smfmac_f32_16x16x64_bf16 v[204:207], v[220:223], v[168:175], v224\n"
    "v_cvt_pk_bf16_f32 v165, v138, v139\n"
    "ds_read_b64_tr_b16 a[192:193], v235 offset:0\n"
    "v_mfma_f32_16x16x32_bf16 v[144:147], a[64:67], a[48:51], v[188:191]\n"
    "v_exp_f32 v140, v140\n"
    "v_mfma_f32_16x16x32_bf16 v[144:147], a[68:71], a[52:55], v[144:147]\n"
    "v_exp_f32 v141, v141\n"
    "v_mfma_f32_16x16x32_bf16 v[144:147], a[72:75], a[56:59], v[144:147]\n"
    "v_cvt_pk_bf16_f32 v166, v140, v141\n"
    "ds_read_b64_tr_b16 a[194:195], v235 offset:4096\n"
    "v_mfma_f32_16x16x32_bf16 v[144:147], a[76:79], a[60:63], v[144:147]\n"
    "buffer_load_dwordx4 v242, s[52:55], s81 offen offset:2048  lds\n"
    "v_mfma_f32_16x16x32_bf16 v[148:151], a[80:83], a[48:51], v[188:191]\n"
    "v_exp_f32 v142, v142\n"
    "v_mfma_f32_16x16x32_bf16 v[148:151], a[84:87], a[52:55], v[148:151]\n"
    "v_exp_f32 v143, v143\n"
    "v_mfma_f32_16x16x32_bf16 v[148:151], a[88:91], a[56:59], v[148:151]\n"
    "v_cvt_pk_bf16_f32 v167, v142, v143\n"
    "ds_read_b64_tr_b16 a[196:197], v235 offset:8192\n"
    "v_mfma_f32_16x16x32_bf16 v[148:151], a[92:95], a[60:63], v[148:151]\n"
    "buffer_load_dwordx4 v243, s[52:55], s81 offen offset:3072  lds\n"
    "v_mfma_f32_16x16x32_bf16 v[152:155], a[96:99], a[48:51], v[188:191]\n"
    "s_add_u32 m0, s73, 126976\n"
    "v_exp_f32 v144, v144\n"
    "v_mfma_f32_16x16x32_bf16 v[152:155], a[100:103], a[52:55], v[152:155]\n"
    "s_add_u32 s81, s81, 18432\n"
    "v_exp_f32 v145, v145\n"
    "v_mfma_f32_16x16x32_bf16 v[152:155], a[104:107], a[56:59], v[152:155]\n"
    "v_cvt_pk_bf16_f32 v168, v144, v145\n"
    "ds_read_b64_tr_b16 a[198:199], v235 offset:12288\n"
    "v_mfma_f32_16x16x32_bf16 v[152:155], a[108:111], a[60:63], v[152:155]\n"
    "buffer_load_dwordx4 v240, s[52:55], s81 offen offset:0  lds\n"
    "v_mfma_f32_16x16x32_bf16 v[156:159], a[112:115], a[48:51], v[188:191]\n"
    "v_exp_f32 v146, v146\n"
    "v_mfma_f32_16x16x32_bf16 v[156:159], a[116:119], a[52:55], v[156:159]\n"
    "v_exp_f32 v147, v147\n"
    "v_mfma_f32_16x16x32_bf16 v[156:159], a[120:123], a[56:59], v[156:159]\n"
    "v_cvt_pk_bf16_f32 v169, v146, v147\n"
    "ds_read_b64_tr_b16 a[200:201], v235 offset:16384\n"
    "v_mfma_f32_16x16x32_bf16 v[156:159], a[124:127], a[60:63], v[156:159]\n"
    "v_exp_f32 v148, v148\n"
    "v_mfma_f32_16x16x32_bf16 v[64:67], a[128:131], v[160:163], v[64:67]\n"
    "buffer_load_dwordx4 v241, s[52:55], s81 offen offset:1024  lds\n"
    "v_mfma_f32_16x16x32_bf16 v[64:67], a[132:135], v[164:167], v[64:67]\n"
    "ds_read_b64_tr_b16 a[202:203], v235 offset:20480\n"
    "ds_read_b64_tr_b16 a[204:205], v235 offset:24576\n"
    "v_mfma_f32_16x16x32_bf16 v[68:71], a[136:139], v[160:163], v[68:71]\n"
    "v_exp_f32 v149, v149\n"
    "v_mfma_f32_16x16x32_bf16 v[68:71], a[140:143], v[164:167], v[68:71]\n"
    "v_cvt_pk_bf16_f32 v170, v148, v149\n"
    "ds_read_b64_tr_b16 a[206:207], v235 offset:28672\n"
    "v_mfma_f32_16x16x32_bf16 v[72:75], a[144:147], v[160:163], v[72:75]\n"
    "buffer_load_dwordx4 v242, s[52:55], s81 offen offset:2048  lds\n"
    "v_mfma_f32_16x16x32_bf16 v[72:75], a[148:151], v[164:167], v[72:75]\n"
    "v_exp_f32 v150, v150\n"
    "v_mfma_f32_16x16x32_bf16 v[76:79], a[152:155], v[160:163], v[76:79]\n"
    "ds_read_b64_tr_b16 a[208:209], v236 offset:0\n"
    "ds_read_b64_tr_b16 a[210:211], v236 offset:4096\n"
    "v_mfma_f32_16x16x32_bf16 v[76:79], a[156:159], v[164:167], v[76:79]\n"
    "v_exp_f32 v151, v151\n"
    "v_mfma_f32_16x16x32_bf16 v[80:83], a[160:163], v[160:163], v[80:83]\n"
    "buffer_load_dwordx4 v243, s[52:55], s81 offen offset:3072  lds\n"
    "v_mfma_f32_16x16x32_bf16 v[80:83], a[164:167], v[164:167], v[80:83]\n"
    "v_cvt_pk_bf16_f32 v171, v150, v151\n"
    "ds_read_b64_tr_b16 a[212:213], v236 offset:8192\n"
    "v_mfma_f32_16x16x32_bf16 v[84:87], a[168:171], v[160:163], v[84:87]\n"
    "v_exp_f32 v152, v152\n"
    "v_mfma_f32_16x16x32_bf16 v[84:87], a[172:175], v[164:167], v[84:87]\n"
    "ds_read_b64_tr_b16 a[214:215], v236 offset:12288\n"
    "ds_read_b64_tr_b16 a[216:217], v236 offset:16384\n"
    "v_mfma_f32_16x16x32_bf16 v[88:91], a[176:179], v[160:163], v[88:91]\n"
    "v_exp_f32 v153, v153\n"
    "v_mfma_f32_16x16x32_bf16 v[88:91], a[180:183], v[164:167], v[88:91]\n"
    "v_cvt_pk_bf16_f32 v172, v152, v153\n"
    "ds_read_b64_tr_b16 a[218:219], v236 offset:20480\n"
    "v_mfma_f32_16x16x32_bf16 v[92:95], a[184:187], v[160:163], v[92:95]\n"
    "s_waitcnt vmcnt(9)\n"
    "s_barrier\n"
    "buffer_store_dword v253, v234, s[56:59], s86 offen sc1\n"
    "v_mfma_f32_16x16x32_bf16 v[92:95], a[188:191], v[164:167], v[92:95]\n"
    "v_exp_f32 v154, v154\n"
    "v_smfmac_f32_16x16x64_bf16 v[200:203], v[220:223], v[160:167], v224\n"
    "ds_read_b64_tr_b16 a[220:221], v236 offset:24576\n"
    "ds_read_b64_tr_b16 a[222:223], v236 offset:28672\n"
    "s_waitcnt lgkmcnt(3)\n"
    "v_mfma_f32_16x16x32_bf16 v[128:131], a[192:195], v[0:3], 0\n"
    "v_exp_f32 v155, v155\n"
    "v_mfma_f32_16x16x32_bf16 v[128:131], a[196:199], v[8:11], v[128:131]\n"
    "v_cvt_pk_bf16_f32 v173, v154, v155\n"
    "ds_read_b64_tr_b16 a[224:225], v235 offset:256\n"
    "v_mfma_f32_16x16x32_bf16 v[128:131], a[200:203], v[16:19], v[128:131]\n"
    "v_exp_f32 v156, v156\n"
    "v_mfma_f32_16x16x32_bf16 v[128:131], a[204:207], v[24:27], v[128:131]\n"
    "s_nop 0\n"
    "v_add_f32 v200, v200, v201\n"
    "v_rcp_f32 v232, v200\n"
    "v_cmp_ngt_f32 vcc, 1.0, v200\n"
    "s_or_b64 s[94:95], s[94:95], vcc\n"
    "v_mfma_f32_16x16x32_bf16 v[132:135], a[192:195], v[32:35], 0\n"
    "v_mul_f32 v64, v64, v232\n"
    "v_mul_f32 v65, v65, v232\n"
    "v_mfma_f32_16x16x32_bf16 v[132:135], a[196:199], v[40:43], v[132:135]\n"
    "v_exp_f32 v157, v157\n"
    "v_mfma_f32_16x16x32_bf16 v[132:135], a[200:203], v[48:51], v[132:135]\n"
    "v_mul_f32 v66, v66, v232\n"
    "v_mul_f32 v67, v67, v232\n"
    "v_mfma_f32_16x16x32_bf16 v[132:135], a[204:207], v[56:59], v[132:135]\n"
    "v_mul_f32 v68, v68, v232\n"
    "v_mul_f32 v69, v69, v232\n"
    "v_mfma_f32_16x16x32_bf16 v[136:139], a[208:211], v[0:3], 0\n"
    "v_cvt_pk_bf16_f32 v174, v156, v157\n"
    "v_mul_f32 v70, v70, v232\n"
    "v_mul_f32 v71, v71, v232\n"
    "v_mfma_f32_16x16x32_bf16 v[136:139], a[212:215], v[8:11], v[136:139]\n"
    "v_max3_f32 v202, |v64|, |v67|, |v70|\n"
    "v_cmp_gt_f32 vcc, 0x33000000, v202\n"
    "s_or_b64 s[94:95], s[94:95], vcc\n"
    "s_waitcnt lgkmcnt(0)\n"
    "v_mfma_f32_16x16x32_bf16 v[136:139], a[216:219], v[16:19], v[136:139]\n"
    "v_exp_f32 v158, v158\n"
    "v_mfma_f32_16x16x32_bf16 v[136:139], a[220:223], v[24:27], v[136:139]\n"
    "v_cvt_pk_bf16_f32 v64, v64, v65\n"
    "v_cvt_pk_bf16_f32 v65, v66, v67\n"
    "v_cvt_pk_bf16_f32 v66, v68, v69\n"
    "v_mfma_f32_16x16x32_bf16 v[140:143], a[208:211], v[32:35], 0\n"
    "v_exp_f32 v159, v159\n"
    "v_cvt_pk_bf16_f32 v67, v70, v71\n"
    "v_mfma_f32_16x16x32_bf16 v[140:143], a[212:215], v[40:43], v[140:143]\n"
    "buffer_store_dwordx4 v[64:67], v244, s[56:59], s82 offen offset:0 sc1\n"
    "v_mfma_f32_16x16x32_bf16 v[140:143], a[216:219], v[48:51], v[140:143]\n"
    "v_cvt_pk_bf16_f32 v175, v158, v159\n"
    "v_mul_f32 v72, v72, v232\n"
    "v_mfma_f32_16x16x32_bf16 v[140:143], a[220:223], v[56:59], v[140:143]\n"
    "v_mul_f32 v73, v73, v232\n"
    "v_mul_f32 v74, v74, v232\n"
    "v_mfma_f32_16x16x32_bf16 v[96:99], a[128:131], v[168:171], v[96:99]\n"
    "v_mul_f32 v75, v75, v232\n"
    "v_mul_f32 v76, v76, v232\n"
    "v_mfma_f32_16x16x32_bf16 v[96:99], a[132:135], v[172:175], v[96:99]\n"
    "v_mul_f32 v77, v77, v232\n"
    "v_mul_f32 v78, v78, v232\n"
    "v_mul_f32 v79, v79, v232\n"
    "v_mfma_f32_16x16x32_bf16 v[100:103], a[136:139], v[168:171], v[100:103]\n"
    "v_cvt_pk_bf16_f32 v72, v72, v73\n"
    "v_cvt_pk_bf16_f32 v73, v74, v75\n"
    "v_mfma_f32_16x16x32_bf16 v[100:103], a[140:143], v[172:175], v[100:103]\n"
    "v_cvt_pk_bf16_f32 v74, v76, v77\n"
    "v_cvt_pk_bf16_f32 v75, v78, v79\n"
    "ds_read_b64_tr_b16 a[226:227], v235 offset:4352\n"
    "v_mfma_f32_16x16x32_bf16 v[104:107], a[144:147], v[168:171], v[104:107]\n"
    "buffer_store_dwordx4 v[72:75], v244, s[56:59], s82 offen offset:1024 sc1\n"
    "v_mfma_f32_16x16x32_bf16 v[104:107], a[148:151], v[172:175], v[104:107]\n"
    "v_mul_f32 v80, v80, v232\n"
    "v_mul_f32 v81, v81, v232\n"
    "ds_read_b64_tr_b16 a[228:229], v235 offset:8448\n"
    "v_mfma_f32_16x16x32_bf16 v[108:111], a[152:155], v[168:171], v[108:111]\n"
    "v_mul_f32 v82, v82, v232\n"
    "v_mul_f32 v83, v83, v232\n"
    "ds_read_b64_tr_b16 a[230:231], v235 offset:12544\n"
    "v_mfma_f32_16x16x32_bf16 v[108:111], a[156:159], v[172:175], v[108:111]\n"
    "v_mul_f32 v84, v84, v232\n"
    "v_mul_f32 v85, v85, v232\n"
    "ds_read_b64_tr_b16 a[232:233], v235 offset:16640\n"
    "v_mfma_f32_16x16x32_bf16 v[112:115], a[160:163], v[168:171], v[112:115]\n"
    "v_mul_f32 v86, v86, v232\n"
    "v_mul_f32 v87, v87, v232\n"
    "ds_read_b64_tr_b16 a[234:235], v235 offset:20736\n"
    "v_mfma_f32_16x16x32_bf16 v[112:115], a[164:167], v[172:175], v[112:115]\n"
    "v_cvt_pk_bf16_f32 v80, v80, v81\n"
    "v_cvt_pk_bf16_f32 v81, v82, v83\n"
    "ds_read_b64_tr_b16 a[236:237], v235 offset:24832\n"
    "v_mfma_f32_16x16x32_bf16 v[116:119], a[168:171], v[168:171], v[116:119]\n"
    "v_cvt_pk_bf16_f32 v82, v84, v85\n"
    "v_cvt_pk_bf16_f32 v83, v86, v87\n"
    "ds_read_b64_tr_b16 a[238:239], v235 offset:28928\n"
    "v_mfma_f32_16x16x32_bf16 v[116:119], a[172:175], v[172:175], v[116:119]\n"
    "buffer_store_dwordx4 v[80:83], v244, s[56:59], s82 offen offset:2048 sc1\n"
    "v_mfma_f32_16x16x32_bf16 v[120:123], a[176:179], v[168:171], v[120:123]\n"
    "ds_read_b64_tr_b16 a[240:241], v236 offset:256\n"
    "ds_read_b64_tr_b16 a[242:243], v236 offset:4352\n"
    "v_mfma_f32_16x16x32_bf16 v[120:123], a[180:183], v[172:175], v[120:123]\n"
    "v_mul_f32 v88, v88, v232\n"
    "v_mul_f32 v89, v89, v232\n"
    "ds_read_b64_tr_b16 a[244:245], v236 offset:8448\n"
    "v_mfma_f32_16x16x32_bf16 v[124:127], a[184:187], v[168:171], v[124:127]\n"
    "v_mul_f32 v90, v90, v232\n"
    "v_mul_f32 v91, v91, v232\n"
    "ds_read_b64_tr_b16 a[246:247], v236 offset:12544\n"
    "v_mfma_f32_16x16x32_bf16 v[124:127], a[188:191], v[172:175], v[124:127]\n"
    "v_mul_f32 v92, v92, v232\n"
    "v_mul_f32 v93, v93, v232\n"
    "ds_read_b64_tr_b16 a[248:249], v236 offset:16640\n"
    "v_smfmac_f32_16x16x64_bf16 v[204:207], v[220:223], v[168:175], v224\n"
    "v_mul_f32 v94, v94, v232\n"
    "v_mul_f32 v95, v95, v232\n"
    "v_cvt_pk_bf16_f32 v88, v88, v89\n"
    "ds_read_b64_tr_b16 a[250:251], v236 offset:20736\n"
    "s_waitcnt lgkmcnt(3)\n"
    "v_mfma_f32_16x16x32_bf16 v[144:147], a[224:227], v[0:3], 0\n"
    "v_cvt_pk_bf16_f32 v89, v90, v91\n"
    "v_cvt_pk_bf16_f32 v90, v92, v93\n"
    "v_cvt_pk_bf16_f32 v91, v94, v95\n"
    "ds_read_b64_tr_b16 a[252:253], v236 offset:24832\n"
    "v_mfma_f32_16x16x32_bf16 v[144:147], a[228:231], v[8:11], v[144:147]\n"
    "buffer_store_dwordx4 v[88:91], v244, s[56:59], s82 offen offset:3072 sc1\n"
    "v_mfma_f32_16x16x32_bf16 v[144:147], a[232:235], v[16:19], v[144:147]\n"
    "ds_read_b64_tr_b16 a[254:255], v236 offset:28928\n"
    "ds_read_b64_tr_b16 a[128:129], v235 offset:512\n"
    "v_mfma_f32_16x16x32_bf16 v[144:147], a[236:239], v[24:27], v[144:147]\n"
    "ds_read_b64_tr_b16 a[130:131], v235 offset:4608\n"
    "ds_read_b64_tr_b16 a[132:133], v235 offset:8704\n"
    "v_mfma_f32_16x16x32_bf16 v[148:151], a[224:227], v[32:35], 0\n"
    "buffer_load_dword v251, v229, s[56:59], s85 offen sc1\n"
    "v_mfma_f32_16x16x32_bf16 v[148:151], a[228:231], v[40:43], v[148:151]\n"
    "s_add_u32 s82, s82, 4096\n"
    "v_add_f32 v204, v204, v205\n"
    "ds_read_b64_tr_b16 a[134:135], v235 offset:12800\n"
    "v_mfma_f32_16x16x32_bf16 v[148:151], a[232:235], v[48:51], v[148:151]\n"
    "v_rcp_f32 v233, v204\n"
    "ds_read_b64_tr_b16 a[136:137], v235 offset:16896\n"
    "v_mfma_f32_16x16x32_bf16 v[148:151], a[236:239], v[56:59], v[148:151]\n"
    "v_cmp_ngt_f32 vcc, 1.0, v204\n"
    "s_or_b64 s[94:95], s[94:95], vcc\n"
    "v_mfma_f32_16x16x32_bf16 v[152:155], a[240:243], v[0:3], 0\n"
    "v_mul_f32 v96, v96, v233\n"
    "v_mul_f32 v97, v97, v233\n"
    "ds_read_b64_tr_b16 a[138:139], v235 offset:20992\n"
    "s_waitcnt lgkmcnt(2)\n"
    "v_mfma_f32_16x16x32_bf16 v[152:155], a[244:247], v[8:11], v[152:155]\n"
    "v_mul_f32 v98, v98, v233\n"
    "v_mul_f32 v99, v99, v233\n"
    "v_mul_f32 v100, v100, v233\n"
    "ds_read_b64_tr_b16 a[140:141], v235 offset:25088\n"
    "v_mfma_f32_16x16x32_bf16 v[152:155], a[248:251], v[16:19], v[152:155]\n"
    "v_mul_f32 v101, v101, v233\n"
    "v_mul_f32 v102, v102, v233\n"
    "v_mul_f32 v103, v103, v233\n"
    "ds_read_b64_tr_b16 a[142:143], v235 offset:29184\n"
    "v_mfma_f32_16x16x32_bf16 v[152:155], a[252:255], v[24:27], v[152:155]\n"
    "v_max3_f32 v206, |v96|, |v99|, |v102|\n"
    "v_cmp_gt_f32 vcc, 0x33000000, v206\n"
    "s_or_b64 s[94:95], s[94:95], vcc\n"
    "v_cvt_pk_bf16_f32 v96, v96, v97\n"
    "v_mfma_f32_16x16x32_bf16 v[156:159], a[240:243], v[32:35], 0\n"
    "v_cvt_pk_bf16_f32 v97, v98, v99\n"
    "v_cvt_pk_bf16_f32 v98, v100, v101\n"
    "v_cvt_pk_bf16_f32 v99, v102, v103\n"
    "ds_read_b64_tr_b16 a[144:145], v236 offset:512\n"
    "v_mfma_f32_16x16x32_bf16 v[156:159], a[244:247], v[40:43], v[156:159]\n"
    "buffer_store_dwordx4 v[96:99], v244, s[56:59], s82 offen offset:0 sc1\n"
    "v_mfma_f32_16x16x32_bf16 v[156:159], a[248:251], v[48:51], v[156:159]\n"
    "ds_read_b64_tr_b16 a[146:147], v236 offset:4608\n"
    "ds_read_b64_tr_b16 a[148:149], v236 offset:8704\n"
    "v_mfma_f32_16x16x32_bf16 v[156:159], a[252:255], v[56:59], v[156:159]\n"
    "ds_read_b64_tr_b16 a[150:151], v236 offset:12800\n"
    "ds_read_b64_tr_b16 a[152:153], v236 offset:16896\n"
    "v_mfma_f32_16x16x32_bf16 v[160:163], a[128:131], v[0:3], 0\n"
    "v_mul_f32 v104, v104, v233\n"
    "v_mul_f32 v105, v105, v233\n"
    "ds_read_b64_tr_b16 a[154:155], v236 offset:20992\n"
    "v_mfma_f32_16x16x32_bf16 v[160:163], a[132:135], v[8:11], v[160:163]\n"
    "v_mul_f32 v106, v106, v233\n"
    "v_mul_f32 v107, v107, v233\n"
    "v_mul_f32 v108, v108, v233\n"
    "ds_read_b64_tr_b16 a[156:157], v236 offset:25088\n"
    "s_waitcnt lgkmcnt(3)\n"
    "v_mfma_f32_16x16x32_bf16 v[160:163], a[136:139], v[16:19], v[160:163]\n"
    "v_mul_f32 v109, v109, v233\n"
    "v_mul_f32 v110, v110, v233\n"
    "v_mul_f32 v111, v111, v233\n"
    "ds_read_b64_tr_b16 a[158:159], v236 offset:29184\n"
    "v_mfma_f32_16x16x32_bf16 v[160:163], a[140:143], v[24:27], v[160:163]\n"
    "v_cvt_pk_bf16_f32 v104, v104, v105\n"
    "v_cvt_pk_bf16_f32 v105, v106, v107\n"
    "v_cvt_pk_bf16_f32 v106, v108, v109\n"
    "v_cvt_pk_bf16_f32 v107, v110, v111\n"
    "v_mfma_f32_16x16x32_bf16 v[164:167], a[128:131], v[32:35], 0\n"
    "buffer_store_dwordx4 v[104:107], v244, s[56:59], s82 offen offset:1024 sc1\n"
    "v_mfma_f32_16x16x32_bf16 v[164:167], a[132:135], v[40:43], v[164:167]\n"
    "ds_read_b64_tr_b16 a[160:161], v235 offset:768\n"
    "ds_read_b64_tr_b16 a[162:163], v235 offset:4864\n"
    "v_mfma_f32_16x16x32_bf16 v[164:167], a[136:139], v[48:51], v[164:167]\n"
    "ds_read_b64_tr_b16 a[164:165], v235 offset:8960\n"
    "ds_read_b64_tr_b16 a[166:167], v235 offset:13056\n"
    "v_mfma_f32_16x16x32_bf16 v[164:167], a[140:143], v[56:59], v[164:167]\n"
    "v_mul_f32 v112, v112, v233\n"
    "v_mul_f32 v113, v113, v233\n"
    "ds_read_b64_tr_b16 a[168:169], v235 offset:17152\n"
    "v_mfma_f32_16x16x32_bf16 v[168:171], a[144:147], v[0:3], 0\n"
    "v_mul_f32 v114, v114, v233\n"
    "v_mul_f32 v115, v115, v233\n"
    "v_mul_f32 v116, v116, v233\n"
    "ds_read_b64_tr_b16 a[170:171], v235 offset:21248\n"
    "v_mfma_f32_16x16x32_bf16 v[168:171], a[148:151], v[8:11], v[168:171]\n"
    "v_mul_f32 v117, v117, v233\n"
    "v_mul_f32 v118, v118, v233\n"
    "v_mul_f32 v119, v119, v233\n"
    "ds_read_b64_tr_b16 a[172:173], v235 offset:25344\n"
    "s_waitcnt lgkmcnt(2)\n"
    "v_mfma_f32_16x16x32_bf16 v[168:171], a[152:155], v[16:19], v[168:171]\n"
    "v_cvt_pk_bf16_f32 v112, v112, v113\n"
    "v_cvt_pk_bf16_f32 v113, v114, v115\n"
    "v_cvt_pk_bf16_f32 v114, v116, v117\n"
    "v_cvt_pk_bf16_f32 v115, v118, v119\n"
    "ds_read_b64_tr_b16 a[174:175], v235 offset:29440\n"
    "v_mfma_f32_16x16x32_bf16 v[168:171], a[156:159], v[24:27], v[168:171]\n"
    "buffer_store_dwordx4 v[112:115], v244, s[56:59], s82 offen offset:2048 sc1\n"
    "v_mfma_f32_16x16x32_bf16 v[172:175], a[144:147], v[32:35], 0\n"
    "ds_read_b64_tr_b16 a[176:177], v236 offset:768\n"
    "ds_read_b64_tr_b16 a[178:179], v236 offset:4864\n"
    "v_mfma_f32_16x16x32_bf16 v[172:175], a[148:151], v[40:43], v[172:175]\n"
    "v_mul_f32 v120, v120, v233\n"
    "ds_read_b64_tr_b16 a[180:181], v236 offset:8960\n"
    "v_mfma_f32_16x16x32_bf16 v[172:175], a[152:155], v[48:51], v[172:175]\n"
    "v_mul_f32 v121, v121, v233\n"
    "v_mul_f32 v122, v122, v233\n"
    "v_mfma_f32_16x16x32_bf16 v[172:175], a[156:159], v[56:59], v[172:175]\n"
    "v_mul_f32 v123, v123, v233\n"
    "ds_read_b64_tr_b16 a[182:183], v236 offset:13056\n"
    "v_mfma_f32_16x16x32_bf16 v[176:179], a[160:163], v[0:3], 0\n"
    "v_mul_f32 v124, v124, v233\n"
    "v_mul_f32 v125, v125, v233\n"
    "v_mul_f32 v126, v126, v233\n"
    "v_mul_f32 v127, v127, v233\n"
    "ds_read_b64_tr_b16 a[184:185], v236 offset:17152\n"
    "v_mfma_f32_16x16x32_bf16 v[176:179], a[164:167], v[8:11], v[176:179]\n"
    "v_cvt_pk_bf16_f32 v120, v120, v121\n"
    "v_cvt_pk_bf16_f32 v121, v122, v123\n"
    "v_cvt_pk_bf16_f32 v122, v124, v125\n"
    "v_cvt_pk_bf16_f32 v123, v126, v127\n"
    "ds_read_b64_tr_b16 a[186:187], v236 offset:21248\n"
    "s_waitcnt lgkmcnt(2)\n"
    "v_mfma_f32_16x16x32_bf16 v[176:179], a[168:171], v[16:19], v[176:179]\n"
    "buffer_store_dwordx4 v[120:123], v244, s[56:59], s82 offen offset:3072 sc1\n"
    "v_mfma_f32_16x16x32_bf16 v[176:179], a[172:175], v[24:27], v[176:179]\n"
    "ds_read_b64_tr_b16 a[188:189], v236 offset:25344\n"
    "ds_read_b64_tr_b16 a[190:191], v236 offset:29440\n"
    "v_mfma_f32_16x16x32_bf16 v[180:183], a[160:163], v[32:35], 0\n"
    "ds_read_b64_tr_b16 a[112:113], v226 offset:0\n"
    "ds_read_b64_tr_b16 a[114:115], v226 offset:512\n"
    "v_mfma_f32_16x16x32_bf16 v[180:183], a[164:167], v[40:43], v[180:183]\n"
    "ds_read_b64_tr_b16 a[116:117], v226 offset:1024\n"
    "ds_read_b64_tr_b16 a[118:119], v226 offset:1536\n"
    "v_mfma_f32_16x16x32_bf16 v[180:183], a[168:171], v[48:51], v[180:183]\n"
    "ds_read_b64_tr_b16 a[120:121], v226 offset:2048\n"
    "ds_read_b64_tr_b16 a[122:123], v226 offset:2560\n"
    "v_mfma_f32_16x16x32_bf16 v[180:183], a[172:175], v[56:59], v[180:183]\n"
    "ds_read_b64_tr_b16 a[124:125], v226 offset:3072\n"
    "ds_read_b64_tr_b16 a[126:127], v226 offset:3584\n"
    "v_mfma_f32_16x16x32_bf16 v[184:187], a[176:179], v[0:3], 0\n"
    "s_or_b32 s84, s94, s95\n"
    "v_mov_b32 v20, s84\n"
    "v_mfma_f32_16x16x32_bf16 v[184:187], a[180:183], v[8:11], v[184:187]\n"
    "s_lshl_b32 s84, s78, 2\n"
    "ds_read_b64_tr_b16 a[192:193], v237 offset:0\n"
    "s_waitcnt lgkmcnt(4)\n"
    "v_mfma_f32_16x16x32_bf16 v[184:187], a[184:187], v[16:19], v[184:187]\n"
    "s_add_u32 s84, s84, 89600\n"
    "v_mov_b32 v21, s84\n"
    "v_mfma_f32_16x16x32_bf16 v[184:187], a[188:191], v[24:27], v[184:187]\n"
    "ds_write_b32 v21, v20\n"
    "ds_read_b64_tr_b16 a[194:195], v237 offset:4096\n"
    "v_mfma_f32_16x16x32_bf16 v[188:191], a[176:179], v[32:35], 0\n"
    "ds_read_b64_tr_b16 a[196:197], v237 offset:8192\n"
    "ds_read_b64_tr_b16 a[198:199], v237 offset:12288\n"
    "v_mfma_f32_16x16x32_bf16 v[188:191], a[180:183], v[40:43], v[188:191]\n"
    "ds_read_b64_tr_b16 a[200:201], v237 offset:16384\n"
    "ds_read_b64_tr_b16 a[202:203], v237 offset:20480\n"
    "v_mfma_f32_16x16x32_bf16 v[188:191], a[184:187], v[48:51], v[188:191]\n"
    "ds_read_b64_tr_b16 a[204:205], v237 offset:24576\n"
    "ds_read_b64_tr_b16 a[206:207], v237 offset:28672\n"
    "v_mfma_f32_16x16x32_bf16 v[188:191], a[188:191], v[56:59], v[188:191]\n"
    "ds_read_b64_tr_b16 a[208:209], v238 offset:0\n"
    "ds_read_b64_tr_b16 a[210:211], v238 offset:4096\n"
    "v_mfma_f32_16x16x32_bf16 v[192:195], a[112:115], v[0:3], 0\n"
    "ds_read_b64_tr_b16 a[212:213], v238 offset:8192\n"
    "ds_read_b64_tr_b16 a[214:215], v238 offset:12288\n"
    "v_mfma_f32_16x16x32_bf16 v[192:195], a[116:119], v[8:11], v[192:195]\n"
    "ds_read_b64_tr_b16 a[216:217], v238 offset:16384\n"
    "ds_read_b64_tr_b16 a[218:219], v238 offset:20480\n"
    "s_waitcnt lgkmcnt(7)\n"
    "v_mfma_f32_16x16x32_bf16 v[192:195], a[120:123], v[16:19], v[192:195]\n"
    "s_waitcnt vmcnt(10)\n"
    "s_barrier\n"
    "ds_read_b32 v250, v230\n"
    "v_mfma_f32_16x16x32_bf16 v[192:195], a[124:127], v[24:27], v[192:195]\n"
    "ds_read_b64_tr_b16 a[220:221], v238 offset:24576\n"
    "ds_read_b64_tr_b16 a[222:223], v238 offset:28672\n"
    "v_mfma_f32_16x16x32_bf16 v[196:199], a[112:115], v[32:35], 0\n"
    "ds_read_b64_tr_b16 a[224:225], v237 offset:256\n"
    "ds_read_b64_tr_b16 a[226:227], v237 offset:4352\n"
    "v_mfma_f32_16x16x32_bf16 v[196:199], a[116:119], v[40:43], v[196:199]\n"
    "ds_read_b64_tr_b16 a[228:229], v237 offset:8448\n"
    "ds_read_b64_tr_b16 a[230:231], v237 offset:12544\n"
    "v_mfma_f32_16x16x32_bf16 v[196:199], a[120:123], v[48:51], v[196:199]\n"
    "ds_read_b64_tr_b16 a[232:233], v237 offset:16640\n"
    "ds_read_b64_tr_b16 a[234:235], v237 offset:20736\n"
    "v_mfma_f32_16x16x32_bf16 v[196:199], a[124:127], v[56:59], v[196:199]\n"
    "ds_read_b64_tr_b16 a[236:237], v237 offset:24832\n"
    "ds_read_b64_tr_b16 a[238:239], v237 offset:28928\n"
    "v_mfma_f32_16x16x32_bf16 v[128:131], a[192:195], v[64:67], v[128:131]\n"
    "s_waitcnt lgkmcnt(6)\n"
    "v_cmp_lt_u32 vcc, s65, v250\n"
    "s_nop 1\n"
    "s_cmp_lg_u64 vcc, 0\n"
    "s_cbranch_scc1 FALLBACK%=\n"
    "s_waitcnt vmcnt(4)\n"
    "v_cmp_eq_u32 vcc, 0, v251\n"
    "s_nop 1\n"
    "s_cmp_lg_u64 vcc, 0\n"
    "s_cbranch_scc1 OSA%=\n"
    "ODA%=:\n"
    "v_mfma_f32_16x16x32_bf16 v[128:131], a[196:199], v[72:75], v[128:131]\n"
    "buffer_load_dwordx4 a[0:3], v244, s[56:59], s72 offen offset:0 sc1 \n"
    "v_mfma_f32_16x16x32_bf16 v[128:131], a[200:203], v[80:83], v[128:131]\n"
    "s_barrier\n"
    "v_mfma_f32_16x16x32_bf16 v[128:131], a[204:207], v[88:91], v[128:131]\n"
    "ds_read_b64_tr_b16 a[240:241], v238 offset:256\n"
    "ds_read_b64_tr_b16 a[242:243], v238 offset:4352\n"
    "v_mfma_f32_16x16x32_bf16 v[132:135], a[192:195], v[96:99], v[132:135]\n"
    "buffer_load_dwordx4 a[16:19], v227, s[56:59], s72 offen offset:0 sc1 \n"
    "v_mfma_f32_16x16x32_bf16 v[132:135], a[196:199], v[104:107], v[132:135]\n"
    "ds_read_b64_tr_b16 a[244:245], v238 offset:8448\n"
    "ds_read_b64_tr_b16 a[246:247], v238 offset:12544\n"
    "v_mfma_f32_16x16x32_bf16 v[132:135], a[200:203], v[112:115], v[132:135]\n"
    "ds_read_b64_tr_b16 a[248:249], v238 offset:16640\n"
    "ds_read_b64_tr_b16 a[250:251], v238 offset:20736\n"
    "v_mfma_f32_16x16x32_bf16 v[132:135], a[204:207], v[120:123], v[132:135]\n"
    "buffer_load_dwordx4 a[4:7], v244, s[56:59], s74 offen offset:0 sc1 \n"
    "v_mfma_f32_16x16x32_bf16 v[136:139], a[208:211], v[64:67], v[136:139]\n"
    "ds_read_b64_tr_b16 a[252:253], v238 offset:24832\n"
    "ds_read_b64_tr_b16 a[254:255], v238 offset:28928\n"
    "v_mfma_f32_16x16x32_bf16 v[136:139], a[212:215], v[72:75], v[136:139]\n"
    "ds_read_b64_tr_b16 a[128:129], v237 offset:512\n"
    "ds_read_b64_tr_b16 a[130:131], v237 offset:4608\n"
    "v_mfma_f32_16x16x32_bf16 v[136:139], a[216:219], v[80:83], v[136:139]\n"
    "buffer_load_dwordx4 a[20:23], v227, s[56:59], s74 offen offset:0 sc1 \n"
    "v_mfma_f32_16x16x32_bf16 v[136:139], a[220:223], v[88:91], v[136:139]\n"
    "ds_read_b64_tr_b16 a[132:133], v237 offset:8704\n"
    "ds_read_b64_tr_b16 a[134:135], v237 offset:12800\n"
    "v_mfma_f32_16x16x32_bf16 v[140:143], a[208:211], v[96:99], v[140:143]\n"
    "ds_read_b64_tr_b16 a[136:137], v237 offset:16896\n"
    "ds_read_b64_tr_b16 a[138:139], v237 offset:20992\n"
    "v_mfma_f32_16x16x32_bf16 v[140:143], a[212:215], v[104:107], v[140:143]\n"
    "buffer_load_dwordx4 a[8:11], v244, s[56:59], s75 offen offset:0 sc1 \n"
    "v_mfma_f32_16x16x32_bf16 v[140:143], a[216:219], v[112:115], v[140:143]\n"
    "ds_read_b64_tr_b16 a[140:141], v237 offset:25088\n"
    "ds_read_b64_tr_b16 a[142:143], v237 offset:29184\n"
    "v_mfma_f32_16x16x32_bf16 v[140:143], a[220:223], v[120:123], v[140:143]\n"
    "ds_read_b64_tr_b16 a[144:145], v238 offset:512\n"
    "ds_read_b64_tr_b16 a[146:147], v238 offset:4608\n"
    "v_mfma_f32_16x16x32_bf16 v[144:147], a[224:227], v[64:67], v[144:147]\n"
    "buffer_load_dwordx4 a[24:27], v227, s[56:59], s75 offen offset:0 sc1 \n"
    "s_waitcnt lgkmcnt(4)\n"
    "v_mfma_f32_16x16x32_bf16 v[144:147], a[228:231], v[72:75], v[144:147]\n"
    "ds_read_b64_tr_b16 a[148:149], v238 offset:8704\n"
    "ds_read_b64_tr_b16 a[150:151], v238 offset:12800\n"
    "v_mfma_f32_16x16x32_bf16 v[144:147], a[232:235], v[80:83], v[144:147]\n"
    "ds_read_b64_tr_b16 a[152:153], v238 offset:16896\n"
    "ds_read_b64_tr_b16 a[154:155], v238 offset:20992\n"
    "v_mfma_f32_16x16x32_bf16 v[144:147], a[236:239], v[88:91], v[144:147]\n"
    "buffer_load_dwordx4 a[12:15], v244, s[56:59], s72 offen offset:1024 sc1 \n"
    "v_mfma_f32_16x16x32_bf16 v[148:151], a[224:227], v[96:99], v[148:151]\n"
    "ds_read_b64_tr_b16 a[156:157], v238 offset:25088\n"
    "ds_read_b64_tr_b16 a[158:159], v238 offset:29184\n"
    "v_mfma_f32_16x16x32_bf16 v[148:151], a[228:231], v[104:107], v[148:151]\n"
    "v_mfma_f32_16x16x32_bf16 v[148:151], a[232:235], v[112:115], v[148:151]\n"
    "buffer_load_dwordx4 a[28:31], v227, s[56:59], s72 offen offset:1024 sc1 \n"
    "v_readlane_b32 s81, v248, 1\n"
    "v_mfma_f32_16x16x32_bf16 v[148:151], a[236:239], v[120:123], v[148:151]\n"
    "s_add_u32 m0, s83, 32768\n"
    "ds_read_b64_tr_b16 a[160:161], v237 offset:768\n"
    "v_mfma_f32_16x16x32_bf16 v[152:155], a[240:243], v[64:67], v[152:155]\n"
    "s_nop 0\n"
    "ds_read_b64_tr_b16 a[162:163], v237 offset:4864\n"
    "v_mfma_f32_16x16x32_bf16 v[152:155], a[244:247], v[72:75], v[152:155]\n"
    "buffer_load_dwordx4 v225, s[52:55], s81 offen lds\n"
    "v_mfma_f32_16x16x32_bf16 v[152:155], a[248:251], v[80:83], v[152:155]\n"
    "s_add_u32 m0, s73, 0\n"
    "s_nop 0\n"
    "v_mfma_f32_16x16x32_bf16 v[152:155], a[252:255], v[88:91], v[152:155]\n"
    "ds_read_b64_tr_b16 a[164:165], v237 offset:8960\n"
    "ds_read_b64_tr_b16 a[166:167], v237 offset:13056\n"
    "v_mfma_f32_16x16x32_bf16 v[156:159], a[240:243], v[96:99], v[156:159]\n"
    "ds_read_b64_tr_b16 a[168:169], v237 offset:17152\n"
    "ds_read_b64_tr_b16 a[170:171], v237 offset:21248\n"
    "v_mfma_f32_16x16x32_bf16 v[156:159], a[244:247], v[104:107], v[156:159]\n"
    "buffer_load_dwordx4 v240, s[52:55], s81 offen offset:0  lds\n"
    "v_mfma_f32_16x16x32_bf16 v[156:159], a[248:251], v[112:115], v[156:159]\n"
    "ds_read_b64_tr_b16 a[172:173], v237 offset:25344\n"
    "ds_read_b64_tr_b16 a[174:175], v237 offset:29440\n"
    "v_mfma_f32_16x16x32_bf16 v[156:159], a[252:255], v[120:123], v[156:159]\n"
    "ds_read_b64_tr_b16 a[176:177], v238 offset:768\n"
    "ds_read_b64_tr_b16 a[178:179], v238 offset:4864\n"
    "v_mfma_f32_16x16x32_bf16 v[160:163], a[128:131], v[64:67], v[160:163]\n"
    "ds_read_b64_tr_b16 a[180:181], v238 offset:8960\n"
    "ds_read_b64_tr_b16 a[182:183], v238 offset:13056\n"
    "v_mfma_f32_16x16x32_bf16 v[160:163], a[132:135], v[72:75], v[160:163]\n"
    "buffer_load_dwordx4 v241, s[52:55], s81 offen offset:1024  lds\n"
    "v_mfma_f32_16x16x32_bf16 v[160:163], a[136:139], v[80:83], v[160:163]\n"
    "ds_read_b64_tr_b16 a[184:185], v238 offset:17152\n"
    "ds_read_b64_tr_b16 a[186:187], v238 offset:21248\n"
    "s_waitcnt lgkmcnt(6)\n"
    "v_mfma_f32_16x16x32_bf16 v[160:163], a[140:143], v[88:91], v[160:163]\n"
    "ds_read_b64_tr_b16 a[188:189], v238 offset:25344\n"
    "ds_read_b64_tr_b16 a[190:191], v238 offset:29440\n"
    "v_mfma_f32_16x16x32_bf16 v[164:167], a[128:131], v[96:99], v[164:167]\n"
    "v_mfma_f32_16x16x32_bf16 v[164:167], a[132:135], v[104:107], v[164:167]\n"
    "buffer_load_dwordx4 v242, s[52:55], s81 offen offset:2048  lds\n"
    "v_mfma_f32_16x16x32_bf16 v[164:167], a[136:139], v[112:115], v[164:167]\n"
    "ds_read_b64_tr_b16 a[112:113], v245 offset:0\n"
    "ds_read_b64_tr_b16 a[114:115], v245 offset:512\n"
    "v_mfma_f32_16x16x32_bf16 v[164:167], a[140:143], v[120:123], v[164:167]\n"
    "ds_read_b64_tr_b16 a[116:117], v245 offset:1024\n"
    "ds_read_b64_tr_b16 a[118:119], v245 offset:1536\n"
    "v_mfma_f32_16x16x32_bf16 v[168:171], a[144:147], v[64:67], v[168:171]\n"
    "s_waitcnt vmcnt(12)\n"
    "s_barrier\n"
    "buffer_store_dword v253, v234, s[56:59], s86 offen offset:16 sc1\n"
    "v_mfma_f32_16x16x32_bf16 v[168:171], a[148:151], v[72:75], v[168:171]\n"
    "buffer_load_dwordx4 v243, s[52:55], s81 offen offset:3072  lds\n"
    "v_mfma_f32_16x16x32_bf16 v[168:171], a[152:155], v[80:83], v[168:171]\n"
    "s_add_u32 m0, s73, 4096\n"
    "s_add_u32 s81, s81, 18432\n"
    "v_mfma_f32_16x16x32_bf16 v[168:171], a[156:159], v[88:91], v[168:171]\n"
    "ds_read_b64_tr_b16 a[120:121], v245 offset:2048\n"
    "ds_read_b64_tr_b16 a[122:123], v245 offset:2560\n"
    "v_mfma_f32_16x16x32_bf16 v[172:175], a[144:147], v[96:99], v[172:175]\n"
    "ds_read_b64_tr_b16 a[124:125], v245 offset:3072\n"
    "ds_read_b64_tr_b16 a[126:127], v245 offset:3584\n"
    "v_mfma_f32_16x16x32_bf16 v[172:175], a[148:151], v[104:107], v[172:175]\n"
    "v_mfma_f32_16x16x32_bf16 v[172:175], a[152:155], v[112:115], v[172:175]\n"
    "buffer_load_dwordx4 v240, s[52:55], s81 offen offset:0  lds\n"
    "v_mfma_f32_16x16x32_bf16 v[172:175], a[156:159], v[120:123], v[172:175]\n"
    "ds_read_b64_tr_b16 a[192:193], v237 offset:32768\n"
    "ds_read_b64_tr_b16 a[194:195], v237 offset:36864\n"
    "v_mfma_f32_16x16x32_bf16 v[176:179], a[160:163], v[64:67], v[176:179]\n"
    "ds_read_b64_tr_b16 a[196:197], v237 offset:40960\n"
    "ds_read_b64_tr_b16 a[198:199], v237 offset:45056\n"
    "v_mfma_f32_16x16x32_bf16 v[176:179], a[164:167], v[72:75], v[176:179]\n"
    "v_mfma_f32_16x16x32_bf16 v[176:179], a[168:171], v[80:83], v[176:179]\n"
    "buffer_load_dwordx4 v241, s[52:55], s81 offen offset:1024  lds\n"
    "v_mfma_f32_16x16x32_bf16 v[176:179], a[172:175], v[88:91], v[176:179]\n"
    "ds_read_b64_tr_b16 a[200:201], v237 offset:49152\n"
    "ds_read_b64_tr_b16 a[202:203], v237 offset:53248\n"
    "v_mfma_f32_16x16x32_bf16 v[180:183], a[160:163], v[96:99], v[180:183]\n"
    "ds_read_b64_tr_b16 a[204:205], v237 offset:57344\n"
    "ds_read_b64_tr_b16 a[206:207], v237 offset:61440\n"
    "v_mfma_f32_16x16x32_bf16 v[180:183], a[164:167], v[104:107], v[180:183]\n"
    "v_mfma_f32_16x16x32_bf16 v[180:183], a[168:171], v[112:115], v[180:183]\n"
    "buffer_load_dwordx4 v242, s[52:55], s81 offen offset:2048  lds\n"
    "v_mfma_f32_16x16x32_bf16 v[180:183], a[172:175], v[120:123], v[180:183]\n"
    "ds_read_b64_tr_b16 a[208:209], v238 offset:32768\n"
    "ds_read_b64_tr_b16 a[210:211], v238 offset:36864\n"
    "s_waitcnt lgkmcnt(5)\n"
    "v_mfma_f32_16x16x32_bf16 v[184:187], a[176:179], v[64:67], v[184:187]\n"
    "ds_read_b64_tr_b16 a[212:213], v238 offset:40960\n"
    "ds_read_b64_tr_b16 a[214:215], v238 offset:45056\n"
    "v_mfma_f32_16x16x32_bf16 v[184:187], a[180:183], v[72:75], v[184:187]\n"
    "v_mfma_f32_16x16x32_bf16 v[184:187], a[184:187], v[80:83], v[184:187]\n"
    "buffer_load_dwordx4 v243, s[52:55], s81 offen offset:3072  lds\n"
    "v_mfma_f32_16x16x32_bf16 v[184:187], a[188:191], v[88:91], v[184:187]\n"
    "ds_read_b64_tr_b16 a[216:217], v238 offset:49152\n"
    "ds_read_b64_tr_b16 a[218:219], v238 offset:53248\n"
    "v_mfma_f32_16x16x32_bf16 v[188:191], a[176:179], v[96:99], v[188:191]\n"
    "ds_read_b64_tr_b16 a[220:221], v238 offset:57344\n"
    "ds_read_b64_tr_b16 a[222:223], v238 offset:61440\n"
    "v_mfma_f32_16x16x32_bf16 v[188:191], a[180:183], v[104:107], v[188:191]\n"
    "v_mfma_f32_16x16x32_bf16 v[188:191], a[184:187], v[112:115], v[188:191]\n"
    "ds_read_b64_tr_b16 a[224:225], v237 offset:33024\n"
    "ds_read_b64_tr_b16 a[226:227], v237 offset:37120\n"
    "v_mfma_f32_16x16x32_bf16 v[188:191], a[188:191], v[120:123], v[188:191]\n"
    "buffer_load_dwordx4 a[32:35], v244, s[56:59], s74 offen offset:1024 sc1 \n"
    "v_mfma_f32_16x16x32_bf16 v[192:195], a[112:115], v[64:67], v[192:195]\n"
    "ds_read_b64_tr_b16 a[228:229], v237 offset:41216\n"
    "ds_read_b64_tr_b16 a[230:231], v237 offset:45312\n"
    "v_mfma_f32_16x16x32_bf16 v[192:195], a[116:119], v[72:75], v[192:195]\n"
    "v_mfma_f32_16x16x32_bf16 v[192:195], a[120:123], v[80:83], v[192:195]\n"
    "ds_read_b64_tr_b16 a[232:233], v237 offset:49408\n"
    "ds_read_b64_tr_b16 a[234:235], v237 offset:53504\n"
    "v_mfma_f32_16x16x32_bf16 v[192:195], a[124:127], v[88:91], v[192:195]\n"
    "buffer_load_dwordx4 a[48:51], v227, s[56:59], s74 offen offset:1024 sc1 \n"
    "v_mfma_f32_16x16x32_bf16 v[196:199], a[112:115], v[96:99], v[196:199]\n"
    "ds_read_b64_tr_b16 a[236:237], v237 offset:57600\n"
    "ds_read_b64_tr_b16 a[238:239], v237 offset:61696\n"
    "v_mfma_f32_16x16x32_bf16 v[196:199], a[116:119], v[104:107], v[196:199]\n"
    "v_mfma_f32_16x16x32_bf16 v[196:199], a[120:123], v[112:115], v[196:199]\n"
    "ds_read_b64_tr_b16 a[240:241], v238 offset:33024\n"
    "ds_read_b64_tr_b16 a[242:243], v238 offset:37120\n"
    "v_mfma_f32_16x16x32_bf16 v[196:199], a[124:127], v[120:123], v[196:199]\n"
    "buffer_load_dwordx4 a[36:39], v244, s[56:59], s75 offen offset:1024 sc1 \n"
    "s_waitcnt vmcnt(20)\n"
    "v_mfma_f32_16x16x32_bf16 v[128:131], a[192:195], a[0:3], v[128:131]\n"
    "ds_read_b64_tr_b16 a[244:245], v238 offset:41216\n"
    "ds_read_b64_tr_b16 a[246:247], v238 offset:45312\n"
    "s_waitcnt vmcnt(18)\n"
    "v_mfma_f32_16x16x32_bf16 v[128:131], a[196:199], a[4:7], v[128:131]\n"
    "s_waitcnt lgkmcnt(4)\n"
    "s_waitcnt vmcnt(16)\n"
    "v_mfma_f32_16x16x32_bf16 v[128:131], a[200:203], a[8:11], v[128:131]\n"
    "s_barrier\n"
    "s_waitcnt vmcnt(14)\n"
    "v_mfma_f32_16x16x32_bf16 v[128:131], a[204:207], a[12:15], v[128:131]\n"
    "buffer_load_dwordx4 a[52:55], v227, s[56:59], s75 offen offset:1024 sc1 \n"
    "v_mfma_f32_16x16x32_bf16 v[132:135], a[192:195], a[16:19], v[132:135]\n"
    "ds_read_b64_tr_b16 a[248:249], v238 offset:49408\n"
    "ds_read_b64_tr_b16 a[250:251], v238 offset:53504\n"
    "v_mfma_f32_16x16x32_bf16 v[132:135], a[196:199], a[20:23], v[132:135]\n"
    "ds_read_b64_tr_b16 a[252:253], v238 offset:57600\n"
    "ds_read_b64_tr_b16 a[254:255], v238 offset:61696\n"
    "v_mfma_f32_16x16x32_bf16 v[132:135], a[200:203], a[24:27], v[132:135]\n"
    "ds_read_b64_tr_b16 a[128:129], v237 offset:33280\n"
    "ds_read_b64_tr_b16 a[130:131], v237 offset:37376\n"
    "s_waitcnt vmcnt(14)\n"
    "v_mfma_f32_16x16x32_bf16 v[132:135], a[204:207], a[28:31], v[132:135]\n"
    "v_mfma_f32_16x16x32_bf16 v[136:139], a[208:211], a[0:3], v[136:139]\n"
    "buffer_load_dwordx4 a[40:43], v244, s[56:59], s72 offen offset:2048 sc1 \n"
    "v_mfma_f32_16x16x32_bf16 v[136:139], a[212:215], a[4:7], v[136:139]\n"
    "ds_read_b64_tr_b16 a[132:133], v237 offset:41472\n"
    "ds_read_b64_tr_b16 a[134:135], v237 offset:45568\n"
    "v_mfma_f32_16x16x32_bf16 v[136:139], a[216:219], a[8:11], v[136:139]\n"
    "ds_read_b64_tr_b16 a[136:137], v237 offset:49664\n"
    "ds_read_b64_tr_b16 a[138:139], v237 offset:53760\n"
    "v_mfma_f32_16x16x32_bf16 v[136:139], a[220:223], a[12:15], v[136:139]\n"
    "v_mfma_f32_16x16x32_bf16 v[140:143], a[208:211], a[16:19], v[140:143]\n"
    "buffer_load_dwordx4 a[56:59], v227, s[56:59], s72 offen offset:2048 sc1 \n"
    "v_mfma_f32_16x16x32_bf16 v[140:143], a[212:215], a[20:23], v[140:143]\n"
    "ds_read_b64_tr_b16 a[140:141], v237 offset:57856\n"
    "ds_read_b64_tr_b16 a[142:143], v237 offset:61952\n"
    "v_mfma_f32_16x16x32_bf16 v[140:143], a[216:219], a[24:27], v[140:143]\n"
    "ds_read_b64_tr_b16 a[144:145], v238 offset:33280\n"
    "ds_read_b64_tr_b16 a[146:147], v238 offset:37376\n"
    "v_mfma_f32_16x16x32_bf16 v[140:143], a[220:223], a[28:31], v[140:143]\n"
    "v_mfma_f32_16x16x32_bf16 v[144:147], a[224:227], a[0:3], v[144:147]\n"
    "buffer_load_dwordx4 a[44:47], v244, s[56:59], s74 offen offset:2048 sc1 \n"
    "v_mfma_f32_16x16x32_bf16 v[144:147], a[228:231], a[4:7], v[144:147]\n"
    "ds_read_b64_tr_b16 a[148:149], v238 offset:41472\n"
    "ds_read_b64_tr_b16 a[150:151], v238 offset:45568\n"
    "v_mfma_f32_16x16x32_bf16 v[144:147], a[232:235], a[8:11], v[144:147]\n"
    "ds_read_b64_tr_b16 a[152:153], v238 offset:49664\n"
    "ds_read_b64_tr_b16 a[154:155], v238 offset:53760\n"
    "v_mfma_f32_16x16x32_bf16 v[144:147], a[236:239], a[12:15], v[144:147]\n"
    "v_mfma_f32_16x16x32_bf16 v[148:151], a[224:227], a[16:19], v[148:151]\n"
    "buffer_load_dwordx4 a[60:63], v227, s[56:59], s74 offen offset:2048 sc1 \n"
    "v_mfma_f32_16x16x32_bf16 v[148:151], a[228:231], a[20:23], v[148:151]\n"
    "v_readlane_b32 s81, v248, 2\n"
    "ds_read_b64_tr_b16 a[156:157], v238 offset:57856\n"
    "v_mfma_f32_16x16x32_bf16 v[148:151], a[232:235], a[24:27], v[148:151]\n"
    "s_add_u32 m0, s83, 155648\n"
    "ds_read_b64_tr_b16 a[158:159], v238 offset:61952\n"
    "v_mfma_f32_16x16x32_bf16 v[148:151], a[236:239], a[28:31], v[148:151]\n"
    "ds_read_b64_tr_b16 a[160:161], v237 offset:33536\n"
    "ds_read_b64_tr_b16 a[162:163], v237 offset:37632\n"
    "s_waitcnt lgkmcnt(4)\n"
    "v_mfma_f32_16x16x32_bf16 v[152:155], a[240:243], a[0:3], v[152:155]\n"
    "s_nop 0\n"
    "ds_read_b64_tr_b16 a[164:165], v237 offset:41728\n"
    "v_mfma_f32_16x16x32_bf16 v[152:155], a[244:247], a[4:7], v[152:155]\n"
    "buffer_load_dwordx4 v225, s[52:55], s81 offen lds\n"
    "v_mfma_f32_16x16x32_bf16 v[152:155], a[248:251], a[8:11], v[152:155]\n"
    "s_add_u32 m0, s73, 90112\n"
    "s_nop 0\n"
    "v_mfma_f32_16x16x32_bf16 v[152:155], a[252:255], a[12:15], v[152:155]\n"
    "ds_read_b64_tr_b16 a[166:167], v237 offset:45824\n"
    "ds_read_b64_tr_b16 a[168:169], v237 offset:49920\n"
    "v_mfma_f32_16x16x32_bf16 v[156:159], a[240:243], a[16:19], v[156:159]\n"
    "ds_read_b64_tr_b16 a[170:171], v237 offset:54016\n"
    "ds_read_b64_tr_b16 a[172:173], v237 offset:58112\n"
    "v_mfma_f32_16x16x32_bf16 v[156:159], a[244:247], a[20:23], v[156:159]\n"
    "buffer_load_dwordx4 v240, s[52:55], s81 offen offset:0  lds\n"
    "v_mfma_f32_16x16x32_bf16 v[156:159], a[248:251], a[24:27], v[156:159]\n"
    "ds_read_b64_tr_b16 a[174:175], v237 offset:62208\n"
    "ds_read_b64_tr_b16 a[176:177], v238 offset:33536\n"
    "v_mfma_f32_16x16x32_bf16 v[156:159], a[252:255], a[28:31], v[156:159]\n"
    "ds_read_b64_tr_b16 a[178:179], v238 offset:37632\n"
    "v_mfma_f32_16x16x32_bf16 v[160:163], a[128:131], a[0:3], v[160:163]\n"
    "ds_read_b64_tr_b16 a[180:181], v238 offset:41728\n"
    "ds_read_b64_tr_b16 a[182:183], v238 offset:45824\n"
    "v_mfma_f32_16x16x32_bf16 v[160:163], a[132:135], a[4:7], v[160:163]\n"
    "buffer_load_dwordx4 v241, s[52:55], s81 offen offset:1024  lds\n"
    "v_mfma_f32_16x16x32_bf16 v[160:163], a[136:139], a[8:11], v[160:163]\n"
    "ds_read_b64_tr_b16 a[184:185], v238 offset:49920\n"
    "ds_read_b64_tr_b16 a[186:187], v238 offset:54016\n"
    "v_mfma_f32_16x16x32_bf16 v[160:163], a[140:143], a[12:15], v[160:163]\n"
    "v_mfma_f32_16x16x32_bf16 v[164:167], a[128:131], a[16:19], v[164:167]\n"
    "ds_read_b64_tr_b16 a[188:189], v238 offset:58112\n"
    "ds_read_b64_tr_b16 a[190:191], v238 offset:62208\n"
    "v_mfma_f32_16x16x32_bf16 v[164:167], a[132:135], a[20:23], v[164:167]\n"
    "buffer_load_dwordx4 v242, s[52:55], s81 offen offset:2048  lds\n"
    "v_mfma_f32_16x16x32_bf16 v[164:167], a[136:139], a[24:27], v[164:167]\n"
    "ds_read_b64_tr_b16 a[112:113], v245 offset:4096\n"
    "ds_read_b64_tr_b16 a[114:115], v245 offset:4608\n"
    "v_mfma_f32_16x16x32_bf16 v[164:167], a[140:143], a[28:31], v[164:167]\n"
    "v_mfma_f32_16x16x32_bf16 v[168:171], a[144:147], a[0:3], v[168:171]\n"
    "ds_read_b64_tr_b16 a[116:117], v245 offset:5120\n"
    "ds_read_b64_tr_b16 a[118:119], v245 offset:5632\n"
    "v_mfma_f32_16x16x32_bf16 v[168:171], a[148:151], a[4:7], v[168:171]\n"
    "buffer_load_dwordx4 v243, s[52:55], s81 offen offset:3072  lds\n"
    "v_mfma_f32_16x16x32_bf16 v[168:171], a[152:155], a[8:11], v[168:171]\n"
    "s_add_u32 m0, s73, 94208\n"
    "s_add_u32 s81, s81, 18432\n"
    "s_waitcnt lgkmcnt(3)\n"
    "v_mfma_f32_16x16x32_bf16 v[168:171], a[156:159], a[12:15], v[168:171]\n"
    "ds_read_b64_tr_b16 a[120:121], v245 offset:6144\n"
    "ds_read_b64_tr_b16 a[122:123], v245 offset:6656\n"
    "v_mfma_f32_16x16x32_bf16 v[172:175], a[144:147], a[16:19], v[172:175]\n"
    "s_waitcnt vmcnt(13)\n"
    "s_barrier\n"
    "v_mfma_f32_16x16x32_bf16 v[172:175], a[148:151], a[20:23], v[172:175]\n"
    "ds_read_b64_tr_b16 a[124:125], v245 offset:7168\n"
    "ds_read_b64_tr_b16 a[126:127], v245 offset:7680\n"
    "v_mfma_f32_16x16x32_bf16 v[172:175], a[152:155], a[24:27], v[172:175]\n"
    "buffer_load_dwordx4 v240, s[52:55], s81 offen offset:0  lds\n"
    "v_mfma_f32_16x16x32_bf16 v[172:175], a[156:159], a[28:31], v[172:175]\n"
    "ds_read_b64_tr_b16 a[192:193], v235 offset:0\n"
    "ds_read_b64_tr_b16 a[194:195], v235 offset:4096\n"
    "v_mfma_f32_16x16x32_bf16 v[176:179], a[160:163], a[0:3], v[176:179]\n"
    "ds_read_b64_tr_b16 a[196:197], v235 offset:8192\n"
    "ds_read_b64_tr_b16 a[198:199], v235 offset:12288\n"
    "v_mfma_f32_16x16x32_bf16 v[176:179], a[164:167], a[4:7], v[176:179]\n"
    "v_mfma_f32_16x16x32_bf16 v[176:179], a[168:171], a[8:11], v[176:179]\n"
    "buffer_load_dwordx4 v241, s[52:55], s81 offen offset:1024  lds\n"
    "v_mfma_f32_16x16x32_bf16 v[176:179], a[172:175], a[12:15], v[176:179]\n"
    "ds_read_b64_tr_b16 a[200:201], v235 offset:16384\n"
    "ds_read_b64_tr_b16 a[202:203], v235 offset:20480\n"
    "v_mfma_f32_16x16x32_bf16 v[180:183], a[160:163], a[16:19], v[180:183]\n"
    "ds_read_b64_tr_b16 a[204:205], v235 offset:24576\n"
    "ds_read_b64_tr_b16 a[206:207], v235 offset:28672\n"
    "v_mfma_f32_16x16x32_bf16 v[180:183], a[164:167], a[20:23], v[180:183]\n"
    "v_mfma_f32_16x16x32_bf16 v[180:183], a[168:171], a[24:27], v[180:183]\n"
    "buffer_load_dwordx4 v242, s[52:55], s81 offen offset:2048  lds\n"
    "v_mfma_f32_16x16x32_bf16 v[180:183], a[172:175], a[28:31], v[180:183]\n"
    "ds_read_b64_tr_b16 a[208:209], v236 offset:0\n"
    "ds_read_b64_tr_b16 a[210:211], v236 offset:4096\n"
    "v_mfma_f32_16x16x32_bf16 v[184:187], a[176:179], a[0:3], v[184:187]\n"
    "ds_read_b64_tr_b16 a[212:213], v236 offset:8192\n"
    "ds_read_b64_tr_b16 a[214:215], v236 offset:12288\n"
    "v_mfma_f32_16x16x32_bf16 v[184:187], a[180:183], a[4:7], v[184:187]\n"
    "v_mfma_f32_16x16x32_bf16 v[184:187], a[184:187], a[8:11], v[184:187]\n"
    "buffer_load_dwordx4 v243, s[52:55], s81 offen offset:3072  lds\n"
    "v_mfma_f32_16x16x32_bf16 v[184:187], a[188:191], a[12:15], v[184:187]\n"
    "ds_read_b64_tr_b16 a[216:217], v236 offset:16384\n"
    "ds_read_b64_tr_b16 a[218:219], v236 offset:20480\n"
    "v_mfma_f32_16x16x32_bf16 v[188:191], a[176:179], a[16:19], v[188:191]\n"
    "ds_read_b64_tr_b16 a[220:221], v236 offset:24576\n"
    "ds_read_b64_tr_b16 a[222:223], v236 offset:28672\n"
    "v_mfma_f32_16x16x32_bf16 v[188:191], a[180:183], a[20:23], v[188:191]\n"
    "v_mfma_f32_16x16x32_bf16 v[188:191], a[184:187], a[24:27], v[188:191]\n"
    "ds_read_b64_tr_b16 a[224:225], v235 offset:256\n"
    "ds_read_b64_tr_b16 a[226:227], v235 offset:4352\n"
    "v_mfma_f32_16x16x32_bf16 v[188:191], a[188:191], a[28:31], v[188:191]\n"
    "buffer_load_dwordx4 a[64:67], v244, s[56:59], s75 offen offset:2048 sc1 \n"
    "s_waitcnt lgkmcnt(5)\n"
    "v_mfma_f32_16x16x32_bf16 v[192:195], a[112:115], a[0:3], v[192:195]\n"
    "ds_read_b64_tr_b16 a[228:229], v235 offset:8448\n"
    "ds_read_b64_tr_b16 a[230:231], v235 offset:12544\n"
    "v_mfma_f32_16x16x32_bf16 v[192:195], a[116:119], a[4:7], v[192:195]\n"
    "v_mfma_f32_16x16x32_bf16 v[192:195], a[120:123], a[8:11], v[192:195]\n"
    "ds_read_b64_tr_b16 a[232:233], v235 offset:16640\n"
    "ds_read_b64_tr_b16 a[234:235], v235 offset:20736\n"
    "v_mfma_f32_16x16x32_bf16 v[192:195], a[124:127], a[12:15], v[192:195]\n"
    "buffer_load_dwordx4 a[80:83], v227, s[56:59], s75 offen offset:2048 sc1 \n"
    "v_mfma_f32_16x16x32_bf16 v[196:199], a[112:115], a[16:19], v[196:199]\n"
    "ds_read_b64_tr_b16 a[236:237], v235 offset:24832\n"
    "ds_read_b64_tr_b16 a[238:239], v235 offset:28928\n"
    "v_mfma_f32_16x16x32_bf16 v[196:199], a[116:119], a[20:23], v[196:199]\n"
    "v_mfma_f32_16x16x32_bf16 v[196:199], a[120:123], a[24:27], v[196:199]\n"
    "ds_read_b64_tr_b16 a[240:241], v236 offset:256\n"
    "ds_read_b64_tr_b16 a[242:243], v236 offset:4352\n"
    "v_mfma_f32_16x16x32_bf16 v[196:199], a[124:127], a[28:31], v[196:199]\n"
    "buffer_load_dwordx4 a[68:71], v244, s[56:59], s72 offen offset:3072 sc1 \n"
    "s_waitcnt vmcnt(19)\n"
    "v_mfma_f32_16x16x32_bf16 v[128:131], a[192:195], a[32:35], v[128:131]\n"
    "ds_read_b64_tr_b16 a[244:245], v236 offset:8448\n"
    "ds_read_b64_tr_b16 a[246:247], v236 offset:12544\n"
    "s_waitcnt vmcnt(17)\n"
    "v_mfma_f32_16x16x32_bf16 v[128:131], a[196:199], a[36:39], v[128:131]\n"
    "s_waitcnt vmcnt(15)\n"
    "v_mfma_f32_16x16x32_bf16 v[128:131], a[200:203], a[40:43], v[128:131]\n"
    "s_barrier\n"
    "s_waitcnt vmcnt(13)\n"
    "v_mfma_f32_16x16x32_bf16 v[128:131], a[204:207], a[44:47], v[128:131]\n"
    "buffer_load_dwordx4 a[84:87], v227, s[56:59], s72 offen offset:3072 sc1 \n"
    "v_mfma_f32_16x16x32_bf16 v[132:135], a[192:195], a[48:51], v[132:135]\n"
    "ds_read_b64_tr_b16 a[248:249], v236 offset:16640\n"
    "ds_read_b64_tr_b16 a[250:251], v236 offset:20736\n"
    "v_mfma_f32_16x16x32_bf16 v[132:135], a[196:199], a[52:55], v[132:135]\n"
    "ds_read_b64_tr_b16 a[252:253], v236 offset:24832\n"
    "ds_read_b64_tr_b16 a[254:255], v236 offset:28928\n"
    "v_mfma_f32_16x16x32_bf16 v[132:135], a[200:203], a[56:59], v[132:135]\n"
    "ds_read_b64_tr_b16 a[128:129], v235 offset:512\n"
    "ds_read_b64_tr_b16 a[130:131], v235 offset:4608\n"
    "s_waitcnt vmcnt(13)\n"
    "v_mfma_f32_16x16x32_bf16 v[132:135], a[204:207], a[60:63], v[132:135]\n"
    "v_mfma_f32_16x16x32_bf16 v[136:139], a[208:211], a[32:35], v[136:139]\n"
    "buffer_load_dwordx4 a[72:75], v244, s[56:59], s74 offen offset:3072 sc1 \n"
    "v_mfma_f32_16x16x32_bf16 v[136:139], a[212:215], a[36:39], v[136:139]\n"
    "ds_read_b64_tr_b16 a[132:133], v235 offset:8704\n"
    "ds_read_b64_tr_b16 a[134:135], v235 offset:12800\n"
    "s_waitcnt lgkmcnt(4)\n"
    "v_mfma_f32_16x16x32_bf16 v[136:139], a[216:219], a[40:43], v[136:139]\n"
    "ds_read_b64_tr_b16 a[136:137], v235 offset:16896\n"
    "ds_read_b64_tr_b16 a[138:139], v235 offset:20992\n"
    "v_mfma_f32_16x16x32_bf16 v[136:139], a[220:223], a[44:47], v[136:139]\n"
    "v_mfma_f32_16x16x32_bf16 v[140:143], a[208:211], a[48:51], v[140:143]\n"
    "buffer_load_dwordx4 a[88:91], v227, s[56:59], s74 offen offset:3072 sc1 \n"
    "v_mfma_f32_16x16x32_bf16 v[140:143], a[212:215], a[52:55], v[140:143]\n"
    "ds_read_b64_tr_b16 a[140:141], v235 offset:25088\n"
    "ds_read_b64_tr_b16 a[142:143], v235 offset:29184\n"
    "v_mfma_f32_16x16x32_bf16 v[140:143], a[216:219], a[56:59], v[140:143]\n"
    "ds_read_b64_tr_b16 a[144:145], v236 offset:512\n"
    "ds_read_b64_tr_b16 a[146:147], v236 offset:4608\n"
    "v_mfma_f32_16x16x32_bf16 v[140:143], a[220:223], a[60:63], v[140:143]\n"
    "v_mfma_f32_16x16x32_bf16 v[144:147], a[224:227], a[32:35], v[144:147]\n"
    "buffer_load_dwordx4 a[76:79], v244, s[56:59], s75 offen offset:3072 sc1 \n"
    "v_mfma_f32_16x16x32_bf16 v[144:147], a[228:231], a[36:39], v[144:147]\n"
    "ds_read_b64_tr_b16 a[148:149], v236 offset:8704\n"
    "ds_read_b64_tr_b16 a[150:151], v236 offset:12800\n"
    "v_mfma_f32_16x16x32_bf16 v[144:147], a[232:235], a[40:43], v[144:147]\n"
    "ds_read_b64_tr_b16 a[152:153], v236 offset:16896\n"
    "ds_read_b64_tr_b16 a[154:155], v236 offset:20992\n"
    "v_mfma_f32_16x16x32_bf16 v[144:147], a[236:239], a[44:47], v[144:147]\n"
    "v_mfma_f32_16x16x32_bf16 v[148:151], a[224:227], a[48:51], v[148:151]\n"
    "buffer_load_dwordx4 a[92:95], v227, s[56:59], s75 offen offset:3072 sc1 \n"
    "v_mfma_f32_16x16x32_bf16 v[148:151], a[228:231], a[52:55], v[148:151]\n"
    "ds_read_b64_tr_b16 a[156:157], v236 offset:25088\n"
    "ds_read_b64_tr_b16 a[158:159], v236 offset:29184\n"
    "v_mfma_f32_16x16x32_bf16 v[148:151], a[232:235], a[56:59], v[148:151]\n"
    "ds_read_b64_tr_b16 a[160:161], v235 offset:768\n"
    "ds_read_b64_tr_b16 a[162:163], v235 offset:4864\n"
    "v_mfma_f32_16x16x32_bf16 v[148:151], a[236:239], a[60:63], v[148:151]\n"
    "v_readlane_b32 s81, v248, 3\n"
    "v_mfma_f32_16x16x32_bf16 v[152:155], a[240:243], a[32:35], v[152:155]\n"
    "s_add_u32 m0, s83, 159744\n"
    "ds_read_b64_tr_b16 a[164:165], v235 offset:8960\n"
    "v_mfma_f32_16x16x32_bf16 v[152:155], a[244:247], a[36:39], v[152:155]\n"
    "buffer_load_dword v251, v229, s[56:59], s85 offen offset:16 sc1\n"
    "v_mfma_f32_16x16x32_bf16 v[152:155], a[248:251], a[40:43], v[152:155]\n"
    "s_nop 0\n"
    "ds_read_b64_tr_b16 a[166:167], v235 offset:13056\n"
    "v_mfma_f32_16x16x32_bf16 v[152:155], a[252:255], a[44:47], v[152:155]\n"
    "buffer_load_dwordx4 v225, s[52:55], s81 offen lds\n"
    "v_mfma_f32_16x16x32_bf16 v[156:159], a[240:243], a[48:51], v[156:159]\n"
    "s_add_u32 m0, s73, 122880\n"
    "s_nop 0\n"
    "v_mfma_f32_16x16x32_bf16 v[156:159], a[244:247], a[52:55], v[156:159]\n"
    "ds_read_b64_tr_b16 a[168:169], v235 offset:17152\n"
    "ds_read_b64_tr_b16 a[170:171], v235 offset:21248\n"
    "v_mfma_f32_16x16x32_bf16 v[156:159], a[248:251], a[56:59], v[156:159]\n"
    "ds_read_b64_tr_b16 a[172:173], v235 offset:25344\n"
    "ds_read_b64_tr_b16 a[174:175], v235 offset:29440\n"
    "v_mfma_f32_16x16x32_bf16 v[156:159], a[252:255], a[60:63], v[156:159]\n"
    "buffer_load_dwordx4 v240, s[52:55], s81 offen offset:0  lds\n"
    "s_waitcnt lgkmcnt(4)\n"
    "v_mfma_f32_16x16x32_bf16 v[160:163], a[128:131], a[32:35], v[160:163]\n"
    "ds_read_b64_tr_b16 a[176:177], v236 offset:768\n"
    "ds_read_b64_tr_b16 a[178:179], v236 offset:4864\n"
    "v_mfma_f32_16x16x32_bf16 v[160:163], a[132:135], a[36:39], v[160:163]\n"
    "ds_read_b64_tr_b16 a[180:181], v236 offset:8960\n"
    "ds_read_b64_tr_b16 a[182:183], v236 offset:13056\n"
    "v_mfma_f32_16x16x32_bf16 v[160:163], a[136:139], a[40:43], v[160:163]\n"
    "ds_read_b64_tr_b16 a[184:185], v236 offset:17152\n"
    "ds_read_b64_tr_b16 a[186:187], v236 offset:21248\n"
    "v_mfma_f32_16x16x32_bf16 v[160:163], a[140:143], a[44:47], v[160:163]\n"
    "buffer_load_dwordx4 v241, s[52:55], s81 offen offset:1024  lds\n"
    "v_mfma_f32_16x16x32_bf16 v[164:167], a[128:131], a[48:51], v[164:167]\n"
    "ds_read_b64_tr_b16 a[188:189], v236 offset:25344\n"
    "ds_read_b64_tr_b16 a[190:191], v236 offset:29440\n"
    "v_mfma_f32_16x16x32_bf16 v[164:167], a[132:135], a[52:55], v[164:167]\n"
    "v_mfma_f32_16x16x32_bf16 v[164:167], a[136:139], a[56:59], v[164:167]\n"
    "ds_read_b64_tr_b16 a[112:113], v226 offset:0\n"
    "ds_read_b64_tr_b16 a[114:115], v226 offset:512\n"
    "v_mfma_f32_16x16x32_bf16 v[164:167], a[140:143], a[60:63], v[164:167]\n"
    "buffer_load_dwordx4 v242, s[52:55], s81 offen offset:2048  lds\n"
    "v_mfma_f32_16x16x32_bf16 v[168:171], a[144:147], a[32:35], v[168:171]\n"
    "ds_read_b64_tr_b16 a[116:117], v226 offset:1024\n"
    "ds_read_b64_tr_b16 a[118:119], v226 offset:1536\n"
    "v_mfma_f32_16x16x32_bf16 v[168:171], a[148:151], a[36:39], v[168:171]\n"
    "v_mfma_f32_16x16x32_bf16 v[168:171], a[152:155], a[40:43], v[168:171]\n"
    "ds_read_b64_tr_b16 a[120:121], v226 offset:2048\n"
    "ds_read_b64_tr_b16 a[122:123], v226 offset:2560\n"
    "v_mfma_f32_16x16x32_bf16 v[168:171], a[156:159], a[44:47], v[168:171]\n"
    "buffer_load_dwordx4 v243, s[52:55], s81 offen offset:3072  lds\n"
    "v_mfma_f32_16x16x32_bf16 v[172:175], a[144:147], a[48:51], v[172:175]\n"
    "s_waitcnt vmcnt(14)\n"
    "s_barrier\n"
    "v_mfma_f32_16x16x32_bf16 v[172:175], a[148:151], a[52:55], v[172:175]\n"
    "s_add_u32 m0, s73, 126976\n"
    "s_add_u32 s81, s81, 18432\n"
    "v_mfma_f32_16x16x32_bf16 v[172:175], a[152:155], a[56:59], v[172:175]\n"
    "ds_read_b64_tr_b16 a[124:125], v226 offset:3072\n"
    "ds_read_b64_tr_b16 a[126:127], v226 offset:3584\n"
    "v_mfma_f32_16x16x32_bf16 v[172:175], a[156:159], a[60:63], v[172:175]\n"
    "ds_read_b64_tr_b16 a[192:193], v237 offset:0\n"
    "ds_read_b64_tr_b16 a[194:195], v237 offset:4096\n"
    "v_mfma_f32_16x16x32_bf16 v[176:179], a[160:163], a[32:35], v[176:179]\n"
    "buffer_load_dwordx4 v240, s[52:55], s81 offen offset:0  lds\n"
    "v_mfma_f32_16x16x32_bf16 v[176:179], a[164:167], a[36:39], v[176:179]\n"
    "ds_read_b64_tr_b16 a[196:197], v237 offset:8192\n"
    "ds_read_b64_tr_b16 a[198:199], v237 offset:12288\n"
    "s_waitcnt lgkmcnt(6)\n"
    "v_mfma_f32_16x16x32_bf16 v[176:179], a[168:171], a[40:43], v[176:179]\n"
    "ds_read_b64_tr_b16 a[200:201], v237 offset:16384\n"
    "ds_read_b64_tr_b16 a[202:203], v237 offset:20480\n"
    "v_mfma_f32_16x16x32_bf16 v[176:179], a[172:175], a[44:47], v[176:179]\n"
    "v_mfma_f32_16x16x32_bf16 v[180:183], a[160:163], a[48:51], v[180:183]\n"
    "buffer_load_dwordx4 v241, s[52:55], s81 offen offset:1024  lds\n"
    "v_mfma_f32_16x16x32_bf16 v[180:183], a[164:167], a[52:55], v[180:183]\n"
    "ds_read_b64_tr_b16 a[204:205], v237 offset:24576\n"
    "ds_read_b64_tr_b16 a[206:207], v237 offset:28672\n"
    "v_mfma_f32_16x16x32_bf16 v[180:183], a[168:171], a[56:59], v[180:183]\n"
    "ds_read_b64_tr_b16 a[208:209], v238 offset:0\n"
    "ds_read_b64_tr_b16 a[210:211], v238 offset:4096\n"
    "v_mfma_f32_16x16x32_bf16 v[180:183], a[172:175], a[60:63], v[180:183]\n"
    "v_mfma_f32_16x16x32_bf16 v[184:187], a[176:179], a[32:35], v[184:187]\n"
    "buffer_load_dwordx4 v242, s[52:55], s81 offen offset:2048  lds\n"
    "v_mfma_f32_16x16x32_bf16 v[184:187], a[180:183], a[36:39], v[184:187]\n"
    "ds_read_b64_tr_b16 a[212:213], v238 offset:8192\n"
    "ds_read_b64_tr_b16 a[214:215], v238 offset:12288\n"
    "v_mfma_f32_16x16x32_bf16 v[184:187], a[184:187], a[40:43], v[184:187]\n"
    "ds_read_b64_tr_b16 a[216:217], v238 offset:16384\n"
    "ds_read_b64_tr_b16 a[218:219], v238 offset:20480\n"
    "v_mfma_f32_16x16x32_bf16 v[184:187], a[188:191], a[44:47], v[184:187]\n"
    "v_mfma_f32_16x16x32_bf16 v[188:191], a[176:179], a[48:51], v[188:191]\n"
    "buffer_load_dwordx4 v243, s[52:55], s81 offen offset:3072  lds\n"
    "v_mfma_f32_16x16x32_bf16 v[188:191], a[180:183], a[52:55], v[188:191]\n"
    "ds_read_b64_tr_b16 a[220:221], v238 offset:24576\n"
    "ds_read_b64_tr_b16 a[222:223], v238 offset:28672\n"
    "v_mfma_f32_16x16x32_bf16 v[188:191], a[184:187], a[56:59], v[188:191]\n"
    "ds_read_b64_tr_b16 a[224:225], v237 offset:256\n"
    "ds_read_b64_tr_b16 a[226:227], v237 offset:4352\n"
    "v_mfma_f32_16x16x32_bf16 v[188:191], a[188:191], a[60:63], v[188:191]\n"
    "v_mfma_f32_16x16x32_bf16 v[192:195], a[112:115], a[32:35], v[192:195]\n"
    "ds_read_b64_tr_b16 a[228:229], v237 offset:8448\n"
    "ds_read_b64_tr_b16 a[230:231], v237 offset:12544\n"
    "v_mfma_f32_16x16x32_bf16 v[192:195], a[116:119], a[36:39], v[192:195]\n"
    "s_waitcnt vmcnt(9)\n"
    "v_cmp_eq_u32 vcc, 0, v251\n"
    "s_nop 1\n"
    "s_cmp_lg_u64 vcc, 0\n"
    "s_cbranch_scc1 OSB%=\n"
    "ODB%=:\n"
    "v_mfma_f32_16x16x32_bf16 v[192:195], a[120:123], a[40:43], v[192:195]\n"
    "buffer_load_dwordx4 a[96:99], v244, s[56:59], s76 offen offset:0 sc1 \n"
    "s_waitcnt lgkmcnt(2)\n"
    "v_mfma_f32_16x16x32_bf16 v[192:195], a[124:127], a[44:47], v[192:195]\n"
    "ds_read_b64_tr_b16 a[232:233], v237 offset:16640\n"
    "ds_read_b64_tr_b16 a[234:235], v237 offset:20736\n"
    "v_mfma_f32_16x16x32_bf16 v[196:199], a[112:115], a[48:51], v[196:199]\n"
    "ds_read_b64_tr_b16 a[236:237], v237 offset:24832\n"
    "ds_read_b64_tr_b16 a[238:239], v237 offset:28928\n"
    "v_mfma_f32_16x16x32_bf16 v[196:199], a[116:119], a[52:55], v[196:199]\n"
    "v_mfma_f32_16x16x32_bf16 v[196:199], a[120:123], a[56:59], v[196:199]\n"
    "buffer_load_dwordx4 v[4:7], v227, s[56:59], s76 offen offset:0 sc1 \n"
    "v_mfma_f32_16x16x32_bf16 v[196:199], a[124:127], a[60:63], v[196:199]\n"
    "ds_read_b64_tr_b16 a[240:241], v238 offset:256\n"
    "ds_read_b64_tr_b16 a[242:243], v238 offset:4352\n"
    "v_mfma_f32_16x16x32_bf16 v[128:131], a[192:195], a[64:67], v[128:131]\n"
    "ds_read_b64_tr_b16 a[244:245], v238 offset:8448\n"
    "ds_read_b64_tr_b16 a[246:247], v238 offset:12544\n"
    "v_mfma_f32_16x16x32_bf16 v[128:131], a[196:199], a[68:71], v[128:131]\n"
    "v_mfma_f32_16x16x32_bf16 v[128:131], a[200:203], a[72:75], v[128:131]\n"
    "s_barrier\n"
    "buffer_load_dwordx4 a[100:103], v244, s[56:59], s77 offen offset:0 sc1 \n"
    "v_mfma_f32_16x16x32_bf16 v[128:131], a[204:207], a[76:79], v[128:131]\n"
    "ds_read_b64_tr_b16 a[248:249], v238 offset:16640\n"
    "ds_read_b64_tr_b16 a[250:251], v238 offset:20736\n"
    "v_mfma_f32_16x16x32_bf16 v[132:135], a[192:195], a[80:83], v[132:135]\n"
    "ds_read_b64_tr_b16 a[252:253], v238 offset:24832\n"
    "ds_read_b64_tr_b16 a[254:255], v238 offset:28928\n"
    "v_mfma_f32_16x16x32_bf16 v[132:135], a[196:199], a[84:87], v[132:135]\n"
    "v_mfma_f32_16x16x32_bf16 v[132:135], a[200:203], a[88:91], v[132:135]\n"
    "buffer_load_dwordx4 v[12:15], v227, s[56:59], s77 offen offset:0 sc1 \n"
    "v_mfma_f32_16x16x32_bf16 v[132:135], a[204:207], a[92:95], v[132:135]\n"
    "ds_read_b64_tr_b16 a[128:129], v237 offset:512\n"
    "ds_read_b64_tr_b16 a[130:131], v237 offset:4608\n"
    "v_mfma_f32_16x16x32_bf16 v[136:139], a[208:211], a[64:67], v[136:139]\n"
    "ds_read_b64_tr_b16 a[132:133], v237 offset:8704\n"
    "ds_read_b64_tr_b16 a[134:135], v237 offset:12800\n"
    "v_mfma_f32_16x16x32_bf16 v[136:139], a[212:215], a[68:71], v[136:139]\n"
    "v_mfma_f32_16x16x32_bf16 v[136:139], a[216:219], a[72:75], v[136:139]\n"
    "buffer_load_dwordx4 a[104:107], v244, s[56:59], s36 offen offset:0 sc1 \n"
    "v_mfma_f32_16x16x32_bf16 v[136:139], a[220:223], a[76:79], v[136:139]\n"
    "ds_read_b64_tr_b16 a[136:137], v237 offset:16896\n"
    "ds_read_b64_tr_b16 a[138:139], v237 offset:20992\n"
    "v_mfma_f32_16x16x32_bf16 v[140:143], a[208:211], a[80:83], v[140:143]\n"
    "ds_read_b64_tr_b16 a[140:141], v237 offset:25088\n"
    "ds_read_b64_tr_b16 a[142:143], v237 offset:29184\n"
    "v_mfma_f32_16x16x32_bf16 v[140:143], a[212:215], a[84:87], v[140:143]\n"
    "v_mfma_f32_16x16x32_bf16 v[140:143], a[216:219], a[88:91], v[140:143]\n"
    "buffer_load_dwordx4 v[20:23], v227, s[56:59], s36 offen offset:0 sc1 \n"
    "v_mfma_f32_16x16x32_bf16 v[140:143], a[220:223], a[92:95], v[140:143]\n"
    "ds_read_b64_tr_b16 a[144:145], v238 offset:512\n"
    "ds_read_b64_tr_b16 a[146:147], v238 offset:4608\n"
    "v_mfma_f32_16x16x32_bf16 v[144:147], a[224:227], a[64:67], v[144:147]\n"
    "ds_read_b64_tr_b16 a[148:149], v238 offset:8704\n"
    "ds_read_b64_tr_b16 a[150:151], v238 offset:12800\n"
    "s_waitcnt lgkmcnt(5)\n"
    "v_mfma_f32_16x16x32_bf16 v[144:147], a[228:231], a[68:71], v[144:147]\n"
    "v_mfma_f32_16x16x32_bf16 v[144:147], a[232:235], a[72:75], v[144:147]\n"
    "buffer_load_dwordx4 a[108:111], v244, s[56:59], s76 offen offset:1024 sc1 \n"
    "v_mfma_f32_16x16x32_bf16 v[144:147], a[236:239], a[76:79], v[144:147]\n"
    "ds_read_b64_tr_b16 a[152:153], v238 offset:16896\n"
    "ds_read_b64_tr_b16 a[154:155], v238 offset:20992\n"
    "v_mfma_f32_16x16x32_bf16 v[148:151], a[224:227], a[80:83], v[148:151]\n"
    "ds_read_b64_tr_b16 a[156:157], v238 offset:25088\n"
    "ds_read_b64_tr_b16 a[158:159], v238 offset:29184\n"
    "v_mfma_f32_16x16x32_bf16 v[148:151], a[228:231], a[84:87], v[148:151]\n"
    "v_mfma_f32_16x16x32_bf16 v[148:151], a[232:235], a[88:91], v[148:151]\n"
    "buffer_load_dwordx4 v[28:31], v227, s[56:59], s76 offen offset:1024 sc1 \n"
    "v_mfma_f32_16x16x32_bf16 v[148:151], a[236:239], a[92:95], v[148:151]\n"
    "v_readlane_b32 s81, v248, 4\n"
    "ds_read_b64_tr_b16 a[160:161], v237 offset:768\n"
    "v_mfma_f32_16x16x32_bf16 v[152:155], a[240:243], a[64:67], v[152:155]\n"
    "s_add_u32 m0, s83, 32768\n"
    "ds_read_b64_tr_b16 a[162:163], v237 offset:4864\n"
    "v_mfma_f32_16x16x32_bf16 v[152:155], a[244:247], a[68:71], v[152:155]\n"
    "ds_read_b64_tr_b16 a[164:165], v237 offset:8960\n"
    "ds_read_b64_tr_b16 a[166:167], v237 offset:13056\n"
    "v_mfma_f32_16x16x32_bf16 v[152:155], a[248:251], a[72:75], v[152:155]\n"
    "s_nop 0\n"
    "ds_read_b64_tr_b16 a[168:169], v237 offset:17152\n"
    "v_mfma_f32_16x16x32_bf16 v[152:155], a[252:255], a[76:79], v[152:155]\n"
    "buffer_load_dwordx4 v225, s[52:55], s81 offen lds\n"
    "v_mfma_f32_16x16x32_bf16 v[156:159], a[240:243], a[80:83], v[156:159]\n"
    "s_add_u32 m0, s73, 0\n"
    "s_nop 0\n"
    "v_mfma_f32_16x16x32_bf16 v[156:159], a[244:247], a[84:87], v[156:159]\n"
    "ds_read_b64_tr_b16 a[170:171], v237 offset:21248\n"
    "ds_read_b64_tr_b16 a[172:173], v237 offset:25344\n"
    "v_mfma_f32_16x16x32_bf16 v[156:159], a[248:251], a[88:91], v[156:159]\n"
    "ds_read_b64_tr_b16 a[174:175], v237 offset:29440\n"
    "ds_read_b64_tr_b16 a[176:177], v238 offset:768\n"
    "v_mfma_f32_16x16x32_bf16 v[156:159], a[252:255], a[92:95], v[156:159]\n"
    "buffer_load_dwordx4 v240, s[52:55], s81 offen offset:0  lds\n"
    "v_mfma_f32_16x16x32_bf16 v[160:163], a[128:131], a[64:67], v[160:163]\n"
    "ds_read_b64_tr_b16 a[178:179], v238 offset:4864\n"
    "ds_read_b64_tr_b16 a[180:181], v238 offset:8960\n"
    "v_mfma_f32_16x16x32_bf16 v[160:163], a[132:135], a[68:71], v[160:163]\n"
    "ds_read_b64_tr_b16 a[182:183], v238 offset:13056\n"
    "v_mfma_f32_16x16x32_bf16 v[160:163], a[136:139], a[72:75], v[160:163]\n"
    "ds_read_b64_tr_b16 a[184:185], v238 offset:17152\n"
    "ds_read_b64_tr_b16 a[186:187], v238 offset:21248\n"
    "s_waitcnt lgkmcnt(5)\n"
    "v_mfma_f32_16x16x32_bf16 v[160:163], a[140:143], a[76:79], v[160:163]\n"
    "buffer_load_dwordx4 v241, s[52:55], s81 offen offset:1024  lds\n"
    "v_mfma_f32_16x16x32_bf16 v[164:167], a[128:131], a[80:83], v[164:167]\n"
    "ds_read_b64_tr_b16 a[188:189], v238 offset:25344\n"
    "ds_read_b64_tr_b16 a[190:191], v238 offset:29440\n"
    "v_mfma_f32_16x16x32_bf16 v[164:167], a[132:135], a[84:87], v[164:167]\n"
    "v_mfma_f32_16x16x32_bf16 v[164:167], a[136:139], a[88:91], v[164:167]\n"
    "ds_read_b64_tr_b16 a[112:113], v245 offset:0\n"
    "ds_read_b64_tr_b16 a[114:115], v245 offset:512\n"
    "v_mfma_f32_16x16x32_bf16 v[164:167], a[140:143], a[92:95], v[164:167]\n"
    "v_mfma_f32_16x16x32_bf16 v[168:171], a[144:147], a[64:67], v[168:171]\n"
    "buffer_load_dwordx4 v242, s[52:55], s81 offen offset:2048  lds\n"
    "v_mfma_f32_16x16x32_bf16 v[168:171], a[148:151], a[68:71], v[168:171]\n"
    "ds_read_b64_tr_b16 a[116:117], v245 offset:1024\n"
    "ds_read_b64_tr_b16 a[118:119], v245 offset:1536\n"
    "v_mfma_f32_16x16x32_bf16 v[168:171], a[152:155], a[72:75], v[168:171]\n"
    "ds_read_b64_tr_b16 a[120:121], v245 offset:2048\n"
    "ds_read_b64_tr_b16 a[122:123], v245 offset:2560\n"
    "v_mfma_f32_16x16x32_bf16 v[168:171], a[156:159], a[76:79], v[168:171]\n"
    "v_mfma_f32_16x16x32_bf16 v[172:175], a[144:147], a[80:83], v[172:175]\n"
    "s_waitcnt vmcnt(12)\n"
    "s_barrier\n"
    "buffer_load_dwordx4 v243, s[52:55], s81 offen offset:3072  lds\n"
    "v_mfma_f32_16x16x32_bf16 v[172:175], a[148:151], a[84:87], v[172:175]\n"
    "s_add_u32 m0, s73, 4096\n"
    "s_add_u32 s81, s81, 18432\n"
    "v_mfma_f32_16x16x32_bf16 v[172:175], a[152:155], a[88:91], v[172:175]\n"
    "ds_read_b64_tr_b16 a[124:125], v245 offset:3072\n"
    "ds_read_b64_tr_b16 a[126:127], v245 offset:3584\n"
    "v_mfma_f32_16x16x32_bf16 v[172:175], a[156:159], a[92:95], v[172:175]\n"
    "ds_read_b64_tr_b16 a[192:193], v237 offset:32768\n"
    "ds_read_b64_tr_b16 a[194:195], v237 offset:36864\n"
    "v_mfma_f32_16x16x32_bf16 v[176:179], a[160:163], a[64:67], v[176:179]\n"
    "buffer_load_dwordx4 v240, s[52:55], s81 offen offset:0  lds\n"
    "v_mfma_f32_16x16x32_bf16 v[176:179], a[164:167], a[68:71], v[176:179]\n"
    "ds_read_b64_tr_b16 a[196:197], v237 offset:40960\n"
    "ds_read_b64_tr_b16 a[198:199], v237 offset:45056\n"
    "v_mfma_f32_16x16x32_bf16 v[176:179], a[168:171], a[72:75], v[176:179]\n"
    "ds_read_b64_tr_b16 a[200:201], v237 offset:49152\n"
    "ds_read_b64_tr_b16 a[202:203], v237 offset:53248\n"
    "v_mfma_f32_16x16x32_bf16 v[176:179], a[172:175], a[76:79], v[176:179]\n"
    "v_mfma_f32_16x16x32_bf16 v[180:183], a[160:163], a[80:83], v[180:183]\n"
    "ds_read_b64_tr_b16 a[204:205], v237 offset:57344\n"
    "ds_read_b64_tr_b16 a[206:207], v237 offset:61440\n"
    "v_mfma_f32_16x16x32_bf16 v[180:183], a[164:167], a[84:87], v[180:183]\n"
    "buffer_load_dwordx4 v241, s[52:55], s81 offen offset:1024  lds\n"
    "v_mfma_f32_16x16x32_bf16 v[180:183], a[168:171], a[88:91], v[180:183]\n"
    "ds_read_b64_tr_b16 a[208:209], v238 offset:32768\n"
    "ds_read_b64_tr_b16 a[210:211], v238 offset:36864\n"
    "v_mfma_f32_16x16x32_bf16 v[180:183], a[172:175], a[92:95], v[180:183]\n"
    "s_waitcnt lgkmcnt(4)\n"
    "v_mfma_f32_16x16x32_bf16 v[184:187], a[176:179], a[64:67], v[184:187]\n"
    "ds_read_b64_tr_b16 a[212:213], v238 offset:40960\n"
    "ds_read_b64_tr_b16 a[214:215], v238 offset:45056\n"
    "v_mfma_f32_16x16x32_bf16 v[184:187], a[180:183], a[68:71], v[184:187]\n"
    "buffer_load_dwordx4 v242, s[52:55], s81 offen offset:2048  lds\n"
    "v_mfma_f32_16x16x32_bf16 v[184:187], a[184:187], a[72:75], v[184:187]\n"
    "ds_read_b64_tr_b16 a[216:217], v238 offset:49152\n"
    "ds_read_b64_tr_b16 a[218:219], v238 offset:53248\n"
    "v_mfma_f32_16x16x32_bf16 v[184:187], a[188:191], a[76:79], v[184:187]\n"
    "v_mfma_f32_16x16x32_bf16 v[188:191], a[176:179], a[80:83], v[188:191]\n"
    "ds_read_b64_tr_b16 a[220:221], v238 offset:57344\n"
    "ds_read_b64_tr_b16 a[222:223], v238 offset:61440\n"
    "v_mfma_f32_16x16x32_bf16 v[188:191], a[180:183], a[84:87], v[188:191]\n"
    "buffer_load_dwordx4 v243, s[52:55], s81 offen offset:3072  lds\n"
    "v_mfma_f32_16x16x32_bf16 v[188:191], a[184:187], a[88:91], v[188:191]\n"
    "ds_read_b64_tr_b16 a[224:225], v237 offset:33024\n"
    "ds_read_b64_tr_b16 a[226:227], v237 offset:37120\n"
    "v_mfma_f32_16x16x32_bf16 v[188:191], a[188:191], a[92:95], v[188:191]\n"
    "v_mfma_f32_16x16x32_bf16 v[192:195], a[112:115], a[64:67], v[192:195]\n"
    "ds_read_b64_tr_b16 a[228:229], v237 offset:41216\n"
    "ds_read_b64_tr_b16 a[230:231], v237 offset:45312\n"
    "v_mfma_f32_16x16x32_bf16 v[192:195], a[116:119], a[68:71], v[192:195]\n"
    "v_mfma_f32_16x16x32_bf16 v[192:195], a[120:123], a[72:75], v[192:195]\n"
    "buffer_load_dwordx4 v[36:39], v244, s[56:59], s77 offen offset:1024 sc1 \n"
    "v_mfma_f32_16x16x32_bf16 v[192:195], a[124:127], a[76:79], v[192:195]\n"
    "ds_read_b64_tr_b16 a[232:233], v237 offset:49408\n"
    "ds_read_b64_tr_b16 a[234:235], v237 offset:53504\n"
    "v_mfma_f32_16x16x32_bf16 v[196:199], a[112:115], a[80:83], v[196:199]\n"
    "ds_read_b64_tr_b16 a[236:237], v237 offset:57600\n"
    "ds_read_b64_tr_b16 a[238:239], v237 offset:61696\n"
    "v_mfma_f32_16x16x32_bf16 v[196:199], a[116:119], a[84:87], v[196:199]\n"
    "v_mfma_f32_16x16x32_bf16 v[196:199], a[120:123], a[88:91], v[196:199]\n"
    "buffer_load_dwordx4 v[68:71], v227, s[56:59], s77 offen offset:1024 sc1 \n"
    "v_mfma_f32_16x16x32_bf16 v[196:199], a[124:127], a[92:95], v[196:199]\n"
    "ds_read_b64_tr_b16 a[240:241], v238 offset:33024\n"
    "ds_read_b64_tr_b16 a[242:243], v238 offset:37120\n"
    "s_waitcnt vmcnt(18)\n"
    "v_mfma_f32_16x16x32_bf16 v[128:131], a[192:195], a[96:99], v[128:131]\n"
    "ds_read_b64_tr_b16 a[244:245], v238 offset:41216\n"
    "ds_read_b64_tr_b16 a[246:247], v238 offset:45312\n"
    "s_waitcnt vmcnt(16)\n"
    "v_mfma_f32_16x16x32_bf16 v[128:131], a[196:199], a[100:103], v[128:131]\n"
    "s_waitcnt vmcnt(14)\n"
    "v_mfma_f32_16x16x32_bf16 v[128:131], a[200:203], a[104:107], v[128:131]\n"
    "s_barrier\n"
    "buffer_load_dwordx4 v[44:47], v244, s[56:59], s36 offen offset:1024 sc1 \n"
    "s_waitcnt lgkmcnt(2)\n"
    "s_waitcnt vmcnt(13)\n"
    "v_mfma_f32_16x16x32_bf16 v[128:131], a[204:207], a[108:111], v[128:131]\n"
    "ds_read_b64_tr_b16 a[248:249], v238 offset:49408\n"
    "ds_read_b64_tr_b16 a[250:251], v238 offset:53504\n"
    "v_mfma_f32_16x16x32_bf16 v[132:135], a[192:195], v[4:7], v[132:135]\n"
    "ds_read_b64_tr_b16 a[252:253], v238 offset:57600\n"
    "ds_read_b64_tr_b16 a[254:255], v238 offset:61696\n"
    "v_mfma_f32_16x16x32_bf16 v[132:135], a[196:199], v[12:15], v[132:135]\n"
    "v_mfma_f32_16x16x32_bf16 v[132:135], a[200:203], v[20:23], v[132:135]\n"
    "buffer_load_dwordx4 v[76:79], v227, s[56:59], s36 offen offset:1024 sc1 \n"
    "s_waitcnt vmcnt(13)\n"
    "v_mfma_f32_16x16x32_bf16 v[132:135], a[204:207], v[28:31], v[132:135]\n"
    "ds_read_b64_tr_b16 a[128:129], v237 offset:33280\n"
    "ds_read_b64_tr_b16 a[130:131], v237 offset:37376\n"
    "v_mfma_f32_16x16x32_bf16 v[136:139], a[208:211], a[96:99], v[136:139]\n"
    "ds_read_b64_tr_b16 a[132:133], v237 offset:41472\n"
    "ds_read_b64_tr_b16 a[134:135], v237 offset:45568\n"
    "v_mfma_f32_16x16x32_bf16 v[136:139], a[212:215], a[100:103], v[136:139]\n"
    "v_mfma_f32_16x16x32_bf16 v[136:139], a[216:219], a[104:107], v[136:139]\n"
    "buffer_load_dwordx4 v[52:55], v244, s[56:59], s76 offen offset:2048 sc1 \n"
    "v_mfma_f32_16x16x32_bf16 v[136:139], a[220:223], a[108:111], v[136:139]\n"
    "ds_read_b64_tr_b16 a[136:137], v237 offset:49664\n"
    "ds_read_b64_tr_b16 a[138:139], v237 offset:53760\n"
    "v_mfma_f32_16x16x32_bf16 v[140:143], a[208:211], v[4:7], v[140:143]\n"
    "ds_read_b64_tr_b16 a[140:141], v237 offset:57856\n"
    "ds_read_b64_tr_b16 a[142:143], v237 offset:61952\n"
    "v_mfma_f32_16x16x32_bf16 v[140:143], a[212:215], v[12:15], v[140:143]\n"
    "v_mfma_f32_16x16x32_bf16 v[140:143], a[216:219], v[20:23], v[140:143]\n"
    "buffer_load_dwordx4 v[84:87], v227, s[56:59], s76 offen offset:2048 sc1 \n"
    "v_mfma_f32_16x16x32_bf16 v[140:143], a[220:223], v[28:31], v[140:143]\n"
    "ds_read_b64_tr_b16 a[144:145], v238 offset:33280\n"
    "ds_read_b64_tr_b16 a[146:147], v238 offset:37376\n"
    "v_mfma_f32_16x16x32_bf16 v[144:147], a[224:227], a[96:99], v[144:147]\n"
    "ds_read_b64_tr_b16 a[148:149], v238 offset:41472\n"
    "ds_read_b64_tr_b16 a[150:151], v238 offset:45568\n"
    "v_mfma_f32_16x16x32_bf16 v[144:147], a[228:231], a[100:103], v[144:147]\n"
    "v_mfma_f32_16x16x32_bf16 v[144:147], a[232:235], a[104:107], v[144:147]\n"
    "buffer_load_dwordx4 v[60:63], v244, s[56:59], s77 offen offset:2048 sc1 \n"
    "v_mfma_f32_16x16x32_bf16 v[144:147], a[236:239], a[108:111], v[144:147]\n"
    "ds_read_b64_tr_b16 a[152:153], v238 offset:49664\n"
    "ds_read_b64_tr_b16 a[154:155], v238 offset:53760\n"
    "v_mfma_f32_16x16x32_bf16 v[148:151], a[224:227], v[4:7], v[148:151]\n"
    "ds_read_b64_tr_b16 a[156:157], v238 offset:57856\n"
    "ds_read_b64_tr_b16 a[158:159], v238 offset:61952\n"
    "v_mfma_f32_16x16x32_bf16 v[148:151], a[228:231], v[12:15], v[148:151]\n"
    "v_mfma_f32_16x16x32_bf16 v[148:151], a[232:235], v[20:23], v[148:151]\n"
    "buffer_load_dwordx4 v[92:95], v227, s[56:59], s77 offen offset:2048 sc1 \n"
    "v_mfma_f32_16x16x32_bf16 v[148:151], a[236:239], v[28:31], v[148:151]\n"
    "v_readlane_b32 s81, v248, 5\n"
    "ds_read_b64_tr_b16 a[160:161], v237 offset:33536\n"
    "v_mfma_f32_16x16x32_bf16 v[152:155], a[240:243], a[96:99], v[152:155]\n"
    "s_add_u32 m0, s83, 155648\n"
    "ds_read_b64_tr_b16 a[162:163], v237 offset:37632\n"
    "s_waitcnt lgkmcnt(3)\n"
    "v_mfma_f32_16x16x32_bf16 v[152:155], a[244:247], a[100:103], v[152:155]\n"
    "ds_read_b64_tr_b16 a[164:165], v237 offset:41728\n"
    "ds_read_b64_tr_b16 a[166:167], v237 offset:45824\n"
    "v_mfma_f32_16x16x32_bf16 v[152:155], a[248:251], a[104:107], v[152:155]\n"
    "s_nop 0\n"
    "ds_read_b64_tr_b16 a[168:169], v237 offset:49920\n"
    "v_mfma_f32_16x16x32_bf16 v[152:155], a[252:255], a[108:111], v[152:155]\n"
    "buffer_load_dwordx4 v225, s[52:55], s81 offen lds\n"
    "v_mfma_f32_16x16x32_bf16 v[156:159], a[240:243], v[4:7], v[156:159]\n"
    "s_add_u32 m0, s73, 90112\n"
    "s_nop 0\n"
    "v_mfma_f32_16x16x32_bf16 v[156:159], a[244:247], v[12:15], v[156:159]\n"
    "ds_read_b64_tr_b16 a[170:171], v237 offset:54016\n"
    "ds_read_b64_tr_b16 a[172:173], v237 offset:58112\n"
    "v_mfma_f32_16x16x32_bf16 v[156:159], a[248:251], v[20:23], v[156:159]\n"
    "ds_read_b64_tr_b16 a[174:175], v237 offset:62208\n"
    "ds_read_b64_tr_b16 a[176:177], v238 offset:33536\n"
    "v_mfma_f32_16x16x32_bf16 v[156:159], a[252:255], v[28:31], v[156:159]\n"
    "buffer_load_dwordx4 v240, s[52:55], s81 offen offset:0  lds\n"
    "v_mfma_f32_16x16x32_bf16 v[160:163], a[128:131], a[96:99], v[160:163]\n"
    "ds_read_b64_tr_b16 a[178:179], v238 offset:37632\n"
    "ds_read_b64_tr_b16 a[180:181], v238 offset:41728\n"
    "v_mfma_f32_16x16x32_bf16 v[160:163], a[132:135], a[100:103], v[160:163]\n"
    "ds_read_b64_tr_b16 a[182:183], v238 offset:45824\n"
    "v_mfma_f32_16x16x32_bf16 v[160:163], a[136:139], a[104:107], v[160:163]\n"
    "ds_read_b64_tr_b16 a[184:185], v238 offset:49920\n"
    "ds_read_b64_tr_b16 a[186:187], v238 offset:54016\n"
    "v_mfma_f32_16x16x32_bf16 v[160:163], a[140:143], a[108:111], v[160:163]\n"
    "buffer_load_dwordx4 v241, s[52:55], s81 offen offset:1024  lds\n"
    "v_mfma_f32_16x16x32_bf16 v[164:167], a[128:131], v[4:7], v[164:167]\n"
    "ds_read_b64_tr_b16 a[188:189], v238 offset:58112\n"
    "ds_read_b64_tr_b16 a[190:191], v238 offset:62208\n"
    "v_mfma_f32_16x16x32_bf16 v[164:167], a[132:135], v[12:15], v[164:167]\n"
    "v_mfma_f32_16x16x32_bf16 v[164:167], a[136:139], v[20:23], v[164:167]\n"
    "ds_read_b64_tr_b16 a[112:113], v245 offset:4096\n"
    "ds_read_b64_tr_b16 a[114:115], v245 offset:4608\n"
    "v_mfma_f32_16x16x32_bf16 v[164:167], a[140:143], v[28:31], v[164:167]\n"
    "v_mfma_f32_16x16x32_bf16 v[168:171], a[144:147], a[96:99], v[168:171]\n"
    "buffer_load_dwordx4 v242, s[52:55], s81 offen offset:2048  lds\n"
    "v_mfma_f32_16x16x32_bf16 v[168:171], a[148:151], a[100:103], v[168:171]\n"
    "ds_read_b64_tr_b16 a[116:117], v245 offset:5120\n"
    "ds_read_b64_tr_b16 a[118:119], v245 offset:5632\n"
    "v_mfma_f32_16x16x32_bf16 v[168:171], a[152:155], a[104:107], v[168:171]\n"
    "ds_read_b64_tr_b16 a[120:121], v245 offset:6144\n"
    "ds_read_b64_tr_b16 a[122:123], v245 offset:6656\n"
    "s_waitcnt lgkmcnt(5)\n"
    "v_mfma_f32_16x16x32_bf16 v[168:171], a[156:159], a[108:111], v[168:171]\n"
    "v_mfma_f32_16x16x32_bf16 v[172:175], a[144:147], v[4:7], v[172:175]\n"
    "s_waitcnt vmcnt(12)\n"
    "s_barrier\n"
    "buffer_load_dwordx4 v243, s[52:55], s81 offen offset:3072  lds\n"
    "v_mfma_f32_16x16x32_bf16 v[172:175], a[148:151], v[12:15], v[172:175]\n"
    "s_add_u32 m0, s73, 94208\n"
    "s_add_u32 s81, s81, 18432\n"
    "v_mfma_f32_16x16x32_bf16 v[172:175], a[152:155], v[20:23], v[172:175]\n"
    "ds_read_b64_tr_b16 a[124:125], v245 offset:7168\n"
    "ds_read_b64_tr_b16 a[126:127], v245 offset:7680\n"
    "v_mfma_f32_16x16x32_bf16 v[172:175], a[156:159], v[28:31], v[172:175]\n"
    "ds_read_b64_tr_b16 a[192:193], v235 offset:0\n"
    "ds_read_b64_tr_b16 a[194:195], v235 offset:4096\n"
    "v_mfma_f32_16x16x32_bf16 v[176:179], a[160:163], a[96:99], v[176:179]\n"
    "buffer_load_dwordx4 v240, s[52:55], s81 offen offset:0  lds\n"
    "v_mfma_f32_16x16x32_bf16 v[176:179], a[164:167], a[100:103], v[176:179]\n"
    "ds_read_b64_tr_b16 a[196:197], v235 offset:8192\n"
    "ds_read_b64_tr_b16 a[198:199], v235 offset:12288\n"
    "v_mfma_f32_16x16x32_bf16 v[176:179], a[168:171], a[104:107], v[176:179]\n"
    "ds_read_b64_tr_b16 a[200:201], v235 offset:16384\n"
    "ds_read_b64_tr_b16 a[202:203], v235 offset:20480\n"
    "v_mfma_f32_16x16x32_bf16 v[176:179], a[172:175], a[108:111], v[176:179]\n"
    "v_mfma_f32_16x16x32_bf16 v[180:183], a[160:163], v[4:7], v[180:183]\n"
    "ds_read_b64_tr_b16 a[204:205], v235 offset:24576\n"
    "ds_read_b64_tr_b16 a[206:207], v235 offset:28672\n"
    "v_mfma_f32_16x16x32_bf16 v[180:183], a[164:167], v[12:15], v[180:183]\n"
    "buffer_load_dwordx4 v241, s[52:55], s81 offen offset:1024  lds\n"
    "v_mfma_f32_16x16x32_bf16 v[180:183], a[168:171], v[20:23], v[180:183]\n"
    "ds_read_b64_tr_b16 a[208:209], v236 offset:0\n"
    "ds_read_b64_tr_b16 a[210:211], v236 offset:4096\n"
    "v_mfma_f32_16x16x32_bf16 v[180:183], a[172:175], v[28:31], v[180:183]\n"
    "v_mfma_f32_16x16x32_bf16 v[184:187], a[176:179], a[96:99], v[184:187]\n"
    "ds_read_b64_tr_b16 a[212:213], v236 offset:8192\n"
    "ds_read_b64_tr_b16 a[214:215], v236 offset:12288\n"
    "v_mfma_f32_16x16x32_bf16 v[184:187], a[180:183], a[100:103], v[184:187]\n"
    "buffer_load_dwordx4 v242, s[52:55], s81 offen offset:2048  lds\n"
    "v_mfma_f32_16x16x32_bf16 v[184:187], a[184:187], a[104:107], v[184:187]\n"
    "ds_read_b64_tr_b16 a[216:217], v236 offset:16384\n"
    "ds_read_b64_tr_b16 a[218:219], v236 offset:20480\n"
    "v_mfma_f32_16x16x32_bf16 v[184:187], a[188:191], a[108:111], v[184:187]\n"
    "v_mfma_f32_16x16x32_bf16 v[188:191], a[176:179], v[4:7], v[188:191]\n"
    "ds_read_b64_tr_b16 a[220:221], v236 offset:24576\n"
    "ds_read_b64_tr_b16 a[222:223], v236 offset:28672\n"
    "v_mfma_f32_16x16x32_bf16 v[188:191], a[180:183], v[12:15], v[188:191]\n"
    "buffer_load_dwordx4 v243, s[52:55], s81 offen offset:3072  lds\n"
    "v_mfma_f32_16x16x32_bf16 v[188:191], a[184:187], v[20:23], v[188:191]\n"
    "ds_read_b64_tr_b16 a[224:225], v235 offset:256\n"
    "ds_read_b64_tr_b16 a[226:227], v235 offset:4352\n"
    "v_mfma_f32_16x16x32_bf16 v[188:191], a[188:191], v[28:31], v[188:191]\n"
    "s_waitcnt lgkmcnt(4)\n"
    "v_mfma_f32_16x16x32_bf16 v[192:195], a[112:115], a[96:99], v[192:195]\n"
    "ds_read_b64_tr_b16 a[228:229], v235 offset:8448\n"
    "ds_read_b64_tr_b16 a[230:231], v235 offset:12544\n"
    "v_mfma_f32_16x16x32_bf16 v[192:195], a[116:119], a[100:103], v[192:195]\n"
    "v_mfma_f32_16x16x32_bf16 v[192:195], a[120:123], a[104:107], v[192:195]\n"
    "buffer_load_dwordx4 v[100:103], v244, s[56:59], s36 offen offset:2048 sc1 \n"
    "v_mfma_f32_16x16x32_bf16 v[192:195], a[124:127], a[108:111], v[192:195]\n"
    "ds_read_b64_tr_b16 a[232:233], v235 offset:16640\n"
    "ds_read_b64_tr_b16 a[234:235], v235 offset:20736\n"
    "v_mfma_f32_16x16x32_bf16 v[196:199], a[112:115], v[4:7], v[196:199]\n"
    "ds_read_b64_tr_b16 a[236:237], v235 offset:24832\n"
    "ds_read_b64_tr_b16 a[238:239], v235 offset:28928\n"
    "v_mfma_f32_16x16x32_bf16 v[196:199], a[116:119], v[12:15], v[196:199]\n"
    "v_mfma_f32_16x16x32_bf16 v[196:199], a[120:123], v[20:23], v[196:199]\n"
    "buffer_load_dwordx4 v[0:3], v227, s[56:59], s36 offen offset:2048 sc1 \n"
    "v_mfma_f32_16x16x32_bf16 v[196:199], a[124:127], v[28:31], v[196:199]\n"
    "ds_read_b64_tr_b16 a[240:241], v236 offset:256\n"
    "ds_read_b64_tr_b16 a[242:243], v236 offset:4352\n"
    "s_waitcnt vmcnt(18)\n"
    "v_mfma_f32_16x16x32_bf16 v[128:131], a[192:195], v[36:39], v[128:131]\n"
    "ds_read_b64_tr_b16 a[244:245], v236 offset:8448\n"
    "ds_read_b64_tr_b16 a[246:247], v236 offset:12544\n"
    "s_waitcnt vmcnt(16)\n"
    "v_mfma_f32_16x16x32_bf16 v[128:131], a[196:199], v[44:47], v[128:131]\n"
    "s_waitcnt vmcnt(14)\n"
    "v_mfma_f32_16x16x32_bf16 v[128:131], a[200:203], v[52:55], v[128:131]\n"
    "buffer_load_dwordx4 v[108:111], v244, s[56:59], s76 offen offset:3072 sc1 \n"
    "s_waitcnt vmcnt(13)\n"
    "v_mfma_f32_16x16x32_bf16 v[128:131], a[204:207], v[60:63], v[128:131]\n"
    "ds_read_b64_tr_b16 a[248:249], v236 offset:16640\n"
    "ds_read_b64_tr_b16 a[250:251], v236 offset:20736\n"
    "v_mfma_f32_16x16x32_bf16 v[132:135], a[192:195], v[68:71], v[132:135]\n"
    "ds_read_b64_tr_b16 a[252:253], v236 offset:24832\n"
    "ds_read_b64_tr_b16 a[254:255], v236 offset:28928\n"
    "v_mfma_f32_16x16x32_bf16 v[132:135], a[196:199], v[76:79], v[132:135]\n"
    "v_mfma_f32_16x16x32_bf16 v[132:135], a[200:203], v[84:87], v[132:135]\n"
    "buffer_load_dwordx4 v[8:11], v227, s[56:59], s76 offen offset:3072 sc1 \n"
    "s_waitcnt vmcnt(13)\n"
    "v_mfma_f32_16x16x32_bf16 v[132:135], a[204:207], v[92:95], v[132:135]\n"
    "ds_read_b64_tr_b16 a[128:129], v235 offset:512\n"
    "ds_read_b64_tr_b16 a[130:131], v235 offset:4608\n"
    "v_mfma_f32_16x16x32_bf16 v[136:139], a[208:211], v[36:39], v[136:139]\n"
    "ds_read_b64_tr_b16 a[132:133], v235 offset:8704\n"
    "ds_read_b64_tr_b16 a[134:135], v235 offset:12800\n"
    "v_mfma_f32_16x16x32_bf16 v[136:139], a[212:215], v[44:47], v[136:139]\n"
    "v_mfma_f32_16x16x32_bf16 v[136:139], a[216:219], v[52:55], v[136:139]\n"
    "ds_read_b64_tr_b16 a[136:137], v235 offset:16896\n"
    "ds_read_b64_tr_b16 a[138:139], v235 offset:20992\n"
    "s_waitcnt lgkmcnt(6)\n"
    "v_mfma_f32_16x16x32_bf16 v[136:139], a[220:223], v[60:63], v[136:139]\n"
    "buffer_load_dwordx4 v[116:119], v244, s[56:59], s77 offen offset:3072 sc1 \n"
    "v_mfma_f32_16x16x32_bf16 v[140:143], a[208:211], v[68:71], v[140:143]\n"
    "ds_read_b64_tr_b16 a[140:141], v235 offset:25088\n"
    "ds_read_b64_tr_b16 a[142:143], v235 offset:29184\n"
    "v_mfma_f32_16x16x32_bf16 v[140:143], a[212:215], v[76:79], v[140:143]\n"
    "v_mfma_f32_16x16x32_bf16 v[140:143], a[216:219], v[84:87], v[140:143]\n"
    "ds_read_b64_tr_b16 a[144:145], v236 offset:512\n"
    "ds_read_b64_tr_b16 a[146:147], v236 offset:4608\n"
    "v_mfma_f32_16x16x32_bf16 v[140:143], a[220:223], v[92:95], v[140:143]\n"
    "buffer_load_dwordx4 v[16:19], v227, s[56:59], s77 offen offset:3072 sc1 \n"
    "v_mfma_f32_16x16x32_bf16 v[144:147], a[224:227], v[36:39], v[144:147]\n"
    "ds_read_b64_tr_b16 a[148:149], v236 offset:8704\n"
    "ds_read_b64_tr_b16 a[150:151], v236 offset:12800\n"
    "v_mfma_f32_16x16x32_bf16 v[144:147], a[228:231], v[44:47], v[144:147]\n"
    "v_mfma_f32_16x16x32_bf16 v[144:147], a[232:235], v[52:55], v[144:147]\n"
    "ds_read_b64_tr_b16 a[152:153], v236 offset:16896\n"
    "ds_read_b64_tr_b16 a[154:155], v236 offset:20992\n"
    "v_mfma_f32_16x16x32_bf16 v[144:147], a[236:239], v[60:63], v[144:147]\n"
    "buffer_load_dwordx4 v[124:127], v244, s[56:59], s36 offen offset:3072 sc1 \n"
    "v_mfma_f32_16x16x32_bf16 v[148:151], a[224:227], v[68:71], v[148:151]\n"
    "ds_read_b64_tr_b16 a[156:157], v236 offset:25088\n"
    "ds_read_b64_tr_b16 a[158:159], v236 offset:29184\n"
    "v_mfma_f32_16x16x32_bf16 v[148:151], a[228:231], v[76:79], v[148:151]\n"
    "v_mfma_f32_16x16x32_bf16 v[148:151], a[232:235], v[84:87], v[148:151]\n"
    "ds_read_b64_tr_b16 a[160:161], v235 offset:768\n"
    "ds_read_b64_tr_b16 a[162:163], v235 offset:4864\n"
    "v_mfma_f32_16x16x32_bf16 v[148:151], a[236:239], v[92:95], v[148:151]\n"
    "buffer_load_dwordx4 v[24:27], v227, s[56:59], s36 offen offset:3072 sc1 \n"
    "v_mfma_f32_16x16x32_bf16 v[152:155], a[240:243], v[36:39], v[152:155]\n"
    "ds_read_b64_tr_b16 a[164:165], v235 offset:8960\n"
    "ds_read_b64_tr_b16 a[166:167], v235 offset:13056\n"
    "v_mfma_f32_16x16x32_bf16 v[152:155], a[244:247], v[44:47], v[152:155]\n"
    "v_mfma_f32_16x16x32_bf16 v[152:155], a[248:251], v[52:55], v[152:155]\n"
    "ds_read_b64_tr_b16 a[168:169], v235 offset:17152\n"
    "ds_read_b64_tr_b16 a[170:171], v235 offset:21248\n"
    "v_mfma_f32_16x16x32_bf16 v[152:155], a[252:255], v[60:63], v[152:155]\n"
    "v_mfma_f32_16x16x32_bf16 v[156:159], a[240:243], v[68:71], v[156:159]\n"
    "ds_read_b64_tr_b16 a[172:173], v235 offset:25344\n"
    "ds_read_b64_tr_b16 a[174:175], v235 offset:29440\n"
    "v_mfma_f32_16x16x32_bf16 v[156:159], a[244:247], v[76:79], v[156:159]\n"
    "s_getpc_b64 s[80:81]\n"
    "CTB%=:\n"
    "s_add_u32 s80, s80, TAILB%=-CTB%=\n"
    "s_addc_u32 s81, s81, 0\n"
    "v_and_b32 v252, 0, v239\n"
    "v_lshl_add_u32 v252, v252, 7, s78\n"
    "s_lshr_b32 s87, %14, 3\n"
    "s_lshl_b32 s87, s87, 2\n"
    "v_add_u32 v252, s87, v252\n"
    "v_min_u32 v252, 79, v252\n"
    "v_lshlrev_b32 v252, 7, v252\n"
    "global_load_dword v254, v252, s[80:81]\n"
    "v_mfma_f32_16x16x32_bf16 v[156:159], a[248:251], v[84:87], v[156:159]\n"
    "ds_read_b64_tr_b16 a[176:177], v236 offset:768\n"
    "ds_read_b64_tr_b16 a[178:179], v236 offset:4864\n"
    "v_mfma_f32_16x16x32_bf16 v[156:159], a[252:255], v[92:95], v[156:159]\n"
    "s_waitcnt lgkmcnt(2)\n"
    "v_mfma_f32_16x16x32_bf16 v[160:163], a[128:131], v[36:39], v[160:163]\n"
    "s_mul_i32 s81, s78, 5120\n"
    "v_add_u32 v248, s81, v244\n"
    "v_mfma_f32_16x16x32_bf16 v[160:163], a[132:135], v[44:47], v[160:163]\n"
    "v_add_u32 v252, 4096, v248\n"
    "s_mov_b32 s92, 41984\n"
    "v_mfma_f32_16x16x32_bf16 v[160:163], a[136:139], v[52:55], v[160:163]\n"
    "buffer_load_dwordx4 v[32:35], v248, s[56:59], s92 offen offset:0 sc1\n"
    "v_mfma_f32_16x16x32_bf16 v[160:163], a[140:143], v[60:63], v[160:163]\n"
    "ds_read_b64_tr_b16 a[180:181], v236 offset:8960\n"
    "ds_read_b64_tr_b16 a[182:183], v236 offset:13056\n"
    "v_mfma_f32_16x16x32_bf16 v[164:167], a[128:131], v[68:71], v[164:167]\n"
    "ds_read_b64_tr_b16 a[184:185], v236 offset:17152\n"
    "ds_read_b64_tr_b16 a[186:187], v236 offset:21248\n"
    "v_mfma_f32_16x16x32_bf16 v[164:167], a[132:135], v[76:79], v[164:167]\n"
    "ds_read_b64_tr_b16 a[188:189], v236 offset:25344\n"
    "ds_read_b64_tr_b16 a[190:191], v236 offset:29440\n"
    "v_mfma_f32_16x16x32_bf16 v[164:167], a[136:139], v[84:87], v[164:167]\n"
    "buffer_load_dwordx4 v[40:43], v248, s[56:59], s92 offen offset:1024 sc1\n"
    "v_mfma_f32_16x16x32_bf16 v[164:167], a[140:143], v[92:95], v[164:167]\n"
    "ds_read_b64_tr_b16 a[112:113], v226 offset:0\n"
    "ds_read_b64_tr_b16 a[114:115], v226 offset:512\n"
    "v_mfma_f32_16x16x32_bf16 v[168:171], a[144:147], v[36:39], v[168:171]\n"
    "ds_read_b64_tr_b16 a[116:117], v226 offset:1024\n"
    "ds_read_b64_tr_b16 a[118:119], v226 offset:1536\n"
    "v_mfma_f32_16x16x32_bf16 v[168:171], a[148:151], v[44:47], v[168:171]\n"
    "v_mfma_f32_16x16x32_bf16 v[168:171], a[152:155], v[52:55], v[168:171]\n"
    "buffer_load_dwordx4 v[48:51], v248, s[56:59], s92 offen offset:2048 sc1\n"
    "v_mfma_f32_16x16x32_bf16 v[168:171], a[156:159], v[60:63], v[168:171]\n"
    "ds_read_b64_tr_b16 a[120:121], v226 offset:2048\n"
    "ds_read_b64_tr_b16 a[122:123], v226 offset:2560\n"
    "v_mfma_f32_16x16x32_bf16 v[172:175], a[144:147], v[68:71], v[172:175]\n"
    "s_waitcnt vmcnt(12)\n"
    "s_barrier\n"
    "v_mfma_f32_16x16x32_bf16 v[172:175], a[148:151], v[76:79], v[172:175]\n"
    "ds_read_b64_tr_b16 a[124:125], v226 offset:3072\n"
    "ds_read_b64_tr_b16 a[126:127], v226 offset:3584\n"
    "v_mfma_f32_16x16x32_bf16 v[172:175], a[152:155], v[84:87], v[172:175]\n"
    "ds_read_b64_tr_b16 a[192:193], v237 offset:0\n"
    "ds_read_b64_tr_b16 a[194:195], v237 offset:4096\n"
    "v_mfma_f32_16x16x32_bf16 v[172:175], a[156:159], v[92:95], v[172:175]\n"
    "buffer_load_dwordx4 v[56:59], v248, s[56:59], s92 offen offset:3072 sc1\n"
    "v_mfma_f32_16x16x32_bf16 v[176:179], a[160:163], v[36:39], v[176:179]\n"
    "ds_read_b64_tr_b16 a[196:197], v237 offset:8192\n"
    "ds_read_b64_tr_b16 a[198:199], v237 offset:12288\n"
    "v_mfma_f32_16x16x32_bf16 v[176:179], a[164:167], v[44:47], v[176:179]\n"
    "v_mfma_f32_16x16x32_bf16 v[176:179], a[168:171], v[52:55], v[176:179]\n"
    "ds_read_b64_tr_b16 a[200:201], v237 offset:16384\n"
    "ds_read_b64_tr_b16 a[202:203], v237 offset:20480\n"
    "v_mfma_f32_16x16x32_bf16 v[176:179], a[172:175], v[60:63], v[176:179]\n"
    "buffer_load_dwordx4 v[64:67], v252, s[56:59], s92 offen offset:0 sc1\n"
    "v_mfma_f32_16x16x32_bf16 v[180:183], a[160:163], v[68:71], v[180:183]\n"
    "ds_read_b64_tr_b16 a[204:205], v237 offset:24576\n"
    "ds_read_b64_tr_b16 a[206:207], v237 offset:28672\n"
    "v_mfma_f32_16x16x32_bf16 v[180:183], a[164:167], v[76:79], v[180:183]\n"
    "v_and_b32 v246, 15, v239\n"
    "v_mul_u32_u24 v246, 2304, v246\n"
    "v_mfma_f32_16x16x32_bf16 v[180:183], a[168:171], v[84:87], v[180:183]\n"
    "ds_read_b64_tr_b16 a[208:209], v238 offset:0\n"
    "ds_read_b64_tr_b16 a[210:211], v238 offset:4096\n"
    "v_mfma_f32_16x16x32_bf16 v[180:183], a[172:175], v[92:95], v[180:183]\n"
    "v_and_b32 v247, 48, v239\n"
    "v_add_u32 v249, v246, v247\n"
    "s_waitcnt lgkmcnt(3)\n"
    "v_mfma_f32_16x16x32_bf16 v[184:187], a[176:179], v[36:39], v[184:187]\n"
    "s_bitcmp1_b32 s78, 0\n"
    "s_cselect_b64 s[48:49], %7, %6\n"
    "s_and_b32 s49, s49, 0xffff\n"
    "v_mfma_f32_16x16x32_bf16 v[184:187], a[180:183], v[44:47], v[184:187]\n"
    "s_mov_b32 s51, 0x20000\n"
    "s_mov_b64 s[40:41], %8\n"
    "v_mfma_f32_16x16x32_bf16 v[184:187], a[184:187], v[52:55], v[184:187]\n"
    "s_and_b32 s41, s41, 0xffff\n"
    "s_mov_b32 s43, 0x20000\n"
    "v_mfma_f32_16x16x32_bf16 v[184:187], a[188:191], v[60:63], v[184:187]\n"
    "s_cmp_lt_u32 s39, 144\n"
    "s_cselect_b32 s50, 5308416, 0\n"
    "v_mfma_f32_16x16x32_bf16 v[188:191], a[176:179], v[68:71], v[188:191]\n"
    "s_mov_b32 s42, s50\n"
    "v_lshrrev_b32 v250, 2, v239\n"
    "v_mfma_f32_16x16x32_bf16 v[188:191], a[180:183], v[76:79], v[188:191]\n"
    "v_mul_u32_u24 v250, 9216, v250\n"
    "v_and_b32 v251, 3, v239\n"
    "v_mfma_f32_16x16x32_bf16 v[188:191], a[184:187], v[84:87], v[188:191]\n"
    "v_lshl_add_u32 v250, v251, 4, v250\n"
    "s_lshr_b32 s81, s78, 1\n"
    "v_mfma_f32_16x16x32_bf16 v[188:191], a[188:191], v[92:95], v[188:191]\n"
    "s_mul_i32 s81, s81, 147456\n"
    "s_lshl_b32 s80, s39, 6\n"
    "v_mfma_f32_16x16x32_bf16 v[192:195], a[112:115], v[36:39], v[192:195]\n"
    "s_add_u32 s81, s81, s80\n"
    "v_add_u32 v230, s81, v250\n"
    "v_mfma_f32_16x16x32_bf16 v[192:195], a[116:119], v[44:47], v[192:195]\n"
    "ds_read_b64_tr_b16 a[212:213], v238 offset:8192\n"
    "ds_read_b64_tr_b16 a[214:215], v238 offset:12288\n"
    "v_mfma_f32_16x16x32_bf16 v[192:195], a[120:123], v[52:55], v[192:195]\n"
    "ds_read_b64_tr_b16 a[216:217], v238 offset:16384\n"
    "ds_read_b64_tr_b16 a[218:219], v238 offset:20480\n"
    "v_mfma_f32_16x16x32_bf16 v[192:195], a[124:127], v[60:63], v[192:195]\n"
    "ds_read_b64_tr_b16 a[220:221], v238 offset:24576\n"
    "ds_read_b64_tr_b16 a[222:223], v238 offset:28672\n"
    "v_mfma_f32_16x16x32_bf16 v[196:199], a[112:115], v[68:71], v[196:199]\n"
    "ds_read_b64_tr_b16 a[224:225], v237 offset:256\n"
    "ds_read_b64_tr_b16 a[226:227], v237 offset:4352\n"
    "v_mfma_f32_16x16x32_bf16 v[196:199], a[116:119], v[76:79], v[196:199]\n"
    "ds_read_b64_tr_b16 a[228:229], v237 offset:8448\n"
    "ds_read_b64_tr_b16 a[230:231], v237 offset:12544\n"
    "v_mfma_f32_16x16x32_bf16 v[196:199], a[120:123], v[84:87], v[196:199]\n"
    "ds_read_b64_tr_b16 a[232:233], v237 offset:16640\n"
    "ds_read_b64_tr_b16 a[234:235], v237 offset:20736\n"
    "v_mfma_f32_16x16x32_bf16 v[196:199], a[124:127], v[92:95], v[196:199]\n"
    "ds_read_b64_tr_b16 a[236:237], v237 offset:24832\n"
    "ds_read_b64_tr_b16 a[238:239], v237 offset:28928\n"
    "s_waitcnt vmcnt(13)\n"
    "v_mfma_f32_16x16x32_bf16 v[128:131], a[192:195], v[100:103], v[128:131]\n"
    "ds_read_b64_tr_b16 a[240:241], v238 offset:256\n"
    "ds_read_b64_tr_b16 a[242:243], v238 offset:4352\n"
    "s_waitcnt vmcnt(11)\n"
    "v_mfma_f32_16x16x32_bf16 v[128:131], a[196:199], v[108:111], v[128:131]\n"
    "ds_read_b64_tr_b16 a[244:245], v238 offset:8448\n"
    "ds_read_b64_tr_b16 a[246:247], v238 offset:12544\n"
    "s_waitcnt vmcnt(9)\n"
    "v_mfma_f32_16x16x32_bf16 v[128:131], a[200:203], v[116:119], v[128:131]\n"
    "s_barrier\n"
    "s_waitcnt lgkmcnt(4)\n"
    "s_waitcnt vmcnt(7)\n"
    "v_mfma_f32_16x16x32_bf16 v[128:131], a[204:207], v[124:127], v[128:131]\n"
    "ds_read_b64_tr_b16 a[248:249], v238 offset:16640\n"
    "ds_read_b64_tr_b16 a[250:251], v238 offset:20736\n"
    "v_mfma_f32_16x16x32_bf16 v[132:135], a[192:195], v[0:3], v[132:135]\n"
    "ds_read_b64_tr_b16 a[252:253], v238 offset:24832\n"
    "ds_read_b64_tr_b16 a[254:255], v238 offset:28928\n"
    "v_mfma_f32_16x16x32_bf16 v[132:135], a[196:199], v[8:11], v[132:135]\n"
    "v_mfma_f32_16x16x32_bf16 v[132:135], a[200:203], v[16:19], v[132:135]\n"
    "ds_read_b64_tr_b16 a[128:129], v237 offset:512\n"
    "ds_read_b64_tr_b16 a[130:131], v237 offset:4608\n"
    "s_waitcnt vmcnt(6)\n"
    "v_mfma_f32_16x16x32_bf16 v[132:135], a[204:207], v[24:27], v[132:135]\n"
    "v_mfma_f32_16x16x32_bf16 v[136:139], a[208:211], v[100:103], v[136:139]\n"
    "ds_read_b64_tr_b16 a[132:133], v237 offset:8704\n"
    "ds_read_b64_tr_b16 a[134:135], v237 offset:12800\n"
    "v_mfma_f32_16x16x32_bf16 v[136:139], a[212:215], v[108:111], v[136:139]\n"
    "v_mfma_f32_16x16x32_bf16 v[136:139], a[216:219], v[116:119], v[136:139]\n"
    "ds_read_b64_tr_b16 a[136:137], v237 offset:16896\n"
    "ds_read_b64_tr_b16 a[138:139], v237 offset:20992\n"
    "v_mfma_f32_16x16x32_bf16 v[136:139], a[220:223], v[124:127], v[136:139]\n"
    "v_cvt_pk_bf16_f32 v128, v128, v129\n"
    "v_cvt_pk_bf16_f32 v129, v130, v131\n"
    "v_mfma_f32_16x16x32_bf16 v[140:143], a[208:211], v[0:3], v[140:143]\n"
    "v_cvt_pk_bf16_f32 v130, v132, v133\n"
    "v_cvt_pk_bf16_f32 v131, v134, v135\n"
    "v_mfma_f32_16x16x32_bf16 v[140:143], a[212:215], v[8:11], v[140:143]\n"
    "ds_read_b64_tr_b16 a[140:141], v237 offset:25088\n"
    "ds_read_b64_tr_b16 a[142:143], v237 offset:29184\n"
    "v_mfma_f32_16x16x32_bf16 v[140:143], a[216:219], v[16:19], v[140:143]\n"
    "ds_read_b64_tr_b16 a[144:145], v238 offset:512\n"
    "ds_read_b64_tr_b16 a[146:147], v238 offset:4608\n"
    "v_mfma_f32_16x16x32_bf16 v[140:143], a[220:223], v[24:27], v[140:143]\n"
    "buffer_store_dwordx4 v[128:131], v249, s[56:59], s91 offen offset:0 sc1\n"
    "v_mfma_f32_16x16x32_bf16 v[144:147], a[224:227], v[100:103], v[144:147]\n"
    "ds_read_b64_tr_b16 a[148:149], v238 offset:8704\n"
    "ds_read_b64_tr_b16 a[150:151], v238 offset:12800\n"
    "v_mfma_f32_16x16x32_bf16 v[144:147], a[228:231], v[108:111], v[144:147]\n"
    "v_mfma_f32_16x16x32_bf16 v[144:147], a[232:235], v[116:119], v[144:147]\n"
    "ds_read_b64_tr_b16 a[152:153], v238 offset:16896\n"
    "ds_read_b64_tr_b16 a[154:155], v238 offset:20992\n"
    "v_mfma_f32_16x16x32_bf16 v[144:147], a[236:239], v[124:127], v[144:147]\n"
    "v_cvt_pk_bf16_f32 v136, v136, v137\n"
    "v_cvt_pk_bf16_f32 v137, v138, v139\n"
    "v_mfma_f32_16x16x32_bf16 v[148:151], a[224:227], v[0:3], v[148:151]\n"
    "v_cvt_pk_bf16_f32 v138, v140, v141\n"
    "v_cvt_pk_bf16_f32 v139, v142, v143\n"
    "v_mfma_f32_16x16x32_bf16 v[148:151], a[228:231], v[8:11], v[148:151]\n"
    "ds_read_b64_tr_b16 a[156:157], v238 offset:25088\n"
    "ds_read_b64_tr_b16 a[158:159], v238 offset:29184\n"
    "v_mfma_f32_16x16x32_bf16 v[148:151], a[232:235], v[16:19], v[148:151]\n"
    "ds_read_b64_tr_b16 a[160:161], v237 offset:768\n"
    "ds_read_b64_tr_b16 a[162:163], v237 offset:4864\n"
    "v_mfma_f32_16x16x32_bf16 v[148:151], a[236:239], v[24:27], v[148:151]\n"
    "buffer_store_dwordx4 v[136:139], v249, s[56:59], s91 offen offset:64 sc1\n"
    "s_waitcnt lgkmcnt(4)\n"
    "v_mfma_f32_16x16x32_bf16 v[152:155], a[240:243], v[100:103], v[152:155]\n"
    "ds_read_b64_tr_b16 a[164:165], v237 offset:8960\n"
    "ds_read_b64_tr_b16 a[166:167], v237 offset:13056\n"
    "v_mfma_f32_16x16x32_bf16 v[152:155], a[244:247], v[108:111], v[152:155]\n"
    "v_mfma_f32_16x16x32_bf16 v[152:155], a[248:251], v[116:119], v[152:155]\n"
    "ds_read_b64_tr_b16 a[168:169], v237 offset:17152\n"
    "ds_read_b64_tr_b16 a[170:171], v237 offset:21248\n"
    "v_mfma_f32_16x16x32_bf16 v[152:155], a[252:255], v[124:127], v[152:155]\n"
    "v_cvt_pk_bf16_f32 v144, v144, v145\n"
    "v_cvt_pk_bf16_f32 v145, v146, v147\n"
    "v_mfma_f32_16x16x32_bf16 v[156:159], a[240:243], v[0:3], v[156:159]\n"
    "v_cvt_pk_bf16_f32 v146, v148, v149\n"
    "v_cvt_pk_bf16_f32 v147, v150, v151\n"
    "v_mfma_f32_16x16x32_bf16 v[156:159], a[244:247], v[8:11], v[156:159]\n"
    "ds_read_b64_tr_b16 a[172:173], v237 offset:25344\n"
    "ds_read_b64_tr_b16 a[174:175], v237 offset:29440\n"
    "v_mfma_f32_16x16x32_bf16 v[156:159], a[248:251], v[16:19], v[156:159]\n"
    "ds_read_b64_tr_b16 a[176:177], v238 offset:768\n"
    "ds_read_b64_tr_b16 a[178:179], v238 offset:4864\n"
    "v_mfma_f32_16x16x32_bf16 v[156:159], a[252:255], v[24:27], v[156:159]\n"
    "buffer_store_dwordx4 v[144:147], v249, s[56:59], s91 offen offset:128 sc1\n"
    "v_mfma_f32_16x16x32_bf16 v[160:163], a[128:131], v[100:103], v[160:163]\n"
    "ds_read_b64_tr_b16 a[180:181], v238 offset:8960\n"
    "ds_read_b64_tr_b16 a[182:183], v238 offset:13056\n"
    "v_mfma_f32_16x16x32_bf16 v[160:163], a[132:135], v[108:111], v[160:163]\n"
    "v_mfma_f32_16x16x32_bf16 v[160:163], a[136:139], v[116:119], v[160:163]\n"
    "ds_read_b64_tr_b16 a[184:185], v238 offset:17152\n"
    "ds_read_b64_tr_b16 a[186:187], v238 offset:21248\n"
    "v_mfma_f32_16x16x32_bf16 v[160:163], a[140:143], v[124:127], v[160:163]\n"
    "v_cvt_pk_bf16_f32 v152, v152, v153\n"
    "v_cvt_pk_bf16_f32 v153, v154, v155\n"
    "v_mfma_f32_16x16x32_bf16 v[164:167], a[128:131], v[0:3], v[164:167]\n"
    "v_cvt_pk_bf16_f32 v154, v156, v157\n"
    "v_cvt_pk_bf16_f32 v155, v158, v159\n"
    "v_mfma_f32_16x16x32_bf16 v[164:167], a[132:135], v[8:11], v[164:167]\n"
    "ds_read_b64_tr_b16 a[188:189], v238 offset:25344\n"
    "ds_read_b64_tr_b16 a[190:191], v238 offset:29440\n"
    "v_mfma_f32_16x16x32_bf16 v[164:167], a[136:139], v[16:19], v[164:167]\n"
    "ds_read_b64_tr_b16 a[112:113], v245 offset:0\n"
    "ds_read_b64_tr_b16 a[114:115], v245 offset:512\n"
    "v_mfma_f32_16x16x32_bf16 v[164:167], a[140:143], v[24:27], v[164:167]\n"
    "buffer_store_dwordx4 v[152:155], v249, s[56:59], s91 offen offset:192 sc1\n"
    "v_mfma_f32_16x16x32_bf16 v[168:171], a[144:147], v[100:103], v[168:171]\n"
    "ds_read_b64_tr_b16 a[116:117], v245 offset:1024\n"
    "ds_read_b64_tr_b16 a[118:119], v245 offset:1536\n"
    "v_mfma_f32_16x16x32_bf16 v[168:171], a[148:151], v[108:111], v[168:171]\n"
    "v_mfma_f32_16x16x32_bf16 v[168:171], a[152:155], v[116:119], v[168:171]\n"
    "ds_read_b64_tr_b16 a[120:121], v245 offset:2048\n"
    "ds_read_b64_tr_b16 a[122:123], v245 offset:2560\n"
    "s_waitcnt lgkmcnt(5)\n"
    "v_mfma_f32_16x16x32_bf16 v[168:171], a[156:159], v[124:127], v[168:171]\n"
    "v_cvt_pk_bf16_f32 v160, v160, v161\n"
    "v_cvt_pk_bf16_f32 v161, v162, v163\n"
    "v_mfma_f32_16x16x32_bf16 v[172:175], a[144:147], v[0:3], v[172:175]\n"
    "s_waitcnt vmcnt(7)\n"
    "v_min3_u32 v72, v33, v35, v41\n"
    "v_max3_u32 v73, v33, v35, v41\n"
    "s_waitcnt vmcnt(6)\n"
    "v_min3_u32 v72, v72, v43, v49\n"
    "v_max3_u32 v73, v73, v43, v49\n"
    "s_waitcnt vmcnt(5)\n"
    "v_min3_u32 v72, v72, v51, v57\n"
    "v_max3_u32 v73, v73, v51, v57\n"
    "s_waitcnt vmcnt(4)\n"
    "v_min3_u32 v72, v72, v59, v65\n"
    "v_max3_u32 v73, v73, v59, v65\n"
    "v_min3_u32 v72, v72, v67, v67\n"
    "v_max3_u32 v73, v73, v67, v67\n"
    "v_xor_b32 v72, s67, v72\n"
    "v_xor_b32 v73, s67, v73\n"
    "v_or_b32 v72, v72, v73\n"
    "v_cmp_ne_u32 vcc, 0, v72\n"
    "s_nop 1\n"
    "s_cmp_lg_u64 vcc, 0\n"
    "s_cbranch_scc1 TSM%=\n"
    "TDM%=:\n"
    "v_mfma_f32_16x16x32_bf16 v[172:175], a[148:151], v[8:11], v[172:175]\n"
    "v_cvt_pk_bf16_f32 v162, v164, v165\n"
    "v_cvt_pk_bf16_f32 v163, v166, v167\n"
    "v_mfma_f32_16x16x32_bf16 v[172:175], a[152:155], v[16:19], v[172:175]\n"
    "ds_read_b64_tr_b16 a[124:125], v245 offset:3072\n"
    "ds_read_b64_tr_b16 a[126:127], v245 offset:3584\n"
    "v_mfma_f32_16x16x32_bf16 v[172:175], a[156:159], v[24:27], v[172:175]\n"
    "buffer_store_dwordx4 v[160:163], v249, s[56:59], s91 offen offset:256 sc1\n"
    "v_mfma_f32_16x16x32_bf16 v[176:179], a[160:163], v[100:103], v[176:179]\n"
    "v_lshrrev_b32 v74, 1, v248\n"
    "v_add_u32 v75, 1024, v74\n"
    "v_mfma_f32_16x16x32_bf16 v[176:179], a[164:167], v[108:111], v[176:179]\n"
    "v_add_u32 v80, 2048, v74\n"
    "ds_write2_b32 v74, v32, v34 offset0:0 offset1:1\n"
    "v_mfma_f32_16x16x32_bf16 v[176:179], a[168:171], v[116:119], v[176:179]\n"
    "ds_write2_b32 v74, v40, v42 offset0:128 offset1:129\n"
    "ds_write2_b32 v75, v48, v50 offset0:0 offset1:1\n"
    "v_mfma_f32_16x16x32_bf16 v[176:179], a[172:175], v[124:127], v[176:179]\n"
    "v_cvt_pk_bf16_f32 v168, v168, v169\n"
    "v_cvt_pk_bf16_f32 v169, v170, v171\n"
    "v_mfma_f32_16x16x32_bf16 v[180:183], a[160:163], v[0:3], v[180:183]\n"
    "v_cvt_pk_bf16_f32 v170, v172, v173\n"
    "v_cvt_pk_bf16_f32 v171, v174, v175\n"
    "v_mfma_f32_16x16x32_bf16 v[180:183], a[164:167], v[8:11], v[180:183]\n"
    "ds_write2_b32 v75, v56, v58 offset0:128 offset1:129\n"
    "ds_write2_b32 v80, v64, v66 offset0:0 offset1:1\n"
    "v_mfma_f32_16x16x32_bf16 v[180:183], a[168:171], v[16:19], v[180:183]\n"
    "v_mfma_f32_16x16x32_bf16 v[180:183], a[172:175], v[24:27], v[180:183]\n"
    "buffer_store_dwordx4 v[168:171], v249, s[56:59], s91 offen offset:320 sc1\n"
    "v_mfma_f32_16x16x32_bf16 v[184:187], a[176:179], v[100:103], v[184:187]\n"
    "v_mfma_f32_16x16x32_bf16 v[184:187], a[180:183], v[108:111], v[184:187]\n"
    "v_mfma_f32_16x16x32_bf16 v[184:187], a[184:187], v[116:119], v[184:187]\n"
    "v_mfma_f32_16x16x32_bf16 v[184:187], a[188:191], v[124:127], v[184:187]\n"
    "v_cvt_pk_bf16_f32 v176, v176, v177\n"
    "v_cvt_pk_bf16_f32 v177, v178, v179\n"
    "v_mfma_f32_16x16x32_bf16 v[188:191], a[176:179], v[0:3], v[188:191]\n"
    "v_cvt_pk_bf16_f32 v178, v180, v181\n"
    "v_cvt_pk_bf16_f32 v179, v182, v183\n"
    "v_mfma_f32_16x16x32_bf16 v[188:191], a[180:183], v[8:11], v[188:191]\n"
    "v_mfma_f32_16x16x32_bf16 v[188:191], a[184:187], v[16:19], v[188:191]\n"
    "v_mfma_f32_16x16x32_bf16 v[188:191], a[188:191], v[24:27], v[188:191]\n"
    "buffer_store_dwordx4 v[176:179], v249, s[56:59], s91 offen offset:384 sc1\n"
    "s_waitcnt lgkmcnt(0)\n"
    "v_mfma_f32_16x16x32_bf16 v[192:195], a[112:115], v[100:103], v[192:195]\n"
    "v_mfma_f32_16x16x32_bf16 v[192:195], a[116:119], v[108:111], v[192:195]\n"
    "v_mfma_f32_16x16x32_bf16 v[192:195], a[120:123], v[116:119], v[192:195]\n"
    "v_mfma_f32_16x16x32_bf16 v[192:195], a[124:127], v[124:127], v[192:195]\n"
    "v_cvt_pk_bf16_f32 v184, v184, v185\n"
    "v_cvt_pk_bf16_f32 v185, v186, v187\n"
    "v_mfma_f32_16x16x32_bf16 v[196:199], a[112:115], v[0:3], v[196:199]\n"
    "v_cvt_pk_bf16_f32 v186, v188, v189\n"
    "v_cvt_pk_bf16_f32 v187, v190, v191\n"
    "v_mfma_f32_16x16x32_bf16 v[196:199], a[116:119], v[8:11], v[196:199]\n"
    "v_mfma_f32_16x16x32_bf16 v[196:199], a[120:123], v[16:19], v[196:199]\n"
    "v_mfma_f32_16x16x32_bf16 v[196:199], a[124:127], v[24:27], v[196:199]\n"
    "buffer_store_dwordx4 v[184:187], v249, s[56:59], s91 offen offset:448 sc1\n"
    "v_cvt_pk_bf16_f32 v192, v192, v193\n"
    "v_cvt_pk_bf16_f32 v193, v194, v195\n"
    "s_nop 4\n"
    "v_cvt_pk_bf16_f32 v194, v196, v197\n"
    "v_cvt_pk_bf16_f32 v195, v198, v199\n"
    "s_and_b32 s82, s88, 3\n"
    "s_mul_i32 s82, s82, 448\n"
    "s_sub_u32 s91, s91, s82\n"
    "buffer_store_dwordx4 v[192:195], v249, s[56:59], s91 offen offset:2048 sc1\n"
    "TAILB%=:\n"
    "s_lshl_b32 s84, s89, 8\n"
    "s_add_u32 s84, s84, 65536\n"
    "s_lshl_b32 s85, s88, 2\n"
    "s_add_u32 s85, s85, s84\n"
    "v_and_b32 v253, 15, v239\n"
    "v_lshlrev_b32 v252, 2, v253\n"
    "v_mov_b32 v247, 1\n"
    "v_lshrrev_b32 v248, 4, v239\n"
    "v_mul_u32_u24 v248, 2359296, v248\n"
    "v_lshl_add_u32 v248, v253, 4, v248\n"
    "s_lshl_b32 s86, s93, 2\n"
    "s_add_u32 s86, s86, s78\n"
    "s_mul_i32 s86, s86, 2304\n"
    "s_add_u32 s86, s86, 8388608\n"
    "s_mov_b32 s87, 0\n"
    "s_mov_b64 s[60:61], %0\n"
    "s_and_b32 s61, s61, 0xffff\n"
    "s_mov_b32 s62, 2359296\n"
    "s_mov_b32 s63, 0x20000\n"
    "s_mov_b64 s[64:65], %5\n"
    "s_and_b32 s65, s65, 0xffff\n"
    "s_mov_b32 s66, 1152\n"
    "s_mov_b32 s67, 0x20000\n"
    "v_and_b32 v250, 32, v239\n"
    "v_lshlrev_b32 v228, 2, v250\n"
    "v_and_b32 v250, 15, v239\n"
    "v_lshl_add_u32 v228, v250, 3, v228\n"
    "s_lshr_b32 s90, s93, 2\n"
    "s_lshl_b32 s90, s90, 5\n"
    "s_and_b32 s81, s93, 3\n"
    "s_lshl_b32 s81, s81, 2\n"
    "s_add_u32 s90, s90, s81\n"
    "s_add_u32 s90, s90, s78\n"
    "s_mul_i32 s81, s90, 1152\n"
    "v_and_b32 v250, 16, v239\n"
    "v_mul_u32_u24 v250, 1152, v250\n"
    "v_add3_u32 v229, v250, v228, s81\n"
    "buffer_load_dwordx2 v[200:201], v229, s[60:63], 0 offen offset:0\n"
    "buffer_load_dwordx2 v[202:203], v229, s[60:63], 0 offen offset:256\n"
    "buffer_load_dwordx2 v[204:205], v229, s[60:63], 0 offen offset:512\n"
    "buffer_load_dwordx2 v[206:207], v229, s[60:63], 0 offen offset:768\n"
    "buffer_load_dwordx2 v[208:209], v229, s[60:63], 0 offen offset:1024\n"
    "buffer_load_dwordx2 v[210:211], v228, s[64:67], 0 offen offset:0\n"
    "buffer_load_dwordx2 v[212:213], v228, s[64:67], 0 offen offset:256\n"
    "buffer_load_dwordx2 v[214:215], v228, s[64:67], 0 offen offset:512\n"
    "buffer_load_dwordx2 v[216:217], v228, s[64:67], 0 offen offset:768\n"
    "buffer_load_dwordx2 v[218:219], v228, s[64:67], 0 offen offset:1024\n"
    "s_waitcnt vmcnt(10) lgkmcnt(0)\n"
    "s_barrier\n"
    "buffer_store_dword v247, v234, s[56:59], s85 offen sc1\n"
    "s_add_u32 m0, s83, 12288\n"
    "s_mov_b32 s81, 0\n"
    "buffer_load_dwordx4 v230, s[48:51], s81 offen lds\n"
    "s_add_u32 m0, s83, 16384\n"
    "s_mov_b32 s81, 294912\n"
    "buffer_load_dwordx4 v230, s[48:51], s81 offen lds\n"
    "s_add_u32 m0, s83, 20480\n"
    "s_mov_b32 s81, 589824\n"
    "buffer_load_dwordx4 v230, s[48:51], s81 offen lds\n"
    "s_add_u32 m0, s83, 24576\n"
    "s_mov_b32 s81, 884736\n"
    "buffer_load_dwordx4 v230, s[48:51], s81 offen lds\n"
    "s_add_u32 m0, s83, 28672\n"
    "s_mov_b32 s81, 1179648\n"
    "buffer_load_dwordx4 v230, s[48:51], s81 offen lds\n"
    "s_add_u32 m0, s83, 32768\n"
    "s_mov_b32 s81, 1474560\n"
    "buffer_load_dwordx4 v230, s[48:51], s81 offen lds\n"
    "v_mov_b32 v163, 1\n"
    "v_lshlrev_b32 v160, 1, v228\n"
    "v_add_u32 v160, 0, v160\n"
    "ds_read_b128 v[0:3], v160 offset:0\n"
    "ds_read_b128 v[4:7], v160 offset:512\n"
    "ds_read_b128 v[8:11], v160 offset:1024\n"
    "ds_read_b128 v[12:15], v160 offset:1536\n"
    "ds_read_b128 v[16:19], v160 offset:2048\n"
    "ds_read_b128 v[20:23], v160 offset:2304\n"
    "ds_read_b128 v[24:27], v160 offset:2816\n"
    "ds_read_b128 v[28:31], v160 offset:3328\n"
    "ds_read_b128 v[32:35], v160 offset:3840\n"
    "ds_read_b128 v[36:39], v160 offset:4352\n"
    "ds_read_b128 v[40:43], v160 offset:4608\n"
    "ds_read_b128 v[44:47], v160 offset:5120\n"
    "ds_read_b128 v[48:51], v160 offset:5632\n"
    "ds_read_b128 v[52:55], v160 offset:6144\n"
    "ds_read_b128 v[56:59], v160 offset:6656\n"
    "ds_read_b128 v[60:63], v160 offset:6912\n"
    "ds_read_b128 v[64:67], v160 offset:7424\n"
    "ds_read_b128 v[68:71], v160 offset:7936\n"
    "ds_read_b128 v[72:75], v160 offset:8448\n"
    "ds_read_b128 v[76:79], v160 offset:8960\n"
    "s_waitcnt vmcnt(16)\n"
    "v_lshlrev_b32 v80, 16, v200\n"
    "v_and_b32 v81, 0xffff0000, v200\n"
    "s_waitcnt vmcnt(11)\n"
    "v_lshlrev_b32 v100, 16, v210\n"
    "v_and_b32 v101, 0xffff0000, v210\n"
    "v_lshlrev_b32 v82, 16, v201\n"
    "v_and_b32 v83, 0xffff0000, v201\n"
    "v_lshlrev_b32 v102, 16, v211\n"
    "v_and_b32 v103, 0xffff0000, v211\n"
    "v_lshlrev_b32 v84, 16, v202\n"
    "v_and_b32 v85, 0xffff0000, v202\n"
    "s_waitcnt vmcnt(10)\n"
    "v_lshlrev_b32 v104, 16, v212\n"
    "v_and_b32 v105, 0xffff0000, v212\n"
    "v_lshlrev_b32 v86, 16, v203\n"
    "v_and_b32 v87, 0xffff0000, v203\n"
    "v_lshlrev_b32 v106, 16, v213\n"
    "v_and_b32 v107, 0xffff0000, v213\n"
    "v_lshlrev_b32 v88, 16, v204\n"
    "v_and_b32 v89, 0xffff0000, v204\n"
    "s_waitcnt vmcnt(9)\n"
    "v_lshlrev_b32 v108, 16, v214\n"
    "v_and_b32 v109, 0xffff0000, v214\n"
    "v_lshlrev_b32 v90, 16, v205\n"
    "v_and_b32 v91, 0xffff0000, v205\n"
    "v_lshlrev_b32 v110, 16, v215\n"
    "v_and_b32 v111, 0xffff0000, v215\n"
    "v_lshlrev_b32 v92, 16, v206\n"
    "v_and_b32 v93, 0xffff0000, v206\n"
    "s_waitcnt vmcnt(8)\n"
    "v_lshlrev_b32 v112, 16, v216\n"
    "v_and_b32 v113, 0xffff0000, v216\n"
    "v_lshlrev_b32 v94, 16, v207\n"
    "v_and_b32 v95, 0xffff0000, v207\n"
    "v_lshlrev_b32 v114, 16, v217\n"
    "v_and_b32 v115, 0xffff0000, v217\n"
    "v_lshlrev_b32 v96, 16, v208\n"
    "v_and_b32 v97, 0xffff0000, v208\n"
    "s_waitcnt vmcnt(7)\n"
    "v_lshlrev_b32 v116, 16, v218\n"
    "v_and_b32 v117, 0xffff0000, v218\n"
    "v_lshlrev_b32 v98, 16, v209\n"
    "v_and_b32 v99, 0xffff0000, v209\n"
    "v_lshlrev_b32 v118, 16, v219\n"
    "v_and_b32 v119, 0xffff0000, v219\n"
    "v_and_b32 v160, 1, v239\n"
    "v_lshlrev_b32 v160, 4, v160\n"
    "v_lshlrev_b32 v160, v160, v163\n"
    "v_mul_u32_u24 v160, 0x3f80, v160\n"
    "v_bfe_u32 v161, v239, 1, 3\n"
    "v_bfe_u32 v162, v239, 1, 2\n"
    "v_cmp_eq_u32 vcc, 0, v161\n"
    "s_nop 1\n"
    "v_cndmask_b32 v168, 0, v160, vcc\n"
    "v_cmp_eq_u32 vcc, 1, v161\n"
    "s_nop 1\n"
    "v_cndmask_b32 v169, 0, v160, vcc\n"
    "v_cmp_eq_u32 vcc, 2, v161\n"
    "s_nop 1\n"
    "v_cndmask_b32 v170, 0, v160, vcc\n"
    "v_cmp_eq_u32 vcc, 3, v161\n"
    "s_nop 1\n"
    "v_cndmask_b32 v171, 0, v160, vcc\n"
    "v_cmp_eq_u32 vcc, 4, v161\n"
    "s_nop 1\n"
    "v_cndmask_b32 v172, 0, v160, vcc\n"
    "v_cmp_eq_u32 vcc, 5, v161\n"
    "s_nop 1\n"
    "v_cndmask_b32 v173, 0, v160, vcc\n"
    "v_cmp_eq_u32 vcc, 6, v161\n"
    "s_nop 1\n"
    "v_cndmask_b32 v174, 0, v160, vcc\n"
    "v_cmp_eq_u32 vcc, 7, v161\n"
    "s_nop 1\n"
    "v_cndmask_b32 v175, 0, v160, vcc\n"
    "v_and_b32 v160, 1, v239\n"
    "v_cmp_eq_u32 vcc, 1, v160\n"
    "v_mov_b32 v160, 0xc0c0100\n"
    "v_mov_b32 v161, 0x1000c0c\n"
    "v_cndmask_b32 v210, v160, v161, vcc\n"
    "v_mov_b32 v160, 0xc0c0302\n"
    "v_mov_b32 v161, 0x3020c0c\n"
    "v_cndmask_b32 v211, v160, v161, vcc\n"
    "v_mov_b32 v160, 0x0c0c0c0c\n"
    "v_cmp_eq_u32 vcc, 0, v162\n"
    "s_nop 1\n"
    "v_cndmask_b32 v220, v160, v210, vcc\n"
    "v_cndmask_b32 v221, v160, v211, vcc\n"
    "v_cmp_eq_u32 vcc, 1, v162\n"
    "s_nop 1\n"
    "v_cndmask_b32 v222, v160, v210, vcc\n"
    "v_cndmask_b32 v223, v160, v211, vcc\n"
    "v_cmp_eq_u32 vcc, 2, v162\n"
    "s_nop 1\n"
    "v_cndmask_b32 v224, v160, v210, vcc\n"
    "v_cndmask_b32 v225, v160, v211, vcc\n"
    "v_cmp_eq_u32 vcc, 3, v162\n"
    "s_nop 1\n"
    "v_cndmask_b32 v226, v160, v210, vcc\n"
    "v_cndmask_b32 v227, v160, v211, vcc\n"
    "s_sub_u32 s81, 0x7ff, s90\n"
    "v_and_b32 v160, 16, v239\n"
    "v_sub_u32 v250, s81, v160\n"
    "v_lshrrev_b32 v160, 2, v160\n"
    "s_lshl_b32 s81, s78, 3\n"
    "s_add_u32 s81, s81, 86016\n"
    "v_add_u32 v251, s81, v160\n"
    "s_mov_b64 s[68:69], %9\n"
    "s_and_b32 s69, s69, 0xffff\n"
    "s_mov_b32 s70, 2359296\n"
    "s_mov_b32 s71, 0x20000\n"
    "POLL%=:\n"
    "buffer_load_dword v246, v252, s[56:59], s84 offen sc1\n"
    "s_waitcnt vmcnt(0)\n"
    "v_cmp_eq_u32 vcc, 0, v246\n"
    "s_nop 1\n"
    "s_cmp_eq_u64 vcc, 0\n"
    "s_cbranch_scc1 PDONE%=\n"
    "s_add_u32 s87, s87, 1\n"
    "s_cmp_gt_u32 s87, 262144\n"
    "s_cbranch_scc1 PDONE%=\n"
    "s_sleep 1\n"
    "s_branch POLL%=\n"
    "PDONE%=:\n"
    "s_waitcnt lgkmcnt(0)\n"
    "s_mov_b32 s87, 0\n"
    "PLOAD%=:\n"
    "buffer_load_dwordx4 v[140:143], v248, s[56:59], s86 offen offset:0 sc1\n"
    "buffer_load_dwordx4 v[144:147], v248, s[56:59], s86 offen offset:256 sc1\n"
    "buffer_load_dwordx4 v[148:151], v248, s[56:59], s86 offen offset:512 sc1\n"
    "buffer_load_dwordx4 v[152:155], v248, s[56:59], s86 offen offset:768 sc1\n"
    "buffer_load_dwordx4 v[156:159], v248, s[56:59], s86 offen offset:1024 sc1\n"
    "buffer_load_dwordx4 v[176:179], v248, s[56:59], s86 offen offset:1280 sc1\n"
    "buffer_load_dwordx4 v[180:183], v248, s[56:59], s86 offen offset:1536 sc1\n"
    "buffer_load_dwordx4 v[184:187], v248, s[56:59], s86 offen offset:1792 sc1\n"
    "buffer_load_dwordx4 v[188:191], v248, s[56:59], s86 offen offset:2048 sc1\n"
    "s_waitcnt vmcnt(8)\n"
    "v_mfma_f32_16x16x32_bf16 v[120:123], v[168:171], v[140:143], 0\n"
    "s_waitcnt vmcnt(7)\n"
    "v_mfma_f32_16x16x32_bf16 v[120:123], v[172:175], v[144:147], v[120:123]\n"
    "s_waitcnt vmcnt(6)\n"
    "v_mfma_f32_16x16x32_bf16 v[124:127], v[168:171], v[148:151], 0\n"
    "s_waitcnt vmcnt(5)\n"
    "v_mfma_f32_16x16x32_bf16 v[124:127], v[172:175], v[152:155], v[124:127]\n"
    "s_waitcnt vmcnt(4)\n"
    "v_mfma_f32_16x16x32_bf16 v[128:131], v[168:171], v[156:159], 0\n"
    "s_waitcnt vmcnt(3)\n"
    "v_mfma_f32_16x16x32_bf16 v[128:131], v[172:175], v[176:179], v[128:131]\n"
    "s_waitcnt vmcnt(2)\n"
    "v_mfma_f32_16x16x32_bf16 v[132:135], v[168:171], v[180:183], 0\n"
    "s_waitcnt vmcnt(1)\n"
    "v_mfma_f32_16x16x32_bf16 v[132:135], v[172:175], v[184:187], v[132:135]\n"
    "s_waitcnt vmcnt(0)\n"
    "v_mfma_f32_16x16x32_bf16 v[136:139], v[168:171], v[188:191], 0\n"
    "v_mov_b32 v176, 0\n"
    "v_mov_b32 v177, 0\n"
    "v_pk_fma_f32 v[80:81], v[0:1], v[120:121], v[80:81]\n"
    "v_cvt_pk_bf16_f32 v150, v80, v81\n"
    "v_lshlrev_b32 v80, 16, v150\n"
    "v_and_b32 v81, 0xffff0000, v150\n"
    "v_pk_add_f32 v[176:177], v[176:177], v[80:81]\n"
    "v_pk_fma_f32 v[82:83], v[2:3], v[122:123], v[82:83]\n"
    "v_cvt_pk_bf16_f32 v151, v82, v83\n"
    "v_lshlrev_b32 v82, 16, v151\n"
    "v_and_b32 v83, 0xffff0000, v151\n"
    "v_pk_add_f32 v[176:177], v[176:177], v[82:83]\n"
    "v_pk_fma_f32 v[84:85], v[4:5], v[124:125], v[84:85]\n"
    "v_cvt_pk_bf16_f32 v152, v84, v85\n"
    "v_lshlrev_b32 v84, 16, v152\n"
    "v_and_b32 v85, 0xffff0000, v152\n"
    "v_pk_add_f32 v[176:177], v[176:177], v[84:85]\n"
    "v_pk_fma_f32 v[86:87], v[6:7], v[126:127], v[86:87]\n"
    "v_cvt_pk_bf16_f32 v153, v86, v87\n"
    "v_lshlrev_b32 v86, 16, v153\n"
    "v_and_b32 v87, 0xffff0000, v153\n"
    "v_pk_add_f32 v[176:177], v[176:177], v[86:87]\n"
    "v_pk_fma_f32 v[88:89], v[8:9], v[128:129], v[88:89]\n"
    "v_cvt_pk_bf16_f32 v154, v88, v89\n"
    "v_lshlrev_b32 v88, 16, v154\n"
    "v_and_b32 v89, 0xffff0000, v154\n"
    "v_pk_add_f32 v[176:177], v[176:177], v[88:89]\n"
    "v_pk_fma_f32 v[90:91], v[10:11], v[130:131], v[90:91]\n"
    "v_cvt_pk_bf16_f32 v155, v90, v91\n"
    "v_lshlrev_b32 v90, 16, v155\n"
    "v_and_b32 v91, 0xffff0000, v155\n"
    "v_pk_add_f32 v[176:177], v[176:177], v[90:91]\n"
    "v_pk_fma_f32 v[92:93], v[12:13], v[132:133], v[92:93]\n"
    "v_cvt_pk_bf16_f32 v156, v92, v93\n"
    "v_lshlrev_b32 v92, 16, v156\n"
    "v_and_b32 v93, 0xffff0000, v156\n"
    "v_pk_add_f32 v[176:177], v[176:177], v[92:93]\n"
    "v_pk_fma_f32 v[94:95], v[14:15], v[134:135], v[94:95]\n"
    "v_cvt_pk_bf16_f32 v157, v94, v95\n"
    "v_lshlrev_b32 v94, 16, v157\n"
    "v_and_b32 v95, 0xffff0000, v157\n"
    "v_pk_add_f32 v[176:177], v[176:177], v[94:95]\n"
    "s_mov_b32 exec_hi, 0\n"
    "v_pk_fma_f32 v[96:97], v[16:17], v[136:137], v[96:97]\n"
    "v_cvt_pk_bf16_f32 v158, v96, v97\n"
    "v_lshlrev_b32 v96, 16, v158\n"
    "v_and_b32 v97, 0xffff0000, v158\n"
    "v_pk_add_f32 v[176:177], v[176:177], v[96:97]\n"
    "v_pk_fma_f32 v[98:99], v[18:19], v[138:139], v[98:99]\n"
    "v_cvt_pk_bf16_f32 v159, v98, v99\n"
    "v_lshlrev_b32 v98, 16, v159\n"
    "v_and_b32 v99, 0xffff0000, v159\n"
    "v_pk_add_f32 v[176:177], v[176:177], v[98:99]\n"
    "s_mov_b32 exec_hi, -1\n"
    "v_add_f32 v176, v176, v177\n"
    "s_nop 1\n"
    "v_add_f32_dpp v176, v176, v176 row_ror:1 row_mask:0xf bank_mask:0xf\n"
    "s_nop 1\n"
    "v_add_f32_dpp v176, v176, v176 row_ror:2 row_mask:0xf bank_mask:0xf\n"
    "s_nop 1\n"
    "v_add_f32_dpp v176, v176, v176 row_ror:4 row_mask:0xf bank_mask:0xf\n"
    "s_nop 1\n"
    "v_add_f32_dpp v176, v176, v176 row_ror:8 row_mask:0xf bank_mask:0xf\n"
    "v_mov_b32 v160, v176\n"
    "s_nop 1\n"
    "v_permlane32_swap_b32 v176, v160\n"
    "s_nop 1\n"
    "v_add_f32 v176, v176, v160\n"
    "v_mul_f32 v178, 0x3ae38e39, v176\n"
    "v_mov_b32 v176, 0\n"
    "v_mov_b32 v177, 0\n"
    "v_pk_add_f32 v[180:181], v[80:81], v[178:179] op_sel_hi:[1,0] neg_lo:[0,1] neg_hi:[0,1]\n"
    "v_pk_fma_f32 v[176:177], v[180:181], v[180:181], v[176:177]\n"
    "v_pk_add_f32 v[182:183], v[82:83], v[178:179] op_sel_hi:[1,0] neg_lo:[0,1] neg_hi:[0,1]\n"
    "v_pk_fma_f32 v[176:177], v[182:183], v[182:183], v[176:177]\n"
    "v_pk_add_f32 v[184:185], v[84:85], v[178:179] op_sel_hi:[1,0] neg_lo:[0,1] neg_hi:[0,1]\n"
    "v_pk_fma_f32 v[176:177], v[184:185], v[184:185], v[176:177]\n"
    "v_pk_add_f32 v[186:187], v[86:87], v[178:179] op_sel_hi:[1,0] neg_lo:[0,1] neg_hi:[0,1]\n"
    "v_pk_fma_f32 v[176:177], v[186:187], v[186:187], v[176:177]\n"
    "v_pk_add_f32 v[188:189], v[88:89], v[178:179] op_sel_hi:[1,0] neg_lo:[0,1] neg_hi:[0,1]\n"
    "v_pk_fma_f32 v[176:177], v[188:189], v[188:189], v[176:177]\n"
    "v_pk_add_f32 v[190:191], v[90:91], v[178:179] op_sel_hi:[1,0] neg_lo:[0,1] neg_hi:[0,1]\n"
    "v_pk_fma_f32 v[176:177], v[190:191], v[190:191], v[176:177]\n"
    "v_pk_add_f32 v[192:193], v[92:93], v[178:179] op_sel_hi:[1,0] neg_lo:[0,1] neg_hi:[0,1]\n"
    "v_pk_fma_f32 v[176:177], v[192:193], v[192:193], v[176:177]\n"
    "v_pk_add_f32 v[194:195], v[94:95], v[178:179] op_sel_hi:[1,0] neg_lo:[0,1] neg_hi:[0,1]\n"
    "v_pk_fma_f32 v[176:177], v[194:195], v[194:195], v[176:177]\n"
    "s_mov_b32 exec_hi, 0\n"
    "v_pk_add_f32 v[196:197], v[96:97], v[178:179] op_sel_hi:[1,0] neg_lo:[0,1] neg_hi:[0,1]\n"
    "v_pk_fma_f32 v[176:177], v[196:197], v[196:197], v[176:177]\n"
    "v_pk_add_f32 v[198:199], v[98:99], v[178:179] op_sel_hi:[1,0] neg_lo:[0,1] neg_hi:[0,1]\n"
    "v_pk_fma_f32 v[176:177], v[198:199], v[198:199], v[176:177]\n"
    "s_mov_b32 exec_hi, -1\n"
    "v_add_f32 v176, v176, v177\n"
    "v_cvt_pk_bf16_f32 v178, v178, v178\n"
    "v_and_b32 v178, 0xffff0000, v178\n"
    "v_add_f32_dpp v176, v176, v176 row_ror:1 row_mask:0xf bank_mask:0xf\n"
    "s_nop 1\n"
    "v_add_f32_dpp v176, v176, v176 row_ror:2 row_mask:0xf bank_mask:0xf\n"
    "s_nop 1\n"
    "v_add_f32_dpp v176, v176, v176 row_ror:4 row_mask:0xf bank_mask:0xf\n"
    "s_nop 1\n"
    "v_add_f32_dpp v176, v176, v176 row_ror:8 row_mask:0xf bank_mask:0xf\n"
    "v_mov_b32 v160, v176\n"
    "s_nop 1\n"
    "v_permlane32_swap_b32 v176, v160\n"
    "s_nop 1\n"
    "v_add_f32 v176, v176, v160\n"
    "v_mul_f32 v176, 0x3ae38e39, v176\n"
    "v_cvt_pk_bf16_f32 v176, v176, v176\n"
    "v_and_b32 v176, 0xffff0000, v176\n"
    "v_add_f32 v176, 0x3727c5ac, v176\n"
    "v_cvt_pk_bf16_f32 v176, v176, v176\n"
    "v_and_b32 v176, 0xffff0000, v176\n"
    "v_sqrt_f32 v176, v176\n"
    "v_pk_add_f32 v[180:181], v[80:81], v[178:179] op_sel_hi:[1,0] neg_lo:[0,1] neg_hi:[0,1]\n"
    "v_pk_mul_f32 v[180:181], v[180:181], v[40:41]\n"
    "v_pk_add_f32 v[182:183], v[82:83], v[178:179] op_sel_hi:[1,0] neg_lo:[0,1] neg_hi:[0,1]\n"
    "v_pk_mul_f32 v[182:183], v[182:183], v[42:43]\n"
    "v_cvt_pk_bf16_f32 v176, v176, v176\n"
    "v_and_b32 v176, 0xffff0000, v176\n"
    "v_rcp_f32 v179, v176\n"
    "s_nop 0\n"
    "v_pk_add_f32 v[184:185], v[84:85], v[178:179] op_sel_hi:[1,0] neg_lo:[0,1] neg_hi:[0,1]\n"
    "v_pk_mul_f32 v[184:185], v[184:185], v[44:45]\n"
    "v_pk_add_f32 v[186:187], v[86:87], v[178:179] op_sel_hi:[1,0] neg_lo:[0,1] neg_hi:[0,1]\n"
    "v_pk_mul_f32 v[186:187], v[186:187], v[46:47]\n"
    "v_pk_add_f32 v[188:189], v[88:89], v[178:179] op_sel_hi:[1,0] neg_lo:[0,1] neg_hi:[0,1]\n"
    "v_pk_mul_f32 v[188:189], v[188:189], v[48:49]\n"
    "v_pk_add_f32 v[190:191], v[90:91], v[178:179] op_sel_hi:[1,0] neg_lo:[0,1] neg_hi:[0,1]\n"
    "v_pk_mul_f32 v[190:191], v[190:191], v[50:51]\n"
    "v_pk_add_f32 v[192:193], v[92:93], v[178:179] op_sel_hi:[1,0] neg_lo:[0,1] neg_hi:[0,1]\n"
    "v_pk_mul_f32 v[192:193], v[192:193], v[52:53]\n"
    "v_pk_add_f32 v[194:195], v[94:95], v[178:179] op_sel_hi:[1,0] neg_lo:[0,1] neg_hi:[0,1]\n"
    "v_pk_mul_f32 v[194:195], v[194:195], v[54:55]\n"
    "v_pk_add_f32 v[196:197], v[96:97], v[178:179] op_sel_hi:[1,0] neg_lo:[0,1] neg_hi:[0,1]\n"
    "v_pk_mul_f32 v[196:197], v[196:197], v[56:57]\n"
    "v_pk_add_f32 v[198:199], v[98:99], v[178:179] op_sel_hi:[1,0] neg_lo:[0,1] neg_hi:[0,1]\n"
    "v_pk_mul_f32 v[198:199], v[198:199], v[58:59]\n"
    "v_mov_b32 v176, 0\n"
    "v_mov_b32 v177, 0\n"
    "v_pk_fma_f32 v[180:181], v[180:181], v[178:179], v[20:21] op_sel:[0,1,0] op_sel_hi:[1,1,1]\n"
    "v_cvt_pk_bf16_f32 v140, v180, v181\n"
    "v_lshlrev_b32 v180, 16, v140\n"
    "v_and_b32 v181, 0xffff0000, v140\n"
    "v_pk_fma_f32 v[176:177], v[180:181], v[100:101], v[176:177]\n"
    "v_pk_fma_f32 v[182:183], v[182:183], v[178:179], v[22:23] op_sel:[0,1,0] op_sel_hi:[1,1,1]\n"
    "v_cvt_pk_bf16_f32 v141, v182, v183\n"
    "v_lshlrev_b32 v182, 16, v141\n"
    "v_and_b32 v183, 0xffff0000, v141\n"
    "v_pk_fma_f32 v[176:177], v[182:183], v[102:103], v[176:177]\n"
    "v_pk_fma_f32 v[184:185], v[184:185], v[178:179], v[24:25] op_sel:[0,1,0] op_sel_hi:[1,1,1]\n"
    "v_cvt_pk_bf16_f32 v142, v184, v185\n"
    "v_lshlrev_b32 v184, 16, v142\n"
    "v_and_b32 v185, 0xffff0000, v142\n"
    "v_pk_fma_f32 v[176:177], v[184:185], v[104:105], v[176:177]\n"
    "v_pk_fma_f32 v[186:187], v[186:187], v[178:179], v[26:27] op_sel:[0,1,0] op_sel_hi:[1,1,1]\n"
    "v_cvt_pk_bf16_f32 v143, v186, v187\n"
    "v_lshlrev_b32 v186, 16, v143\n"
    "v_and_b32 v187, 0xffff0000, v143\n"
    "v_pk_fma_f32 v[176:177], v[186:187], v[106:107], v[176:177]\n"
    "v_pk_fma_f32 v[188:189], v[188:189], v[178:179], v[28:29] op_sel:[0,1,0] op_sel_hi:[1,1,1]\n"
    "v_cvt_pk_bf16_f32 v144, v188, v189\n"
    "v_lshlrev_b32 v188, 16, v144\n"
    "v_and_b32 v189, 0xffff0000, v144\n"
    "v_pk_fma_f32 v[176:177], v[188:189], v[108:109], v[176:177]\n"
    "v_pk_fma_f32 v[190:191], v[190:191], v[178:179], v[30:31] op_sel:[0,1,0] op_sel_hi:[1,1,1]\n"
    "v_cvt_pk_bf16_f32 v145, v190, v191\n"
    "v_lshlrev_b32 v190, 16, v145\n"
    "v_and_b32 v191, 0xffff0000, v145\n"
    "v_pk_fma_f32 v[176:177], v[190:191], v[110:111], v[176:177]\n"
    "v_pk_fma_f32 v[192:193], v[192:193], v[178:179], v[32:33] op_sel:[0,1,0] op_sel_hi:[1,1,1]\n"
    "v_cvt_pk_bf16_f32 v146, v192, v193\n"
    "v_lshlrev_b32 v192, 16, v146\n"
    "v_and_b32 v193, 0xffff0000, v146\n"
    "v_pk_fma_f32 v[176:177], v[192:193], v[112:113], v[176:177]\n"
    "v_pk_fma_f32 v[194:195], v[194:195], v[178:179], v[34:35] op_sel:[0,1,0] op_sel_hi:[1,1,1]\n"
    "v_cvt_pk_bf16_f32 v147, v194, v195\n"
    "v_lshlrev_b32 v194, 16, v147\n"
    "v_and_b32 v195, 0xffff0000, v147\n"
    "v_pk_fma_f32 v[176:177], v[194:195], v[114:115], v[176:177]\n"
    "s_mov_b32 exec_hi, 0\n"
    "v_pk_fma_f32 v[196:197], v[196:197], v[178:179], v[36:37] op_sel:[0,1,0] op_sel_hi:[1,1,1]\n"
    "v_cvt_pk_bf16_f32 v148, v196, v197\n"
    "v_lshlrev_b32 v196, 16, v148\n"
    "v_and_b32 v197, 0xffff0000, v148\n"
    "v_pk_fma_f32 v[176:177], v[196:197], v[116:117], v[176:177]\n"
    "v_pk_fma_f32 v[198:199], v[198:199], v[178:179], v[38:39] op_sel:[0,1,0] op_sel_hi:[1,1,1]\n"
    "v_cvt_pk_bf16_f32 v149, v198, v199\n"
    "v_lshlrev_b32 v198, 16, v149\n"
    "v_and_b32 v199, 0xffff0000, v149\n"
    "v_pk_fma_f32 v[176:177], v[198:199], v[118:119], v[176:177]\n"
    "s_mov_b32 exec_hi, -1\n"
    "v_add_f32 v176, v176, v177\n"
    "s_nop 1\n"
    "v_add_f32_dpp v176, v176, v176 row_ror:1 row_mask:0xf bank_mask:0xf\n"
    "s_nop 1\n"
    "v_add_f32_dpp v176, v176, v176 row_ror:2 row_mask:0xf bank_mask:0xf\n"
    "s_nop 1\n"
    "v_add_f32_dpp v176, v176, v176 row_ror:4 row_mask:0xf bank_mask:0xf\n"
    "s_nop 1\n"
    "v_add_f32_dpp v176, v176, v176 row_ror:8 row_mask:0xf bank_mask:0xf\n"
    "v_mov_b32 v160, v176\n"
    "s_nop 1\n"
    "v_permlane32_swap_b32 v176, v160\n"
    "s_nop 1\n"
    "v_add_f32 v176, v176, v160\n"
    "v_ashrrev_i32 v160, 31, v176\n"
    "v_or_b32 v160, 0x80000000, v160\n"
    "v_xor_b32 v176, v176, v160\n"
    "v_max_u32 v176, 0x800, v176\n"
    "v_and_b32 v176, 0xfffff800, v176\n"
    "v_or_b32 v178, v176, v250\n"
    "ds_write_b32 v251, v178\n"
    "v_mov_b32 v160, 86016\n"
    "v_pk_fma_f32 v[180:181], v[60:61], v[180:181], v[80:81]\n"
    "v_cvt_pk_bf16_f32 v120, v180, v181\n"
    "v_pk_fma_f32 v[182:183], v[62:63], v[182:183], v[82:83]\n"
    "v_cvt_pk_bf16_f32 v121, v182, v183\n"
    "v_pk_fma_f32 v[184:185], v[64:65], v[184:185], v[84:85]\n"
    "v_cvt_pk_bf16_f32 v122, v184, v185\n"
    "v_pk_fma_f32 v[186:187], v[66:67], v[186:187], v[86:87]\n"
    "v_cvt_pk_bf16_f32 v123, v186, v187\n"
    "s_waitcnt lgkmcnt(0)\n"
    "s_barrier\n"
    "ds_read_b128 v[200:203], v160\n"
    "ds_read_b128 v[204:207], v160 offset:16\n"
    "v_min_u32 v140, -2, v140\n"
    "v_min_u32 v141, -2, v141\n"
    "v_min_u32 v142, -2, v142\n"
    "v_min_u32 v143, -2, v143\n"
    "v_min_u32 v144, -2, v144\n"
    "v_min_u32 v145, -2, v145\n"
    "v_min_u32 v146, -2, v146\n"
    "v_min_u32 v147, -2, v147\n"
    "v_min_u32 v148, -2, v148\n"
    "v_min_u32 v149, -2, v149\n"
    "v_min_u32 v150, -2, v150\n"
    "v_min_u32 v151, -2, v151\n"
    "v_min_u32 v152, -2, v152\n"
    "v_min_u32 v153, -2, v153\n"
    "v_min_u32 v154, -2, v154\n"
    "v_min_u32 v155, -2, v155\n"
    "v_min_u32 v156, -2, v156\n"
    "v_min_u32 v157, -2, v157\n"
    "v_min_u32 v158, -2, v158\n"
    "v_min_u32 v159, -2, v159\n"
    "s_waitcnt lgkmcnt(0)\n"
    "v_max_u32 v161, v200, v201\n"
    "v_min_u32 v201, v200, v201\n"
    "v_mov_b32 v200, v161\n"
    "v_max_u32 v161, v202, v203\n"
    "v_min_u32 v203, v202, v203\n"
    "v_mov_b32 v202, v161\n"
    "v_max_u32 v161, v204, v205\n"
    "v_min_u32 v205, v204, v205\n"
    "v_mov_b32 v204, v161\n"
    "v_max_u32 v161, v206, v207\n"
    "v_min_u32 v207, v206, v207\n"
    "v_mov_b32 v206, v161\n"
    "v_min_u32 v161, v200, v202\n"
    "v_max_u32 v200, v200, v202\n"
    "v_max3_u32 v201, v161, v201, v203\n"
    "v_min_u32 v161, v204, v206\n"
    "v_max_u32 v204, v204, v206\n"
    "v_max3_u32 v205, v161, v205, v207\n"
    "v_min_u32 v161, v200, v204\n"
    "v_max_u32 v200, v200, v204\n"
    "v_max3_u32 v201, v161, v201, v205\n"
    "s_lshl_b32 s81, s93, 7\n"
    "s_add_u32 s81, s81, 73728\n"
    "buffer_store_dwordx2 v[200:201], v234, s[56:59], s81 offen sc1\n"
    "s_lshl_b32 s85, s78, 13\n"
    "s_add_u32 s85, s85, 73728\n"
    "v_lshlrev_b32 v202, 7, v239\n"
    "s_mov_b32 s87, 0\n"
    "v_mov_b32 v161, 2147483632\n"
    "v_add_u32 v162, 2560, v228\n"
    "v_cmp_eq_u32 vcc, v178, v201\n"
    "s_mul_i32 s81, s93, 5120\n"
    "s_add_u32 s81, s81, 4194304\n"
    "v_cndmask_b32 v161, v161, v162, vcc\n"
    "v_cmp_eq_u32 vcc, v178, v200\n"
    "s_nop 1\n"
    "v_cndmask_b32 v161, v161, v228, vcc\n"
    "buffer_store_dwordx2 v[140:141], v161, s[56:59], s81 offen offset:0 sc1\n"
    "buffer_store_dwordx2 v[142:143], v161, s[56:59], s81 offen offset:256 sc1\n"
    "buffer_store_dwordx2 v[144:145], v161, s[56:59], s81 offen offset:512 sc1\n"
    "buffer_store_dwordx2 v[146:147], v161, s[56:59], s81 offen offset:768 sc1\n"
    "s_mov_b32 exec_hi, 0\n"
    "buffer_store_dwordx2 v[148:149], v161, s[56:59], s81 offen offset:1024 sc1\n"
    "s_mov_b32 exec_hi, -1\n"
    "buffer_store_dwordx2 v[150:151], v161, s[56:59], s81 offen offset:1152 sc1\n"
    "buffer_store_dwordx2 v[152:153], v161, s[56:59], s81 offen offset:1408 sc1\n"
    "buffer_store_dwordx2 v[154:155], v161, s[56:59], s81 offen offset:1664 sc1\n"
    "buffer_store_dwordx2 v[156:157], v161, s[56:59], s81 offen offset:1920 sc1\n"
    "s_mov_b32 exec_hi, 0\n"
    "buffer_store_dwordx2 v[158:159], v161, s[56:59], s81 offen offset:2176 sc1\n"
    "buffer_store_dword v179, v161, s[56:59], s81 offen offset:2304 sc1\n"
    "s_mov_b32 exec_hi, -1\n"
    "v_lshrrev_b32 v164, 4, v239\n"
    "v_mul_u32_u24 v164, 9216, v164\n"
    "v_and_b32 v165, 7, v239\n"
    "v_lshl_add_u32 v165, v165, 4, v164\n"
    "s_cmp_eq_u32 s78, 0\n"
    "s_cselect_b32 s81, 1, 0x100000\n"
    "s_lshl_b32 s81, s81, 10\n"
    "v_add_u32 v165, s81, v165\n"
    "v_and_b32 v230, 15, v239\n"
    "v_lshl_add_u32 v164, v230, 4, v164\n"
    "s_lshl_b32 s81, s78, 8\n"
    "v_add_u32 v164, s81, v164\n"
    "s_mul_i32 s81, s39, 36864\n"
    "buffer_load_dwordx4 a[0:3], v164, s[40:43], s81 offen offset:0\n"
    "buffer_load_dwordx4 a[4:7], v165, s[40:43], s81 offen offset:0\n"
    "buffer_load_dwordx4 a[8:11], v164, s[40:43], s81 offen offset:1152\n"
    "buffer_load_dwordx4 a[12:15], v165, s[40:43], s81 offen offset:1152\n"
    "buffer_load_dwordx4 a[16:19], v164, s[40:43], s81 offen offset:2304\n"
    "buffer_load_dwordx4 a[20:23], v165, s[40:43], s81 offen offset:2304\n"
    "s_add_u32 s81, s81, 3456\n"
    "buffer_load_dwordx4 a[24:27], v164, s[40:43], s81 offen offset:0\n"
    "buffer_load_dwordx4 a[28:31], v165, s[40:43], s81 offen offset:0\n"
    "buffer_load_dwordx4 a[32:35], v164, s[40:43], s81 offen offset:1152\n"
    "buffer_load_dwordx4 a[36:39], v165, s[40:43], s81 offen offset:1152\n"
    "buffer_load_dwordx4 a[40:43], v164, s[40:43], s81 offen offset:2304\n"
    "buffer_load_dwordx4 a[44:47], v165, s[40:43], s81 offen offset:2304\n"
    "s_add_u32 s81, s81, 3456\n"
    "buffer_load_dwordx4 a[48:51], v164, s[40:43], s81 offen offset:0\n"
    "buffer_load_dwordx4 a[52:55], v165, s[40:43], s81 offen offset:0\n"
    "buffer_load_dwordx4 a[56:59], v164, s[40:43], s81 offen offset:1152\n"
    "buffer_load_dwordx4 a[60:63], v165, s[40:43], s81 offen offset:1152\n"
    "buffer_load_dwordx2 v[204:205], v202, s[56:59], s85 offen sc1\n"
    "v_and_b32 v164, 32, v239\n"
    "v_lshlrev_b32 v165, 6, v164\n"
    "v_and_b32 v164, 16, v239\n"
    "v_lshl_add_u32 v165, v164, 5, v165\n"
    "v_and_b32 v164, 12, v239\n"
    "v_lshl_add_u32 v165, v164, 4, v165\n"
    "v_and_b32 v164, 2, v239\n"
    "v_lshl_add_u32 v165, v164, 9, v165\n"
    "v_and_b32 v164, 1, v239\n"
    "v_lshl_add_u32 v165, v164, 3, v165\n"
    "s_lshl_b32 s82, s78, 4\n"
    "s_add_u32 s82, s82, 12288\n"
    "v_add_u32 v165, s82, v165\n"
    "v_add_u32 v166, 0x10000, v165\n"
    "v_pk_fma_f32 v[188:189], v[68:69], v[188:189], v[88:89]\n"
    "v_cvt_pk_bf16_f32 v124, v188, v189\n"
    "v_pk_fma_f32 v[190:191], v[70:71], v[190:191], v[90:91]\n"
    "v_cvt_pk_bf16_f32 v125, v190, v191\n"
    "v_pk_fma_f32 v[192:193], v[72:73], v[192:193], v[92:93]\n"
    "v_cvt_pk_bf16_f32 v126, v192, v193\n"
    "v_pk_fma_f32 v[194:195], v[74:75], v[194:195], v[94:95]\n"
    "v_cvt_pk_bf16_f32 v127, v194, v195\n"
    "v_pk_fma_f32 v[196:197], v[76:77], v[196:197], v[96:97]\n"
    "v_cvt_pk_bf16_f32 v128, v196, v197\n"
    "v_pk_fma_f32 v[198:199], v[78:79], v[198:199], v[98:99]\n"
    "v_cvt_pk_bf16_f32 v129, v198, v199\n"
    "v_lshrrev_b32 v160, 4, v239\n"
    "s_lshl_b32 s81, s78, 5\n"
    "s_add_u32 s81, s81, 86784\n"
    "v_lshl_add_u32 v160, v160, 3, s81\n"
    "s_mov_b32 s92, 0\n"
    "s_lshl_b32 s81, s78, 9\n"
    "s_add_u32 s81, s81, 87040\n"
    "v_lshl_add_u32 v200, v239, 3, s81\n"
    "v_lshlrev_b32 v201, 5, v239\n"
    "v_add_u32 v201, 87040, v201\n"
    "v_and_b32 v162, 15, v239\n"
    "v_lshlrev_b32 v162, 3, v162\n"
    "v_add_u32 v162, 86784, v162\n"
    "s_waitcnt vmcnt(0)\n"
    "v_min_u32 v161, v204, v205\n"
    "s_branch GCHK%=\n"
    "GPOLL%=:\n"
    "buffer_load_dwordx2 v[204:205], v202, s[56:59], s85 offen sc1\n"
    "s_waitcnt vmcnt(0)\n"
    "v_min_u32 v161, v204, v205\n"
    "GCHK%=:\n"
    "v_cmp_eq_u32 vcc, 0, v161\n"
    "s_nop 1\n"
    "s_cmp_eq_u64 vcc, 0\n"
    "s_cbranch_scc1 GDONE%=\n"
    "s_add_u32 s87, s87, 1\n"
    "s_cmp_gt_u32 s87, 262144\n"
    "s_cbranch_scc1 GDONE%=\n"
    "s_sleep 1\n"
    "s_branch GPOLL%=\n"
    "GDONE%=:\n"
    "ds_write_b64 v200, v[204:205]\n"
    "v_min_u32_dpp v161, v204, v204 row_ror:1 row_mask:0xf bank_mask:0xf\n"
    "v_max_u32_dpp v163, v205, v205 row_ror:1 row_mask:0xf bank_mask:0xf\n"
    "v_max_u32_dpp v204, v204, v204 row_ror:1 row_mask:0xf bank_mask:0xf\n"
    "v_max_u32 v205, v161, v163\n"
    "s_nop 0\n"
    "v_min_u32_dpp v161, v204, v204 row_ror:2 row_mask:0xf bank_mask:0xf\n"
    "v_max_u32_dpp v163, v205, v205 row_ror:2 row_mask:0xf bank_mask:0xf\n"
    "v_max_u32_dpp v204, v204, v204 row_ror:2 row_mask:0xf bank_mask:0xf\n"
    "v_max_u32 v205, v161, v163\n"
    "s_nop 0\n"
    "v_min_u32_dpp v161, v204, v204 row_ror:4 row_mask:0xf bank_mask:0xf\n"
    "v_max_u32_dpp v163, v205, v205 row_ror:4 row_mask:0xf bank_mask:0xf\n"
    "v_max_u32_dpp v204, v204, v204 row_ror:4 row_mask:0xf bank_mask:0xf\n"
    "v_max_u32 v205, v161, v163\n"
    "s_nop 0\n"
    "v_min_u32_dpp v161, v204, v204 row_ror:8 row_mask:0xf bank_mask:0xf\n"
    "v_max_u32_dpp v163, v205, v205 row_ror:8 row_mask:0xf bank_mask:0xf\n"
    "v_max_u32_dpp v204, v204, v204 row_ror:8 row_mask:0xf bank_mask:0xf\n"
    "v_max_u32 v205, v161, v163\n"
    "ds_write_b64 v160, v[204:205]\n"
    "s_waitcnt lgkmcnt(0)\n"
    "s_barrier\n"
    "ds_read_b64 v[204:205], v162\n"
    "ds_read_b128 v[208:211], v201\n"
    "ds_read_b128 v[212:215], v201 offset:16\n"
    "s_waitcnt lgkmcnt(2)\n"
    "v_min_u32_dpp v161, v204, v204 row_ror:1 row_mask:0xf bank_mask:0xf\n"
    "v_max_u32_dpp v163, v205, v205 row_ror:1 row_mask:0xf bank_mask:0xf\n"
    "v_max_u32_dpp v204, v204, v204 row_ror:1 row_mask:0xf bank_mask:0xf\n"
    "v_max_u32 v205, v161, v163\n"
    "s_nop 0\n"
    "v_min_u32_dpp v161, v204, v204 row_ror:2 row_mask:0xf bank_mask:0xf\n"
    "v_max_u32_dpp v163, v205, v205 row_ror:2 row_mask:0xf bank_mask:0xf\n"
    "v_max_u32_dpp v204, v204, v204 row_ror:2 row_mask:0xf bank_mask:0xf\n"
    "v_max_u32 v205, v161, v163\n"
    "s_nop 0\n"
    "v_min_u32_dpp v161, v204, v204 row_ror:4 row_mask:0xf bank_mask:0xf\n"
    "v_max_u32_dpp v163, v205, v205 row_ror:4 row_mask:0xf bank_mask:0xf\n"
    "v_max_u32_dpp v204, v204, v204 row_ror:4 row_mask:0xf bank_mask:0xf\n"
    "v_max_u32 v205, v161, v163\n"
    "s_nop 0\n"
    "v_min_u32_dpp v161, v204, v204 row_ror:8 row_mask:0xf bank_mask:0xf\n"
    "v_max_u32_dpp v163, v205, v205 row_ror:8 row_mask:0xf bank_mask:0xf\n"
    "v_max_u32_dpp v204, v204, v204 row_ror:8 row_mask:0xf bank_mask:0xf\n"
    "v_max_u32 v205, v161, v163\n"
    "v_readfirstlane_b32 s72, v204\n"
    "v_readfirstlane_b32 s73, v205\n"
    "s_andn2_b32 s74, 0x7ff, s72\n"
    "s_andn2_b32 s75, 0x7ff, s73\n"
    "s_lshr_b32 s76, s74, 5\n"
    "s_lshl_b32 s76, s76, 2\n"
    "s_bfe_u32 s84, s74, 0x20002\n"
    "s_or_b32 s76, s76, s84\n"
    "s_lshr_b32 s77, s75, 5\n"
    "s_lshl_b32 s77, s77, 2\n"
    "s_bfe_u32 s84, s75, 0x20002\n"
    "s_or_b32 s77, s77, s84\n"
    "s_cmp_eq_u32 s76, s77\n"
    "s_cselect_b32 s84, 2560, 0\n"
    "s_mul_i32 s76, s76, 5120\n"
    "s_mul_i32 s77, s77, 5120\n"
    "s_add_u32 s77, s77, s84\n"
    "s_waitcnt lgkmcnt(0)\n"
    "DISP%=:\n"
    "s_mov_b32 s87, 0\n"
    "s_mov_b32 s81, 4194304\n"
    "s_cmp_ge_u32 s39, 144\n"
    "s_cbranch_scc1 NOTMOE%=\n"
    "s_sub_u32 s82, s77, 1152\n"
    "v_add_u32 v160, 0, v244\n"
    "v_min_u32 v160, 2288, v160\n"
    "v_cmp_gt_u32 vcc, 1152, v160\n"
    "v_mov_b32 v161, s76\n"
    "v_mov_b32 v162, s82\n"
    "v_cndmask_b32 v161, v162, v161, vcc\n"
    "v_add_u32 v84, v160, v161\n"
    "v_add_u32 v160, 1024, v244\n"
    "v_min_u32 v160, 2288, v160\n"
    "v_cmp_gt_u32 vcc, 1152, v160\n"
    "v_mov_b32 v161, s76\n"
    "v_mov_b32 v162, s82\n"
    "v_cndmask_b32 v161, v162, v161, vcc\n"
    "v_add_u32 v85, v160, v161\n"
    "v_add_u32 v160, 2048, v244\n"
    "v_min_u32 v160, 2288, v160\n"
    "v_cmp_gt_u32 vcc, 1152, v160\n"
    "v_mov_b32 v161, s76\n"
    "v_mov_b32 v162, s82\n"
    "v_cndmask_b32 v161, v162, v161, vcc\n"
    "v_add_u32 v86, v160, v161\n"
    "buffer_load_dwordx4 v[72:75], v84, s[56:59], s81 offen sc1\n"
    "buffer_load_dwordx4 v[76:79], v85, s[56:59], s81 offen sc1\n"
    "buffer_load_dwordx4 v[80:83], v86, s[56:59], s81 offen sc1\n"
    "ds_read_b64_tr_b16 a[128:129], v165 offset:0\n"
    "ds_read_b64_tr_b16 a[130:131], v165 offset:256\n"
    "ds_read_b64_tr_b16 a[132:133], v165 offset:4096\n"
    "ds_read_b64_tr_b16 a[134:135], v165 offset:4352\n"
    "ds_read_b64_tr_b16 a[136:137], v165 offset:8192\n"
    "ds_read_b64_tr_b16 a[138:139], v165 offset:8448\n"
    "ds_read_b64_tr_b16 a[140:141], v165 offset:12288\n"
    "ds_read_b64_tr_b16 a[142:143], v165 offset:12544\n"
    "ds_read_b64_tr_b16 a[144:145], v165 offset:16384\n"
    "ds_read_b64_tr_b16 a[146:147], v165 offset:16640\n"
    "ds_read_b64_tr_b16 a[148:149], v165 offset:20480\n"
    "ds_read_b64_tr_b16 a[150:151], v165 offset:20736\n"
    "ds_read_b64_tr_b16 a[152:153], v165 offset:24576\n"
    "ds_read_b64_tr_b16 a[154:155], v165 offset:24832\n"
    "ds_read_b64_tr_b16 a[156:157], v165 offset:28672\n"
    "ds_read_b64_tr_b16 a[158:159], v165 offset:28928\n"
    "ds_read_b64_tr_b16 a[160:161], v165 offset:32768\n"
    "ds_read_b64_tr_b16 a[162:163], v165 offset:33024\n"
    "ds_read_b64_tr_b16 a[164:165], v165 offset:36864\n"
    "ds_read_b64_tr_b16 a[166:167], v165 offset:37120\n"
    "ds_read_b64_tr_b16 a[168:169], v165 offset:40960\n"
    "ds_read_b64_tr_b16 a[170:171], v165 offset:41216\n"
    "ds_read_b64_tr_b16 a[172:173], v165 offset:45056\n"
    "ds_read_b64_tr_b16 a[174:175], v165 offset:45312\n"
    "ds_read_b64_tr_b16 a[176:177], v165 offset:49152\n"
    "ds_read_b64_tr_b16 a[178:179], v165 offset:49408\n"
    "ds_read_b64_tr_b16 a[180:181], v165 offset:53248\n"
    "ds_read_b64_tr_b16 a[182:183], v165 offset:53504\n"
    "ds_read_b64_tr_b16 a[184:185], v165 offset:57344\n"
    "ds_read_b64_tr_b16 a[186:187], v165 offset:57600\n"
    "ds_read_b64_tr_b16 a[188:189], v165 offset:61440\n"
    "ds_read_b64_tr_b16 a[190:191], v165 offset:61696\n"
    "ds_read_b64_tr_b16 a[192:193], v166 offset:0\n"
    "ds_read_b64_tr_b16 a[194:195], v166 offset:256\n"
    "ds_read_b64_tr_b16 a[196:197], v166 offset:4096\n"
    "ds_read_b64_tr_b16 a[198:199], v166 offset:4352\n"
    "v_min3_u32 v207, v208, v210, v212\n"
    "v_min_u32 v207, v207, v214\n"
    "v_sub_u32 v192, v208, v205\n"
    "v_sub_u32 v193, v209, v205\n"
    "v_sub_u32 v194, v210, v205\n"
    "v_sub_u32 v195, v211, v205\n"
    "v_sub_u32 v196, v212, v205\n"
    "v_sub_u32 v197, v213, v205\n"
    "v_sub_u32 v198, v214, v205\n"
    "v_sub_u32 v199, v215, v205\n"
    "v_max3_u32 v206, v192, v193, v194\n"
    "v_max3_u32 v206, v206, v195, v196\n"
    "v_max3_u32 v206, v206, v197, v198\n"
    "v_max_u32 v206, v206, v199\n"
    "v_min_u32_dpp v207, v207, v207 row_ror:1 row_mask:0xf bank_mask:0xf\n"
    "s_nop 0\n"
    "v_max_u32_dpp v206, v206, v206 row_ror:1 row_mask:0xf bank_mask:0xf\n"
    "v_min_u32_dpp v207, v207, v207 row_ror:2 row_mask:0xf bank_mask:0xf\n"
    "s_nop 0\n"
    "v_max_u32_dpp v206, v206, v206 row_ror:2 row_mask:0xf bank_mask:0xf\n"
    "v_min_u32_dpp v207, v207, v207 row_ror:4 row_mask:0xf bank_mask:0xf\n"
    "s_nop 0\n"
    "v_max_u32_dpp v206, v206, v206 row_ror:4 row_mask:0xf bank_mask:0xf\n"
    "v_min_u32_dpp v207, v207, v207 row_ror:8 row_mask:0xf bank_mask:0xf\n"
    "s_nop 0\n"
    "v_max_u32_dpp v206, v206, v206 row_ror:8 row_mask:0xf bank_mask:0xf\n"
    "v_mov_b32 v192, v207\n"
    "v_mov_b32 v193, v206\n"
    "s_nop 0\n"
    "v_permlane16_swap_b32 v207, v192\n"
    "v_permlane16_swap_b32 v206, v193\n"
    "s_nop 1\n"
    "v_min_u32 v207, v207, v192\n"
    "v_max_u32 v206, v206, v193\n"
    "v_mov_b32 v192, v207\n"
    "v_mov_b32 v193, v206\n"
    "s_nop 0\n"
    "v_permlane32_swap_b32 v207, v192\n"
    "v_permlane32_swap_b32 v206, v193\n"
    "s_nop 1\n"
    "v_min_u32 v207, v207, v192\n"
    "v_max_u32 v206, v206, v193\n"
    "v_add_u32 v206, v206, v205\n"
    "v_ashrrev_i32 v195, 31, v205\n"
    "v_not_b32 v195, v195\n"
    "v_or_b32 v195, 0x80000000, v195\n"
    "v_xor_b32 v192, v205, v195\n"
    "v_ashrrev_i32 v195, 31, v206\n"
    "v_not_b32 v195, v195\n"
    "v_or_b32 v195, 0x80000000, v195\n"
    "v_xor_b32 v193, v206, v195\n"
    "v_ashrrev_i32 v195, 31, v207\n"
    "v_not_b32 v195, v195\n"
    "v_or_b32 v195, 0x80000000, v195\n"
    "v_xor_b32 v194, v207, v195\n"
    "v_max_f32 v195, |v192|, |v193|\n"
    "v_and_b32 v195, 0x7f800000, v195\n"
    "v_sub_f32 v193, v192, v193\n"
    "v_sub_f32 v194, v192, v194\n"
    "v_fmac_f32 v193, 0x3d000000, v195\n"
    "v_mul_f32 v194, 0x3cb43958, v194\n"
    "v_cmp_lt_f32 vcc, v193, v194\n"
    "s_nop 1\n"
    "s_cmp_lg_u32 s92, 0\n"
    "s_cselect_b64 vcc, 0, vcc\n"
    "s_cmp_lg_u64 vcc, 0\n"
    "s_cbranch_scc1 REFINE%=\n"
    "v_cmp_ne_u32 vcc, s72, v178\n"
    "v_cmp_ne_u32 s[84:85], s73, v178\n"
    "v_mov_b32 v161, 2147483632\n"
    "s_nop 0\n"
    "s_and_b64 vcc, vcc, s[84:85]\n"
    "s_nop 1\n"
    "v_cndmask_b32 v229, v161, v229, vcc\n"
    "buffer_store_dwordx2 v[120:121], v229, s[68:71], 0 offen offset:0 sc1\n"
    "buffer_store_dwordx2 v[122:123], v229, s[68:71], 0 offen offset:256 sc1\n"
    "buffer_store_dwordx2 v[124:125], v229, s[68:71], 0 offen offset:512 sc1\n"
    "buffer_store_dwordx2 v[126:127], v229, s[68:71], 0 offen offset:768 sc1\n"
    "s_mov_b32 exec_hi, 0\n"
    "buffer_store_dwordx2 v[128:129], v229, s[68:71], 0 offen offset:1024 sc1\n"
    "s_mov_b32 exec_hi, -1\n"
    "s_add_u32 s82, s79, 90112\n"
    "v_add_u32 v87, s82, v244\n"
    "v_and_b32 v160, 1, v239\n"
    "v_mul_u32_u24 v160, 1152, v160\n"
    "v_and_b32 v161, 48, v239\n"
    "v_add3_u32 v88, v160, v161, s82\n"
    "v_and_b32 v160, 1, v239\n"
    "v_lshlrev_b32 v92, 6, v160\n"
    "v_bfe_u32 v160, v239, 4, 1\n"
    "v_lshl_add_u32 v92, v160, 3, v92\n"
    "v_lshrrev_b32 v163, 5, v239\n"
    "v_lshl_add_u32 v92, v163, 2, v92\n"
    "s_lshl_b32 s82, s78, 4\n"
    "s_add_u32 s82, s82, 86080\n"
    "v_add_u32 v92, s82, v92\n"
    "v_and_b32 v93, 8, v239\n"
    "v_lshlrev_b32 v93, 3, v93\n"
    "v_and_b32 v161, 48, v239\n"
    "v_add3_u32 v93, v93, v161, 64\n"
    "v_add_u32 v93, 86016, v93\n"
    "v_bfe_u32 v161, v239, 1, 3\n"
    "v_mul_u32_u24 v94, 18432, v161\n"
    "v_lshl_add_u32 v94, v163, 6, v94\n"
    "v_lshl_add_u32 v94, v160, 4, v94\n"
    "v_and_b32 v161, 1, v239\n"
    "v_lshl_add_u32 v94, v161, 5, v94\n"
    "v_and_b32 v161, 8, v239\n"
    "v_or_b32 v161, s78, v161\n"
    "v_cmp_eq_u32 vcc, 0, v161\n"
    "v_add_u32 v161, 589824, v94\n"
    "v_mov_b32 v160, 2147483632\n"
    "v_cndmask_b32 v95, v160, v161, vcc\n"
    "s_mul_i32 s82, s78, 147456\n"
    "v_add_u32 v94, s82, v94\n"
    "s_lshl_b32 s82, s39, 7\n"
    "s_add_u32 s82, s82, 6291456\n"
    "s_waitcnt vmcnt(5)\n"
    "s_branch XGO%=\n"
    "XPOLL%=:\n"
    "buffer_load_dwordx4 v[72:75], v84, s[56:59], s81 offen sc1\n"
    "buffer_load_dwordx4 v[76:79], v85, s[56:59], s81 offen sc1\n"
    "buffer_load_dwordx4 v[80:83], v86, s[56:59], s81 offen sc1\n"
    "s_waitcnt vmcnt(0)\n"
    "XGO%=:\n"
    "ds_write_b128 v87, v[72:75] offset:0\n"
    "ds_write_b128 v87, v[76:79] offset:1024\n"
    "ds_write_b128 v87, v[80:83] offset:2048\n"
    "v_max_u32 v160, v72, v73\n"
    "v_max3_u32 v160, v160, v74, v75\n"
    "v_max3_u32 v160, v160, v76, v77\n"
    "v_max3_u32 v160, v160, v78, v79\n"
    "v_max3_u32 v160, v160, v80, v81\n"
    "v_max3_u32 v160, v160, v82, v83\n"
    "v_cmp_eq_u32 vcc, -1, v160\n"
    "s_nop 1\n"
    "s_cmp_eq_u64 vcc, 0\n"
    "s_cbranch_scc1 XDONE%=\n"
    "s_add_u32 s87, s87, 1\n"
    "s_cmp_gt_u32 s87, 262144\n"
    "s_cbranch_scc1 XDONE%=\n"
    "s_sleep 1\n"
    "s_branch XPOLL%=\n"
    "XDONE%=:\n"
    "s_waitcnt lgkmcnt(0)\n"
    "ds_read_b128 v[0:3], v88 offset:0\n"
    "ds_read_b128 v[4:7], v88 offset:64\n"
    "ds_read_b128 v[8:11], v88 offset:128\n"
    "ds_read_b128 v[12:15], v88 offset:192\n"
    "ds_read_b128 v[16:19], v88 offset:256\n"
    "ds_read_b128 v[20:23], v88 offset:320\n"
    "ds_read_b128 v[24:27], v88 offset:384\n"
    "ds_read_b128 v[28:31], v88 offset:448\n"
    "ds_read_b128 v[32:35], v88 offset:512\n"
    "ds_read_b128 v[36:39], v88 offset:576\n"
    "ds_read_b128 v[40:43], v88 offset:640\n"
    "ds_read_b128 v[44:47], v88 offset:704\n"
    "ds_read_b128 v[48:51], v88 offset:768\n"
    "ds_read_b128 v[52:55], v88 offset:832\n"
    "ds_read_b128 v[56:59], v88 offset:896\n"
    "ds_read_b128 v[60:63], v88 offset:960\n"
    "ds_read_b128 v[64:67], v88 offset:1024\n"
    "ds_read_b128 v[68:71], v88 offset:1088\n"
    "s_waitcnt lgkmcnt(10)\n"
    "v_mfma_f32_16x16x32_bf16 v[100:103], a[128:131], v[0:3], 0\n"
    "v_mfma_f32_16x16x32_bf16 v[104:107], a[132:135], v[4:7], 0\n"
    "v_mfma_f32_16x16x32_bf16 v[100:103], a[136:139], v[8:11], v[100:103]\n"
    "v_mfma_f32_16x16x32_bf16 v[104:107], a[140:143], v[12:15], v[104:107]\n"
    "v_mfma_f32_16x16x32_bf16 v[100:103], a[144:147], v[16:19], v[100:103]\n"
    "v_mfma_f32_16x16x32_bf16 v[104:107], a[148:151], v[20:23], v[104:107]\n"
    "v_mfma_f32_16x16x32_bf16 v[100:103], a[152:155], v[24:27], v[100:103]\n"
    "v_mfma_f32_16x16x32_bf16 v[104:107], a[156:159], v[28:31], v[104:107]\n"
    "s_waitcnt lgkmcnt(1)\n"
    "v_mfma_f32_16x16x32_bf16 v[100:103], a[160:163], v[32:35], v[100:103]\n"
    "v_mfma_f32_16x16x32_bf16 v[104:107], a[164:167], v[36:39], v[104:107]\n"
    "v_mfma_f32_16x16x32_bf16 v[100:103], a[168:171], v[40:43], v[100:103]\n"
    "v_mfma_f32_16x16x32_bf16 v[104:107], a[172:175], v[44:47], v[104:107]\n"
    "v_mfma_f32_16x16x32_bf16 v[100:103], a[176:179], v[48:51], v[100:103]\n"
    "v_mfma_f32_16x16x32_bf16 v[104:107], a[180:183], v[52:55], v[104:107]\n"
    "v_mfma_f32_16x16x32_bf16 v[100:103], a[184:187], v[56:59], v[100:103]\n"
    "v_mfma_f32_16x16x32_bf16 v[104:107], a[188:191], v[60:63], v[104:107]\n"
    "v_mfma_f32_16x16x32_bf16 v[100:103], a[192:195], v[64:67], v[100:103]\n"
    "s_waitcnt lgkmcnt(0)\n"
    "v_mfma_f32_16x16x32_bf16 v[104:107], a[196:199], v[68:71], v[104:107]\n"
    "s_nop 7\n"
    "v_pk_add_f32 v[100:101], v[100:101], v[104:105]\n"
    "v_pk_add_f32 v[102:103], v[102:103], v[106:107]\n"
    "s_nop 1\n"
    "v_permlane32_swap_b32 v100, v102\n"
    "v_permlane32_swap_b32 v101, v103\n"
    "s_nop 1\n"
    "v_cvt_pk_bf16_f32 v160, v100, v101\n"
    "v_lshlrev_b32 v100, 16, v160\n"
    "v_and_b32 v101, 0xffff0000, v160\n"
    "v_cvt_pk_bf16_f32 v161, v102, v103\n"
    "v_lshlrev_b32 v102, 16, v161\n"
    "v_and_b32 v103, 0xffff0000, v161\n"
    "v_mul_f32 v160, 0xbfb8aa3b, v100\n"
    "v_mul_f32 v161, 0xbfb8aa3b, v101\n"
    "v_exp_f32 v160, v160\n"
    "v_exp_f32 v161, v161\n"
    "v_add_f32 v160, 1.0, v160\n"
    "v_add_f32 v161, 1.0, v161\n"
    "v_rcp_f32 v160, v160\n"
    "v_rcp_f32 v161, v161\n"
    "v_mul_f32 v100, v100, v160\n"
    "v_mul_f32 v101, v101, v161\n"
    "v_cvt_pk_bf16_f32 v160, v100, v101\n"
    "v_lshlrev_b32 v100, 16, v160\n"
    "v_and_b32 v101, 0xffff0000, v160\n"
    "v_mul_f32 v100, v100, v102\n"
    "v_mul_f32 v101, v101, v103\n"
    "v_cvt_pk_bf16_f32 v160, v100, v101\n"
    "ds_write_b32 v92, v160\n"
    "s_waitcnt lgkmcnt(0)\n"
    "s_barrier\n"
    "ds_read_b128 v[164:167], v93\n"
    "s_waitcnt lgkmcnt(0)\n"
    "v_perm_b32 v0, v164, v164, v220\n"
    "v_perm_b32 v1, v164, v164, v222\n"
    "v_perm_b32 v2, v164, v164, v224\n"
    "v_perm_b32 v3, v164, v164, v226\n"
    "v_perm_b32 v4, v164, v164, v221\n"
    "v_perm_b32 v5, v164, v164, v223\n"
    "v_perm_b32 v6, v164, v164, v225\n"
    "v_perm_b32 v7, v164, v164, v227\n"
    "v_mfma_f32_16x16x32_bf16 v[100:103], v[0:3], a[0:3], 0\n"
    "v_mfma_f32_16x16x32_bf16 v[104:107], v[0:3], a[4:7], 0\n"
    "v_perm_b32 v8, v165, v165, v220\n"
    "v_perm_b32 v9, v165, v165, v222\n"
    "v_perm_b32 v10, v165, v165, v224\n"
    "v_perm_b32 v11, v165, v165, v226\n"
    "v_mfma_f32_16x16x32_bf16 v[100:103], v[4:7], a[8:11], v[100:103]\n"
    "v_mfma_f32_16x16x32_bf16 v[104:107], v[4:7], a[12:15], v[104:107]\n"
    "v_perm_b32 v12, v165, v165, v221\n"
    "v_perm_b32 v13, v165, v165, v223\n"
    "v_perm_b32 v14, v165, v165, v225\n"
    "v_perm_b32 v15, v165, v165, v227\n"
    "v_mfma_f32_16x16x32_bf16 v[100:103], v[8:11], a[16:19], v[100:103]\n"
    "v_mfma_f32_16x16x32_bf16 v[104:107], v[8:11], a[20:23], v[104:107]\n"
    "v_perm_b32 v16, v166, v166, v220\n"
    "v_perm_b32 v17, v166, v166, v222\n"
    "v_perm_b32 v18, v166, v166, v224\n"
    "v_perm_b32 v19, v166, v166, v226\n"
    "v_mfma_f32_16x16x32_bf16 v[100:103], v[12:15], a[24:27], v[100:103]\n"
    "v_mfma_f32_16x16x32_bf16 v[104:107], v[12:15], a[28:31], v[104:107]\n"
    "v_perm_b32 v20, v166, v166, v221\n"
    "v_perm_b32 v21, v166, v166, v223\n"
    "v_perm_b32 v22, v166, v166, v225\n"
    "v_perm_b32 v23, v166, v166, v227\n"
    "v_mfma_f32_16x16x32_bf16 v[100:103], v[16:19], a[32:35], v[100:103]\n"
    "v_mfma_f32_16x16x32_bf16 v[104:107], v[16:19], a[36:39], v[104:107]\n"
    "v_perm_b32 v24, v167, v167, v220\n"
    "v_perm_b32 v25, v167, v167, v222\n"
    "v_perm_b32 v26, v167, v167, v224\n"
    "v_perm_b32 v27, v167, v167, v226\n"
    "v_mfma_f32_16x16x32_bf16 v[100:103], v[20:23], a[40:43], v[100:103]\n"
    "v_mfma_f32_16x16x32_bf16 v[104:107], v[20:23], a[44:47], v[104:107]\n"
    "v_perm_b32 v28, v167, v167, v221\n"
    "v_perm_b32 v29, v167, v167, v223\n"
    "v_perm_b32 v30, v167, v167, v225\n"
    "v_perm_b32 v31, v167, v167, v227\n"
    "v_mfma_f32_16x16x32_bf16 v[100:103], v[24:27], a[48:51], v[100:103]\n"
    "v_mfma_f32_16x16x32_bf16 v[104:107], v[24:27], a[52:55], v[104:107]\n"
    "v_mfma_f32_16x16x32_bf16 v[100:103], v[28:31], a[56:59], v[100:103]\n"
    "v_mfma_f32_16x16x32_bf16 v[104:107], v[28:31], a[60:63], v[104:107]\n"
    "s_nop 6\n"
    "v_min_u32 v100, -2, v100\n"
    "v_min_u32 v101, -2, v101\n"
    "v_min_u32 v102, -2, v102\n"
    "v_min_u32 v103, -2, v103\n"
    "buffer_store_dwordx4 v[100:103], v94, s[56:59], s82 offen sc1\n"
    "v_min_u32 v104, -2, v104\n"
    "v_min_u32 v105, -2, v105\n"
    "v_min_u32 v106, -2, v106\n"
    "v_min_u32 v107, -2, v107\n"
    "buffer_store_dwordx4 v[104:107], v95, s[56:59], s82 offen sc1\n"
    "s_branch FIN%=\n"
    "NOTMOE%=:\n"
    "v_min3_u32 v207, v208, v210, v212\n"
    "v_min_u32 v207, v207, v214\n"
    "v_sub_u32 v192, v208, v205\n"
    "v_sub_u32 v193, v209, v205\n"
    "v_sub_u32 v194, v210, v205\n"
    "v_sub_u32 v195, v211, v205\n"
    "v_sub_u32 v196, v212, v205\n"
    "v_sub_u32 v197, v213, v205\n"
    "v_sub_u32 v198, v214, v205\n"
    "v_sub_u32 v199, v215, v205\n"
    "v_max3_u32 v206, v192, v193, v194\n"
    "v_max3_u32 v206, v206, v195, v196\n"
    "v_max3_u32 v206, v206, v197, v198\n"
    "v_max_u32 v206, v206, v199\n"
    "v_min_u32_dpp v207, v207, v207 row_ror:1 row_mask:0xf bank_mask:0xf\n"
    "s_nop 0\n"
    "v_max_u32_dpp v206, v206, v206 row_ror:1 row_mask:0xf bank_mask:0xf\n"
    "v_min_u32_dpp v207, v207, v207 row_ror:2 row_mask:0xf bank_mask:0xf\n"
    "s_nop 0\n"
    "v_max_u32_dpp v206, v206, v206 row_ror:2 row_mask:0xf bank_mask:0xf\n"
    "v_min_u32_dpp v207, v207, v207 row_ror:4 row_mask:0xf bank_mask:0xf\n"
    "s_nop 0\n"
    "v_max_u32_dpp v206, v206, v206 row_ror:4 row_mask:0xf bank_mask:0xf\n"
    "v_min_u32_dpp v207, v207, v207 row_ror:8 row_mask:0xf bank_mask:0xf\n"
    "s_nop 0\n"
    "v_max_u32_dpp v206, v206, v206 row_ror:8 row_mask:0xf bank_mask:0xf\n"
    "v_mov_b32 v192, v207\n"
    "v_mov_b32 v193, v206\n"
    "s_nop 0\n"
    "v_permlane16_swap_b32 v207, v192\n"
    "v_permlane16_swap_b32 v206, v193\n"
    "s_nop 1\n"
    "v_min_u32 v207, v207, v192\n"
    "v_max_u32 v206, v206, v193\n"
    "v_mov_b32 v192, v207\n"
    "v_mov_b32 v193, v206\n"
    "s_nop 0\n"
    "v_permlane32_swap_b32 v207, v192\n"
    "v_permlane32_swap_b32 v206, v193\n"
    "s_nop 1\n"
    "v_min_u32 v207, v207, v192\n"
    "v_max_u32 v206, v206, v193\n"
    "v_add_u32 v206, v206, v205\n"
    "v_ashrrev_i32 v195, 31, v205\n"
    "v_not_b32 v195, v195\n"
    "v_or_b32 v195, 0x80000000, v195\n"
    "v_xor_b32 v192, v205, v195\n"
    "v_ashrrev_i32 v195, 31, v206\n"
    "v_not_b32 v195, v195\n"
    "v_or_b32 v195, 0x80000000, v195\n"
    "v_xor_b32 v193, v206, v195\n"
    "v_ashrrev_i32 v195, 31, v207\n"
    "v_not_b32 v195, v195\n"
    "v_or_b32 v195, 0x80000000, v195\n"
    "v_xor_b32 v194, v207, v195\n"
    "v_max_f32 v195, |v192|, |v193|\n"
    "v_and_b32 v195, 0x7f800000, v195\n"
    "v_sub_f32 v193, v192, v193\n"
    "v_sub_f32 v194, v192, v194\n"
    "v_fmac_f32 v193, 0x3d000000, v195\n"
    "v_mul_f32 v194, 0x3cb43958, v194\n"
    "v_cmp_lt_f32 vcc, v193, v194\n"
    "s_nop 1\n"
    "s_cmp_lg_u32 s92, 0\n"
    "s_cselect_b64 vcc, 0, vcc\n"
    "s_cmp_lg_u64 vcc, 0\n"
    "s_cbranch_scc1 REFINE%=\n"
    "v_cmp_ne_u32 vcc, s72, v178\n"
    "v_cmp_ne_u32 s[84:85], s73, v178\n"
    "v_mov_b32 v161, 2147483632\n"
    "s_nop 0\n"
    "s_and_b64 vcc, vcc, s[84:85]\n"
    "s_nop 1\n"
    "v_cndmask_b32 v229, v161, v229, vcc\n"
    "buffer_store_dwordx2 v[120:121], v229, s[68:71], 0 offen offset:0 sc1\n"
    "buffer_store_dwordx2 v[122:123], v229, s[68:71], 0 offen offset:256 sc1\n"
    "buffer_store_dwordx2 v[124:125], v229, s[68:71], 0 offen offset:512 sc1\n"
    "buffer_store_dwordx2 v[126:127], v229, s[68:71], 0 offen offset:768 sc1\n"
    "s_mov_b32 exec_hi, 0\n"
    "buffer_store_dwordx2 v[128:129], v229, s[68:71], 0 offen offset:1024 sc1\n"
    "s_mov_b32 exec_hi, -1\n"
    "s_cmp_ge_u32 s39, 180\n"
    "s_cbranch_scc1 FIN%=\n"
    "s_sub_u32 s82, s39, 144\n"
    "v_and_b32 v160, 16, v239\n"
    "v_cmp_eq_u32 vcc, 16, v160\n"
    "v_mov_b32 v162, s76\n"
    "v_mov_b32 v163, s77\n"
    "v_and_b32 v160, 14, v239\n"
    "v_cndmask_b32 v2, v162, v163, vcc\n"
    "v_lshl_add_u32 v2, v160, 1, v2\n"
    "s_lshl_b32 s83, s82, 5\n"
    "s_add_u32 s81, s81, s83\n"
    "v_mov_b32 v162, s74\n"
    "v_mov_b32 v163, s75\n"
    "v_cndmask_b32 v3, v162, v163, vcc\n"
    "v_mov_b32 v162, s72\n"
    "v_mov_b32 v163, s73\n"
    "v_cndmask_b32 v5, v162, v163, vcc\n"
    "CPOLL%=:\n"
    "buffer_load_dword v0, v2, s[56:59], s81 offen sc1\n"
    "buffer_load_dword v1, v2, s[56:59], s81 offen offset:1152 sc1\n"
    "s_waitcnt vmcnt(0)\n"
    "v_max_u32 v160, v0, v1\n"
    "v_cmp_eq_u32 vcc, -1, v160\n"
    "s_nop 1\n"
    "s_cmp_eq_u64 vcc, 0\n"
    "s_cbranch_scc1 CDONE%=\n"
    "s_add_u32 s87, s87, 1\n"
    "s_cmp_gt_u32 s87, 262144\n"
    "s_cbranch_scc1 CDONE%=\n"
    "s_sleep 1\n"
    "s_branch CPOLL%=\n"
    "CDONE%=:\n"
    "s_lshl_b32 s84, s78, 10\n"
    "v_lshl_add_u32 v6, v239, 4, s84\n"
    "s_cmp_lt_u32 s78, 2\n"
    "s_cselect_b32 s84, 0, 2147483632\n"
    "v_or_b32 v7, s84, v6\n"
    "s_mul_i32 s84, s82, 18432\n"
    "s_add_u32 s84, s84, 6291456\n"
    "v_ashrrev_i32 v160, 31, v5\n"
    "v_not_b32 v160, v160\n"
    "v_or_b32 v160, 0x80000000, v160\n"
    "v_xor_b32 v5, v5, v160\n"
    "v_cvt_pk_bf16_f32 v5, v5, v5\n"
    "v_and_b32 v5, 0xffff0000, v5\n"
    "v_mul_f32 v5, 0xbfb8aa3b, v5\n"
    "v_exp_f32 v5, v5\n"
    "v_and_b32 v160, 1, v239\n"
    "v_add_f32 v5, 1.0, v5\n"
    "v_rcp_f32 v5, v5\n"
    "v_lshlrev_b32 v160, 4, v160\n"
    "v_lshrrev_b32 v0, v160, v0\n"
    "v_lshlrev_b32 v0, 16, v0\n"
    "v_lshrrev_b32 v1, v160, v1\n"
    "v_lshlrev_b32 v1, 16, v1\n"
    "v_cvt_pk_bf16_f32 v5, v5, v5\n"
    "v_and_b32 v5, 0xffff0000, v5\n"
    "v_and_b32 v160, 15, v239\n"
    "v_mul_u32_u24 v3, 1152, v3\n"
    "v_lshl_add_u32 v3, v160, 1, v3\n"
    "s_lshl_b32 s85, s82, 6\n"
    "s_add_u32 s85, s85, 6912\n"
    "v_lshl_add_u32 v160, v160, 2, s85\n"
    "ds_read_b32 v4, v160\n"
    "v_and_b32 v160, 7, v239\n"
    "v_lshrrev_b32 v161, 4, v239\n"
    "v_lshlrev_b32 v161, 2, v161\n"
    "v_lshl_add_u32 v28, v160, 4, v161\n"
    "s_lshl_b32 s85, s78, 7\n"
    "s_add_u32 s85, s85, 86272\n"
    "v_add_u32 v28, s85, v28\n"
    "v_and_b32 v160, 31, v239\n"
    "v_lshlrev_b32 v29, 2, v160\n"
    "v_add_u32 v29, 86272, v29\n"
    "s_mov_b32 s87, 0\n"
    "YPOLL%=:\n"
    "buffer_load_dwordx4 v[8:11], v6, s[56:59], s84 offen sc1\n"
    "s_add_u32 s85, s84, 4096\n"
    "buffer_load_dwordx4 v[12:15], v6, s[56:59], s85 offen sc1\n"
    "s_add_u32 s85, s84, 8192\n"
    "buffer_load_dwordx4 v[16:19], v6, s[56:59], s85 offen sc1\n"
    "s_add_u32 s85, s84, 12288\n"
    "buffer_load_dwordx4 v[20:23], v6, s[56:59], s85 offen sc1\n"
    "s_add_u32 s85, s84, 16384\n"
    "buffer_load_dwordx4 v[24:27], v7, s[56:59], s85 offen sc1\n"
    "s_waitcnt vmcnt(0)\n"
    "v_max_u32 v160, v8, v9\n"
    "v_max3_u32 v160, v160, v10, v11\n"
    "v_max3_u32 v160, v160, v12, v13\n"
    "v_max3_u32 v160, v160, v14, v15\n"
    "v_max3_u32 v160, v160, v16, v17\n"
    "v_max3_u32 v160, v160, v18, v19\n"
    "v_max3_u32 v160, v160, v20, v21\n"
    "v_max3_u32 v160, v160, v22, v23\n"
    "v_max3_u32 v160, v160, v24, v25\n"
    "v_max3_u32 v160, v160, v26, v27\n"
    "v_cmp_eq_u32 vcc, -1, v160\n"
    "s_nop 1\n"
    "s_cmp_eq_u64 vcc, 0\n"
    "s_cbranch_scc1 YDONE%=\n"
    "s_add_u32 s87, s87, 1\n"
    "s_cmp_gt_u32 s87, 262144\n"
    "s_cbranch_scc1 YDONE%=\n"
    "s_sleep 1\n"
    "s_branch YPOLL%=\n"
    "YDONE%=:\n"
    "v_pk_add_f32 v[8:9], v[8:9], v[12:13]\n"
    "v_pk_add_f32 v[10:11], v[10:11], v[14:15]\n"
    "v_pk_add_f32 v[16:17], v[16:17], v[20:21]\n"
    "v_pk_add_f32 v[18:19], v[18:19], v[22:23]\n"
    "v_pk_add_f32 v[8:9], v[8:9], v[16:17]\n"
    "v_pk_add_f32 v[10:11], v[10:11], v[18:19]\n"
    "v_pk_add_f32 v[8:9], v[8:9], v[24:25]\n"
    "v_pk_add_f32 v[10:11], v[10:11], v[26:27]\n"
    "s_nop 0\n"
    "v_permlane16_swap_b32 v8, v9\n"
    "v_permlane16_swap_b32 v10, v11\n"
    "s_nop 1\n"
    "v_add_f32 v8, v8, v9\n"
    "v_add_f32 v10, v10, v11\n"
    "s_nop 1\n"
    "v_permlane32_swap_b32 v8, v10\n"
    "s_nop 1\n"
    "v_add_f32 v8, v8, v10\n"
    "s_nop 1\n"
    "v_add_f32_dpp v8, v8, v8 row_ror:8 row_mask:0xf bank_mask:0xf\n"
    "ds_write_b32 v28, v8\n"
    "s_waitcnt lgkmcnt(0)\n"
    "s_barrier\n"
    "s_cmp_lg_u32 s78, 0\n"
    "s_cbranch_scc1 FIN%=\n"
    "ds_read_b32 v8, v29 offset:0\n"
    "ds_read_b32 v9, v29 offset:128\n"
    "ds_read_b32 v10, v29 offset:256\n"
    "ds_read_b32 v11, v29 offset:384\n"
    "s_waitcnt lgkmcnt(2)\n"
    "v_add_f32 v8, v8, v9\n"
    "s_waitcnt lgkmcnt(0)\n"
    "v_add_f32 v10, v10, v11\n"
    "v_add_f32 v8, v8, v10\n"
    "v_cvt_pk_bf16_f32 v8, v8, v8\n"
    "v_and_b32 v8, 0xffff0000, v8\n"
    "v_mul_f32 v8, v8, v5\n"
    "v_cvt_pk_bf16_f32 v8, v8, v8\n"
    "v_and_b32 v8, 0xffff0000, v8\n"
    "v_add_f32 v8, v8, v0\n"
    "v_cvt_pk_bf16_f32 v8, v8, v8\n"
    "v_and_b32 v8, 0xffff0000, v8\n"
    "v_mul_f32 v8, v8, v4\n"
    "v_cvt_pk_bf16_f32 v8, v8, v8\n"
    "v_and_b32 v8, 0xffff0000, v8\n"
    "v_add_f32 v8, v8, v1\n"
    "v_cvt_pk_bf16_f32 v8, v8, v8\n"
    "s_mov_b32 exec_hi, 0\n"
    "buffer_store_short v8, v3, s[68:71], s83 offen sc1\n"
    "s_mov_b32 exec_hi, -1\n"
    "FIN%=:\n"
    "s_branch KEND%=\n"
    "EV0%=:\n"
    "s_cmp_eq_u32 s72, 376832\n"
    "s_cbranch_scc1 EVSW%=\n"
    "s_mov_b32 s66, 376832\n"
    "s_bitcmp1_b32 s78, 0\n"
    "s_cselect_b64 s[68:69], %7, %6\n"
    "s_and_b32 s69, s69, 0xffff\n"
    "s_mov_b32 s71, 0x20000\n"
    "s_cmp_lt_u32 s39, 144\n"
    "s_cselect_b32 s70, 5308416, 0\n"
    "v_lshrrev_b32 v250, 2, v239\n"
    "v_mul_u32_u24 v250, 9216, v250\n"
    "v_and_b32 v251, 3, v239\n"
    "v_lshl_add_u32 v250, v251, 4, v250\n"
    "s_lshr_b32 s81, s78, 1\n"
    "s_mul_i32 s81, s81, 147456\n"
    "s_lshl_b32 s80, s39, 6\n"
    "s_add_u32 s81, s81, s80\n"
    "v_add_u32 v227, s81, v250\n"
    "s_lshl_b32 s73, s78, 10\n"
    "s_add_u32 s73, s73, 36864\n"
    "s_mov_b32 s92, 1769472\n"
    "s_mov_b32 s36, 294912\n"
    "s_cmp_eq_u32 0, 0\n"
    "s_branch NOSW%=\n"
    "EVSW%=:\n"
    "s_mov_b32 s66, -1\n"
    "s_mov_b64 s[68:69], s[60:61]\n"
    "s_mov_b64 s[70:71], s[62:63]\n"
    "s_mov_b32 s36, s64\n"
    "s_mov_b32 s92, 0\n"
    "s_lshl_b32 s73, s78, 10\n"
    "s_add_u32 s73, s73, 86016\n"
    "s_mov_b32 s37, 0\n"
    "v_mov_b32 v227, v234\n"
    "s_cmp_eq_u32 0, 1\n"
    "s_branch NOSW%=\n"
    "SPEC%=:\n"
    "v_mfma_f32_16x16x32_bf16 v[128:131], a[64:67], a[0:3], v[176:179]\n"
    "v_exp_f32 v156, v156\n"
    "v_add_f32 v246, v196, v197\n"
    "v_cmp_ngt_f32 vcc, 1.0, v246\n"
    "s_or_b64 s[94:95], s[94:95], vcc\n"
    "v_and_b32 v246, 0x7f800000, v246\n"
    "v_min_u32 v246, 0x4b800000, v246\n"
    "v_max_u32 v246, 0xc800000, v246\n"
    "v_sub_u32 v248, 0x4c000000, v246\n"
    "v_lshrrev_b32 v246, 23, v246\n"
    "v_subrev_u32 v246, 25, v246\n"
    "v_cvt_f32_u32 v246, v246\n"
    "v_sub_f32 v180, v180, v246\n"
    "v_sub_f32 v181, v181, v246\n"
    "v_sub_f32 v182, v182, v246\n"
    "v_mfma_f32_16x16x32_bf16 v[128:131], a[68:71], a[4:7], v[128:131]\n"
    "s_mov_b32 m0, s75\n"
    "v_sub_f32 v183, v183, v246\n"
    "v_mul_f32 v196, v196, v248\n"
    "v_mul_f32 v197, v197, v248\n"
    "v_mfma_f32_16x16x32_bf16 v[128:131], a[72:75], a[8:11], v[128:131]\n"
    "v_exp_f32 v157, v157\n"
    "v_mul_f32 v32, v32, v248\n"
    "v_mfma_f32_16x16x32_bf16 v[128:131], a[76:79], a[12:15], v[128:131]\n"
    "buffer_load_dwordx4 v214, s[40:43], s72 offen offset:0 nt lds\n"
    "v_mfma_f32_16x16x32_bf16 v[132:135], a[80:83], a[0:3], v[176:179]\n"
    "v_exp_f32 v158, v158\n"
    "v_mul_f32 v33, v33, v248\n"
    "v_mul_f32 v34, v34, v248\n"
    "v_mfma_f32_16x16x32_bf16 v[132:135], a[84:87], a[4:7], v[132:135]\n"
    "v_cvt_pk_bf16_f32 v174, v156, v157\n"
    "v_mul_f32 v35, v35, v248\n"
    "v_mul_f32 v36, v36, v248\n"
    "v_mfma_f32_16x16x32_bf16 v[132:135], a[88:91], a[8:11], v[132:135]\n"
    "v_exp_f32 v159, v159\n"
    "v_mul_f32 v37, v37, v248\n"
    "v_mfma_f32_16x16x32_bf16 v[132:135], a[92:95], a[12:15], v[132:135]\n"
    "v_cvt_pk_bf16_f32 v175, v158, v159\n"
    "v_mul_f32 v38, v38, v248\n"
    "v_mul_f32 v39, v39, v248\n"
    "v_mfma_f32_16x16x32_bf16 v[136:139], a[96:99], a[0:3], v[176:179]\n"
    "v_exp_f32 v128, v128\n"
    "v_mul_f32 v40, v40, v248\n"
    "v_mfma_f32_16x16x32_bf16 v[136:139], a[100:103], a[4:7], v[136:139]\n"
    "v_mul_f32 v41, v41, v248\n"
    "v_mul_f32 v42, v42, v248\n"
    "v_mul_f32 v43, v43, v248\n"
    "v_mfma_f32_16x16x32_bf16 v[136:139], a[104:107], a[8:11], v[136:139]\n"
    "v_exp_f32 v129, v129\n"
    "v_mul_f32 v44, v44, v248\n"
    "v_mfma_f32_16x16x32_bf16 v[136:139], a[108:111], a[12:15], v[136:139]\n"
    "buffer_load_dwordx4 v215, s[40:43], s72 offen offset:1024 nt lds\n"
    "v_mfma_f32_16x16x32_bf16 v[140:143], a[112:115], a[0:3], v[176:179]\n"
    "v_exp_f32 v130, v130\n"
    "v_mul_f32 v45, v45, v248\n"
    "v_mul_f32 v46, v46, v248\n"
    "v_mfma_f32_16x16x32_bf16 v[140:143], a[116:119], a[4:7], v[140:143]\n"
    "v_cvt_pk_bf16_f32 v160, v128, v129\n"
    "v_mul_f32 v47, v47, v248\n"
    "v_mul_f32 v48, v48, v248\n"
    "v_mfma_f32_16x16x32_bf16 v[140:143], a[120:123], a[8:11], v[140:143]\n"
    "v_exp_f32 v131, v131\n"
    "v_mul_f32 v49, v49, v248\n"
    "v_mfma_f32_16x16x32_bf16 v[140:143], a[124:127], a[12:15], v[140:143]\n"
    "v_cvt_pk_bf16_f32 v161, v130, v131\n"
    "v_mul_f32 v50, v50, v248\n"
    "v_mul_f32 v51, v51, v248\n"
    "v_mfma_f32_16x16x32_bf16 v[96:99], a[128:131], v[168:171], v[96:99]\n"
    "v_exp_f32 v132, v132\n"
    "v_mul_f32 v52, v52, v248\n"
    "v_mul_f32 v53, v53, v248\n"
    "v_mfma_f32_16x16x32_bf16 v[96:99], a[132:135], v[172:175], v[96:99]\n"
    "buffer_load_dwordx4 v216, s[40:43], s72 offen offset:2048 nt lds\n"
    "v_mfma_f32_16x16x32_bf16 v[100:103], a[136:139], v[168:171], v[100:103]\n"
    "v_exp_f32 v133, v133\n"
    "v_mul_f32 v54, v54, v248\n"
    "v_mfma_f32_16x16x32_bf16 v[100:103], a[140:143], v[172:175], v[100:103]\n"
    "v_cvt_pk_bf16_f32 v162, v132, v133\n"
    "v_mul_f32 v55, v55, v248\n"
    "v_mul_f32 v56, v56, v248\n"
    "v_mfma_f32_16x16x32_bf16 v[104:107], a[144:147], v[168:171], v[104:107]\n"
    "v_exp_f32 v134, v134\n"
    "v_mul_f32 v57, v57, v248\n"
    "v_mfma_f32_16x16x32_bf16 v[104:107], a[148:151], v[172:175], v[104:107]\n"
    "v_mul_f32 v58, v58, v248\n"
    "v_mul_f32 v59, v59, v248\n"
    "v_mul_f32 v60, v60, v248\n"
    "v_mfma_f32_16x16x32_bf16 v[108:111], a[152:155], v[168:171], v[108:111]\n"
    "v_exp_f32 v135, v135\n"
    "v_mul_f32 v61, v61, v248\n"
    "v_mfma_f32_16x16x32_bf16 v[108:111], a[156:159], v[172:175], v[108:111]\n"
    "v_cvt_pk_bf16_f32 v163, v134, v135\n"
    "v_mul_f32 v62, v62, v248\n"
    "v_mul_f32 v63, v63, v248\n"
    "v_mfma_f32_16x16x32_bf16 v[112:115], a[160:163], v[168:171], v[112:115]\n"
    "v_exp_f32 v136, v136\n"
    "v_add_f32 v246, v200, v201\n"
    "v_mfma_f32_16x16x32_bf16 v[112:115], a[164:167], v[172:175], v[112:115]\n"
    "buffer_load_dwordx4 v217, s[40:43], s72 offen offset:3072 nt lds\n"
    "v_mfma_f32_16x16x32_bf16 v[116:119], a[168:171], v[168:171], v[116:119]\n"
    "v_cmp_ngt_f32 vcc, 1.0, v246\n"
    "s_or_b64 s[94:95], s[94:95], vcc\n"
    "v_and_b32 v246, 0x7f800000, v246\n"
    "v_mfma_f32_16x16x32_bf16 v[116:119], a[172:175], v[172:175], v[116:119]\n"
    "v_exp_f32 v137, v137\n"
    "s_nop 0\n"
    "v_cvt_pk_bf16_f32 v164, v136, v137\n"
    "v_min_u32 v246, 0x4b800000, v246\n"
    "v_mfma_f32_16x16x32_bf16 v[120:123], a[176:179], v[168:171], v[120:123]\n"
    "v_exp_f32 v138, v138\n"
    "v_max_u32 v246, 0xc800000, v246\n"
    "v_mfma_f32_16x16x32_bf16 v[120:123], a[180:183], v[172:175], v[120:123]\n"
    "s_mov_b32 m0, s77\n"
    "v_sub_u32 v248, 0x4c000000, v246\n"
    "v_lshrrev_b32 v246, 23, v246\n"
    "v_subrev_u32 v246, 25, v246\n"
    "v_mfma_f32_16x16x32_bf16 v[124:127], a[184:187], v[168:171], v[124:127]\n"
    "v_exp_f32 v139, v139\n"
    "v_cvt_f32_u32 v246, v246\n"
    "v_sub_f32 v184, v184, v246\n"
    "v_mfma_f32_16x16x32_bf16 v[124:127], a[188:191], v[172:175], v[124:127]\n"
    "buffer_load_dwordx4 v218, s[44:47], s72 offen offset:0 nt lds\n"
    "v_sub_f32 v185, v185, v246\n"
    "v_smfmac_f32_16x16x64_bf16 v[204:207], v[220:223], v[168:175], v224\n"
    "v_exp_f32 v140, v140\n"
    "v_sub_f32 v186, v186, v246\n"
    "v_sub_f32 v187, v187, v246\n"
    "v_mfma_f32_16x16x32_bf16 v[144:147], a[64:67], a[16:19], v[180:183]\n"
    "v_cvt_pk_bf16_f32 v165, v138, v139\n"
    "v_mul_f32 v200, v200, v248\n"
    "v_mul_f32 v201, v201, v248\n"
    "v_mfma_f32_16x16x32_bf16 v[144:147], a[68:71], a[20:23], v[144:147]\n"
    "v_exp_f32 v141, v141\n"
    "v_mul_f32 v64, v64, v248\n"
    "v_mfma_f32_16x16x32_bf16 v[144:147], a[72:75], a[24:27], v[144:147]\n"
    "v_cvt_pk_bf16_f32 v166, v140, v141\n"
    "ds_read_b64_tr_b16 a[128:129], v212 offset:16384\n"
    "v_mul_f32 v65, v65, v248\n"
    "v_mul_f32 v66, v66, v248\n"
    "v_mfma_f32_16x16x32_bf16 v[144:147], a[76:79], a[28:31], v[144:147]\n"
    "v_exp_f32 v142, v142\n"
    "v_mul_f32 v67, v67, v248\n"
    "v_mfma_f32_16x16x32_bf16 v[148:151], a[80:83], a[16:19], v[180:183]\n"
    "ds_read_b64_tr_b16 a[130:131], v212 offset:20480\n"
    "v_mul_f32 v68, v68, v248\n"
    "v_mul_f32 v69, v69, v248\n"
    "v_mfma_f32_16x16x32_bf16 v[148:151], a[84:87], a[20:23], v[148:151]\n"
    "v_exp_f32 v143, v143\n"
    "ds_read_b64_tr_b16 a[132:133], v212 offset:24576\n"
    "v_mul_f32 v70, v70, v248\n"
    "v_mfma_f32_16x16x32_bf16 v[148:151], a[88:91], a[24:27], v[148:151]\n"
    "buffer_load_dwordx4 v219, s[44:47], s72 offen offset:1024 nt lds\n"
    "v_mfma_f32_16x16x32_bf16 v[148:151], a[92:95], a[28:31], v[148:151]\n"
    "v_cvt_pk_bf16_f32 v167, v142, v143\n"
    "v_mul_f32 v71, v71, v248\n"
    "v_mul_f32 v72, v72, v248\n"
    "v_mul_f32 v73, v73, v248\n"
    "v_mfma_f32_16x16x32_bf16 v[152:155], a[96:99], a[16:19], v[180:183]\n"
    "v_exp_f32 v144, v144\n"
    "v_mul_f32 v74, v74, v248\n"
    "v_mul_f32 v75, v75, v248\n"
    "v_mfma_f32_16x16x32_bf16 v[152:155], a[100:103], a[20:23], v[152:155]\n"
    "ds_read_b64_tr_b16 a[134:135], v212 offset:28672\n"
    "v_mul_f32 v76, v76, v248\n"
    "v_mul_f32 v77, v77, v248\n"
    "v_mfma_f32_16x16x32_bf16 v[152:155], a[104:107], a[24:27], v[152:155]\n"
    "v_exp_f32 v145, v145\n"
    "ds_read_b64_tr_b16 a[136:137], v213 offset:16384\n"
    "v_mul_f32 v78, v78, v248\n"
    "v_mfma_f32_16x16x32_bf16 v[152:155], a[108:111], a[28:31], v[152:155]\n"
    "v_cvt_pk_bf16_f32 v168, v144, v145\n"
    "v_mul_f32 v79, v79, v248\n"
    "v_mul_f32 v80, v80, v248\n"
    "v_mfma_f32_16x16x32_bf16 v[156:159], a[112:115], a[16:19], v[180:183]\n"
    "v_exp_f32 v146, v146\n"
    "ds_read_b64_tr_b16 a[138:139], v213 offset:20480\n"
    "v_mul_f32 v81, v81, v248\n"
    "v_mfma_f32_16x16x32_bf16 v[156:159], a[116:119], a[20:23], v[156:159]\n"
    "buffer_load_dwordx4 v218, s[44:47], s72 offen offset:2048 nt lds\n"
    "v_mul_f32 v82, v82, v248\n"
    "v_mfma_f32_16x16x32_bf16 v[156:159], a[120:123], a[24:27], v[156:159]\n"
    "v_exp_f32 v147, v147\n"
    "v_mul_f32 v83, v83, v248\n"
    "v_mul_f32 v84, v84, v248\n"
    "v_mfma_f32_16x16x32_bf16 v[156:159], a[124:127], a[28:31], v[156:159]\n"
    "v_cvt_pk_bf16_f32 v169, v146, v147\n"
    "ds_read_b64_tr_b16 a[140:141], v213 offset:24576\n"
    "v_mul_f32 v85, v85, v248\n"
    "v_mfma_f32_16x16x32_bf16 v[0:3], a[192:195], v[160:163], v[0:3]\n"
    "v_exp_f32 v148, v148\n"
    "v_mul_f32 v86, v86, v248\n"
    "v_mul_f32 v87, v87, v248\n"
    "v_mfma_f32_16x16x32_bf16 v[0:3], a[196:199], v[164:167], v[0:3]\n"
    "ds_read_b64_tr_b16 a[142:143], v213 offset:28672\n"
    "v_mul_f32 v88, v88, v248\n"
    "v_mul_f32 v89, v89, v248\n"
    "v_mfma_f32_16x16x32_bf16 v[4:7], a[200:203], v[160:163], v[4:7]\n"
    "v_exp_f32 v149, v149\n"
    "ds_read_b64_tr_b16 a[144:145], v212 offset:16640\n"
    "v_mul_f32 v90, v90, v248\n"
    "v_mfma_f32_16x16x32_bf16 v[4:7], a[204:207], v[164:167], v[4:7]\n"
    "buffer_load_dwordx4 v219, s[44:47], s72 offen offset:3072 nt lds\n"
    "v_mfma_f32_16x16x32_bf16 v[8:11], a[208:211], v[160:163], v[8:11]\n"
    "v_exp_f32 v150, v150\n"
    "v_mul_f32 v91, v91, v248\n"
    "v_mul_f32 v92, v92, v248\n"
    "v_mfma_f32_16x16x32_bf16 v[8:11], a[212:215], v[164:167], v[8:11]\n"
    "v_cvt_pk_bf16_f32 v170, v148, v149\n"
    "v_mul_f32 v93, v93, v248\n"
    "v_mul_f32 v94, v94, v248\n"
    "v_mfma_f32_16x16x32_bf16 v[12:15], a[216:219], v[160:163], v[12:15]\n"
    "v_exp_f32 v151, v151\n"
    "ds_read_b64_tr_b16 a[146:147], v212 offset:20736\n"
    "v_mul_f32 v95, v95, v248\n"
    "v_mfma_f32_16x16x32_bf16 v[12:15], a[220:223], v[164:167], v[12:15]\n"
    "v_add_f32 v246, v204, v205\n"
    "v_cmp_ngt_f32 vcc, 1.0, v246\n"
    "s_or_b64 s[94:95], s[94:95], vcc\n"
    "v_and_b32 v246, 0x7f800000, v246\n"
    "v_mfma_f32_16x16x32_bf16 v[16:19], a[224:227], v[160:163], v[16:19]\n"
    "v_cvt_pk_bf16_f32 v171, v150, v151\n"
    "v_exp_f32 v152, v152\n"
    "v_mfma_f32_16x16x32_bf16 v[16:19], a[228:231], v[164:167], v[16:19]\n"
    "s_mov_b32 m0, s73\n"
    "ds_read_b64_tr_b16 a[148:149], v212 offset:24832\n"
    "v_min_u32 v246, 0x4b800000, v246\n"
    "v_max_u32 v246, 0xc800000, v246\n"
    "v_mfma_f32_16x16x32_bf16 v[20:23], a[232:235], v[160:163], v[20:23]\n"
    "v_exp_f32 v153, v153\n"
    "ds_read_b64_tr_b16 a[150:151], v212 offset:28928\n"
    "v_sub_u32 v248, 0x4c000000, v246\n"
    "v_mfma_f32_16x16x32_bf16 v[20:23], a[236:239], v[164:167], v[20:23]\n"
    "buffer_load_dwordx4 v227, s[68:71], s92 offen lds\n"
    "v_lshrrev_b32 v246, 23, v246\n"
    "v_mfma_f32_16x16x32_bf16 v[24:27], a[240:243], v[160:163], v[24:27]\n"
    "v_exp_f32 v154, v154\n"
    "v_subrev_u32 v246, 25, v246\n"
    "v_cvt_f32_u32 v246, v246\n"
    "v_mfma_f32_16x16x32_bf16 v[24:27], a[244:247], v[164:167], v[24:27]\n"
    "v_cvt_pk_bf16_f32 v172, v152, v153\n"
    "v_sub_f32 v188, v188, v246\n"
    "v_sub_f32 v189, v189, v246\n"
    "v_mfma_f32_16x16x32_bf16 v[28:31], a[248:251], v[160:163], v[28:31]\n"
    "v_exp_f32 v155, v155\n"
    "ds_read_b64_tr_b16 a[152:153], v213 offset:16640\n"
    "v_sub_f32 v190, v190, v246\n"
    "v_mfma_f32_16x16x32_bf16 v[28:31], a[252:255], v[164:167], v[28:31]\n"
    "v_cvt_pk_bf16_f32 v173, v154, v155\n"
    "v_sub_f32 v191, v191, v246\n"
    "v_mul_f32 v204, v204, v248\n"
    "v_smfmac_f32_16x16x64_bf16 v[192:195], v[220:223], v[160:167], v224\n"
    "v_exp_f32 v156, v156\n"
    "ds_read_b64_tr_b16 a[154:155], v213 offset:20736\n"
    "v_mul_f32 v205, v205, v248\n"
    "v_mfma_f32_16x16x32_bf16 v[128:131], a[64:67], a[32:35], v[184:187]\n"
    "buffer_load_dword v254, v228, s[48:51], s72 offen\n"
    "v_mul_f32 v96, v96, v248\n"
    "v_mfma_f32_16x16x32_bf16 v[128:131], a[68:71], a[36:39], v[128:131]\n"
    "v_exp_f32 v157, v157\n"
    "v_mul_f32 v97, v97, v248\n"
    "v_mul_f32 v98, v98, v248\n"
    "v_mfma_f32_16x16x32_bf16 v[128:131], a[72:75], a[40:43], v[128:131]\n"
    "v_cvt_pk_bf16_f32 v174, v156, v157\n"
    "ds_read_b64_tr_b16 a[156:157], v213 offset:24832\n"
    "v_mul_f32 v99, v99, v248\n"
    "v_mfma_f32_16x16x32_bf16 v[128:131], a[76:79], a[44:47], v[128:131]\n"
    "v_exp_f32 v158, v158\n"
    "v_mul_f32 v100, v100, v248\n"
    "v_mfma_f32_16x16x32_bf16 v[132:135], a[80:83], a[32:35], v[184:187]\n"
    "ds_read_b64_tr_b16 a[158:159], v213 offset:28928\n"
    "ds_read_b64_tr_b16 a[160:161], v212 offset:16896\n"
    "v_mul_f32 v101, v101, v248\n"
    "v_mul_f32 v102, v102, v248\n"
    "v_mfma_f32_16x16x32_bf16 v[132:135], a[84:87], a[36:39], v[132:135]\n"
    "v_exp_f32 v159, v159\n"
    "v_mul_f32 v103, v103, v248\n"
    "v_mfma_f32_16x16x32_bf16 v[132:135], a[88:91], a[40:43], v[132:135]\n"
    "v_cvt_pk_bf16_f32 v175, v158, v159\n"
    "ds_read_b64_tr_b16 a[162:163], v212 offset:20992\n"
    "v_mul_f32 v104, v104, v248\n"
    "v_mfma_f32_16x16x32_bf16 v[132:135], a[92:95], a[44:47], v[132:135]\n"
    "ds_read_b64_tr_b16 a[164:165], v212 offset:25088\n"
    "v_mul_f32 v105, v105, v248\n"
    "v_mul_f32 v106, v106, v248\n"
    "v_mfma_f32_16x16x32_bf16 v[136:139], a[96:99], a[32:35], v[184:187]\n"
    "v_exp_f32 v128, v128\n"
    "v_mul_f32 v107, v107, v248\n"
    "v_mfma_f32_16x16x32_bf16 v[136:139], a[100:103], a[36:39], v[136:139]\n"
    "ds_read_b64_tr_b16 a[166:167], v212 offset:29184\n"
    "v_mul_f32 v108, v108, v248\n"
    "v_mul_f32 v109, v109, v248\n"
    "v_mfma_f32_16x16x32_bf16 v[136:139], a[104:107], a[40:43], v[136:139]\n"
    "v_exp_f32 v129, v129\n"
    "v_mul_f32 v110, v110, v248\n"
    "v_mul_f32 v111, v111, v248\n"
    "v_mfma_f32_16x16x32_bf16 v[136:139], a[108:111], a[44:47], v[136:139]\n"
    "v_cvt_pk_bf16_f32 v160, v128, v129\n"
    "ds_read_b64_tr_b16 a[168:169], v213 offset:16896\n"
    "v_mul_f32 v112, v112, v248\n"
    "v_mfma_f32_16x16x32_bf16 v[140:143], a[112:115], a[32:35], v[184:187]\n"
    "v_exp_f32 v130, v130\n"
    "v_mul_f32 v113, v113, v248\n"
    "v_mfma_f32_16x16x32_bf16 v[140:143], a[116:119], a[36:39], v[140:143]\n"
    "ds_read_b64_tr_b16 a[170:171], v213 offset:20992\n"
    "v_mul_f32 v114, v114, v248\n"
    "v_mul_f32 v115, v115, v248\n"
    "v_mfma_f32_16x16x32_bf16 v[140:143], a[120:123], a[40:43], v[140:143]\n"
    "v_exp_f32 v131, v131\n"
    "v_mul_f32 v116, v116, v248\n"
    "v_mul_f32 v117, v117, v248\n"
    "v_mfma_f32_16x16x32_bf16 v[140:143], a[124:127], a[44:47], v[140:143]\n"
    "v_cvt_pk_bf16_f32 v161, v130, v131\n"
    "ds_read_b64_tr_b16 a[172:173], v213 offset:25088\n"
    "v_mul_f32 v118, v118, v248\n"
    "v_mfma_f32_16x16x32_bf16 v[32:35], a[192:195], v[168:171], v[32:35]\n"
    "v_exp_f32 v132, v132\n"
    "v_mul_f32 v119, v119, v248\n"
    "v_mfma_f32_16x16x32_bf16 v[32:35], a[196:199], v[172:175], v[32:35]\n"
    "ds_read_b64_tr_b16 a[174:175], v213 offset:29184\n"
    "v_mul_f32 v120, v120, v248\n"
    "v_mul_f32 v121, v121, v248\n"
    "v_mfma_f32_16x16x32_bf16 v[36:39], a[200:203], v[168:171], v[36:39]\n"
    "v_exp_f32 v133, v133\n"
    "v_mul_f32 v122, v122, v248\n"
    "v_mfma_f32_16x16x32_bf16 v[36:39], a[204:207], v[172:175], v[36:39]\n"
    "v_cvt_pk_bf16_f32 v162, v132, v133\n"
    "ds_read_b64_tr_b16 a[176:177], v212 offset:17152\n"
    "v_mul_f32 v123, v123, v248\n"
    "v_mfma_f32_16x16x32_bf16 v[40:43], a[208:211], v[168:171], v[40:43]\n"
    "v_exp_f32 v134, v134\n"
    "v_mul_f32 v124, v124, v248\n"
    "v_mfma_f32_16x16x32_bf16 v[40:43], a[212:215], v[172:175], v[40:43]\n"
    "ds_read_b64_tr_b16 a[178:179], v212 offset:21248\n"
    "v_mul_f32 v125, v125, v248\n"
    "v_mul_f32 v126, v126, v248\n"
    "v_mfma_f32_16x16x32_bf16 v[44:47], a[216:219], v[168:171], v[44:47]\n"
    "v_exp_f32 v135, v135\n"
    "v_mul_f32 v127, v127, v248\n"
    "v_mfma_f32_16x16x32_bf16 v[44:47], a[220:223], v[172:175], v[44:47]\n"
    "v_cvt_pk_bf16_f32 v163, v134, v135\n"
    "ds_read_b64_tr_b16 a[180:181], v212 offset:25344\n"
    "v_add_f32 v246, v192, v193\n"
    "v_mfma_f32_16x16x32_bf16 v[48:51], a[224:227], v[168:171], v[48:51]\n"
    "v_cmp_ngt_f32 vcc, 1.0, v246\n"
    "s_or_b64 s[94:95], s[94:95], vcc\n"
    "v_and_b32 v246, 0x7f800000, v246\n"
    "v_mfma_f32_16x16x32_bf16 v[48:51], a[228:231], v[172:175], v[48:51]\n"
    "v_exp_f32 v136, v136\n"
    "ds_read_b64_tr_b16 a[182:183], v212 offset:29440\n"
    "v_mfma_f32_16x16x32_bf16 v[52:55], a[232:235], v[168:171], v[52:55]\n"
    "v_exp_f32 v137, v137\n"
    "v_min_u32 v246, 0x4b800000, v246\n"
    "v_mfma_f32_16x16x32_bf16 v[52:55], a[236:239], v[172:175], v[52:55]\n"
    "v_cvt_pk_bf16_f32 v164, v136, v137\n"
    "ds_read_b64_tr_b16 a[184:185], v213 offset:17152\n"
    "v_max_u32 v246, 0xc800000, v246\n"
    "v_mfma_f32_16x16x32_bf16 v[56:59], a[240:243], v[168:171], v[56:59]\n"
    "v_exp_f32 v138, v138\n"
    "v_sub_u32 v248, 0x4c000000, v246\n"
    "v_mfma_f32_16x16x32_bf16 v[56:59], a[244:247], v[172:175], v[56:59]\n"
    "ds_read_b64_tr_b16 a[186:187], v213 offset:21248\n"
    "v_lshrrev_b32 v246, 23, v246\n"
    "v_subrev_u32 v246, 25, v246\n"
    "v_mfma_f32_16x16x32_bf16 v[60:63], a[248:251], v[168:171], v[60:63]\n"
    "v_exp_f32 v139, v139\n"
    "v_cvt_f32_u32 v246, v246\n"
    "v_mfma_f32_16x16x32_bf16 v[60:63], a[252:255], v[172:175], v[60:63]\n"
    "v_cvt_pk_bf16_f32 v165, v138, v139\n"
    "ds_read_b64_tr_b16 a[188:189], v213 offset:25344\n"
    "v_sub_f32 v176, v176, v246\n"
    "v_smfmac_f32_16x16x64_bf16 v[196:199], v[220:223], v[168:175], v224\n"
    "v_exp_f32 v140, v140\n"
    "v_sub_f32 v177, v177, v246\n"
    "v_mfma_f32_16x16x32_bf16 v[144:147], a[64:67], a[48:51], v[188:191]\n"
    "ds_read_b64_tr_b16 a[190:191], v213 offset:29440\n"
    "v_sub_f32 v178, v178, v246\n"
    "v_sub_f32 v179, v179, v246\n"
    "v_mfma_f32_16x16x32_bf16 v[144:147], a[68:71], a[52:55], v[144:147]\n"
    "v_exp_f32 v141, v141\n"
    "v_mul_f32 v192, v192, v248\n"
    "v_mfma_f32_16x16x32_bf16 v[144:147], a[72:75], a[56:59], v[144:147]\n"
    "v_cvt_pk_bf16_f32 v166, v140, v141\n"
    "ds_read_b128 a[64:67], v208 offset:0\n"
    "v_mul_f32 v193, v193, v248\n"
    "v_mfma_f32_16x16x32_bf16 v[144:147], a[76:79], a[60:63], v[144:147]\n"
    "v_exp_f32 v142, v142\n"
    "v_mul_f32 v0, v0, v248\n"
    "v_mfma_f32_16x16x32_bf16 v[148:151], a[80:83], a[48:51], v[188:191]\n"
    "ds_read_b128 a[68:71], v209 offset:0\n"
    "ds_read_b128 a[72:75], v210 offset:0\n"
    "v_mul_f32 v1, v1, v248\n"
    "v_mul_f32 v2, v2, v248\n"
    "v_mfma_f32_16x16x32_bf16 v[148:151], a[84:87], a[52:55], v[148:151]\n"
    "v_exp_f32 v143, v143\n"
    "v_mul_f32 v3, v3, v248\n"
    "v_mfma_f32_16x16x32_bf16 v[148:151], a[88:91], a[56:59], v[148:151]\n"
    "v_cvt_pk_bf16_f32 v167, v142, v143\n"
    "ds_read_b128 a[76:79], v211 offset:0\n"
    "v_mul_f32 v4, v4, v248\n"
    "v_mfma_f32_16x16x32_bf16 v[148:151], a[92:95], a[60:63], v[148:151]\n"
    "ds_read_b128 a[80:83], v208 offset:4096\n"
    "ds_read_b128 a[84:87], v209 offset:4096\n"
    "v_mul_f32 v5, v5, v248\n"
    "v_mul_f32 v6, v6, v248\n"
    "v_mfma_f32_16x16x32_bf16 v[152:155], a[96:99], a[48:51], v[188:191]\n"
    "v_exp_f32 v144, v144\n"
    "v_mul_f32 v7, v7, v248\n"
    "v_mfma_f32_16x16x32_bf16 v[152:155], a[100:103], a[52:55], v[152:155]\n"
    "ds_read_b128 a[88:91], v210 offset:4096\n"
    "ds_read_b128 a[92:95], v211 offset:4096\n"
    "v_mul_f32 v8, v8, v248\n"
    "v_mul_f32 v9, v9, v248\n"
    "v_mfma_f32_16x16x32_bf16 v[152:155], a[104:107], a[56:59], v[152:155]\n"
    "v_exp_f32 v145, v145\n"
    "v_mul_f32 v10, v10, v248\n"
    "v_mfma_f32_16x16x32_bf16 v[152:155], a[108:111], a[60:63], v[152:155]\n"
    "v_cvt_pk_bf16_f32 v168, v144, v145\n"
    "ds_read_b128 a[96:99], v208 offset:8192\n"
    "v_mul_f32 v11, v11, v248\n"
    "v_mfma_f32_16x16x32_bf16 v[156:159], a[112:115], a[48:51], v[188:191]\n"
    "v_exp_f32 v146, v146\n"
    "v_mul_f32 v12, v12, v248\n"
    "v_mfma_f32_16x16x32_bf16 v[156:159], a[116:119], a[52:55], v[156:159]\n"
    "ds_read_b128 a[100:103], v209 offset:8192\n"
    "ds_read_b128 a[104:107], v210 offset:8192\n"
    "v_mul_f32 v13, v13, v248\n"
    "v_mul_f32 v14, v14, v248\n"
    "v_mfma_f32_16x16x32_bf16 v[156:159], a[120:123], a[56:59], v[156:159]\n"
    "v_exp_f32 v147, v147\n"
    "v_mul_f32 v15, v15, v248\n"
    "v_mfma_f32_16x16x32_bf16 v[156:159], a[124:127], a[60:63], v[156:159]\n"
    "v_cvt_pk_bf16_f32 v169, v146, v147\n"
    "ds_read_b128 a[108:111], v211 offset:8192\n"
    "v_mul_f32 v16, v16, v248\n"
    "v_mfma_f32_16x16x32_bf16 v[64:67], a[192:195], v[160:163], v[64:67]\n"
    "v_exp_f32 v148, v148\n"
    "v_mul_f32 v17, v17, v248\n"
    "v_mfma_f32_16x16x32_bf16 v[64:67], a[196:199], v[164:167], v[64:67]\n"
    "ds_read_b128 a[112:115], v208 offset:12288\n"
    "v_mul_f32 v18, v18, v248\n"
    "v_mul_f32 v19, v19, v248\n"
    "v_mfma_f32_16x16x32_bf16 v[68:71], a[200:203], v[160:163], v[68:71]\n"
    "v_exp_f32 v149, v149\n"
    "v_mul_f32 v20, v20, v248\n"
    "v_mfma_f32_16x16x32_bf16 v[68:71], a[204:207], v[164:167], v[68:71]\n"
    "v_cvt_pk_bf16_f32 v170, v148, v149\n"
    "ds_read_b128 a[116:119], v209 offset:12288\n"
    "v_mul_f32 v21, v21, v248\n"
    "v_mfma_f32_16x16x32_bf16 v[72:75], a[208:211], v[160:163], v[72:75]\n"
    "v_exp_f32 v150, v150\n"
    "v_mul_f32 v22, v22, v248\n"
    "v_mfma_f32_16x16x32_bf16 v[72:75], a[212:215], v[164:167], v[72:75]\n"
    "s_add_u32 s72, s72, 0x4000\n"
    "ds_read_b128 a[120:123], v210 offset:12288\n"
    "v_mul_f32 v23, v23, v248\n"
    "v_mfma_f32_16x16x32_bf16 v[76:79], a[216:219], v[160:163], v[76:79]\n"
    "v_exp_f32 v151, v151\n"
    "v_mul_f32 v24, v24, v248\n"
    "v_mfma_f32_16x16x32_bf16 v[76:79], a[220:223], v[164:167], v[76:79]\n"
    "s_add_u32 s73, s73, s37\n"
    "v_cvt_pk_bf16_f32 v171, v150, v151\n"
    "ds_read_b128 a[124:127], v211 offset:12288\n"
    "v_mfma_f32_16x16x32_bf16 v[80:83], a[224:227], v[160:163], v[80:83]\n"
    "v_exp_f32 v152, v152\n"
    "v_mul_f32 v25, v25, v248\n"
    "v_mfma_f32_16x16x32_bf16 v[80:83], a[228:231], v[164:167], v[80:83]\n"
    "s_add_u32 s92, s92, s36\n"
    "v_mul_f32 v26, v26, v248\n"
    "v_mul_f32 v27, v27, v248\n"
    "v_mfma_f32_16x16x32_bf16 v[84:87], a[232:235], v[160:163], v[84:87]\n"
    "v_exp_f32 v153, v153\n"
    "v_mfma_f32_16x16x32_bf16 v[84:87], a[236:239], v[164:167], v[84:87]\n"
    "v_cvt_pk_bf16_f32 v172, v152, v153\n"
    "v_mul_f32 v28, v28, v248\n"
    "v_mfma_f32_16x16x32_bf16 v[88:91], a[240:243], v[160:163], v[88:91]\n"
    "v_exp_f32 v154, v154\n"
    "v_mfma_f32_16x16x32_bf16 v[88:91], a[244:247], v[164:167], v[88:91]\n"
    "s_waitcnt vmcnt(2) lgkmcnt(0)\n"
    "s_barrier\n"
    "v_mfma_f32_16x16x32_bf16 v[92:95], a[248:251], v[160:163], v[92:95]\n"
    "v_mul_f32 v29, v29, v248\n"
    "v_mul_f32 v30, v30, v248\n"
    "v_mfma_f32_16x16x32_bf16 v[92:95], a[252:255], v[164:167], v[92:95]\n"
    "v_exp_f32 v155, v155\n"
    "v_smfmac_f32_16x16x64_bf16 v[200:203], v[220:223], v[160:167], v224\n"
    "v_cvt_pk_bf16_f32 v173, v154, v155\n"
    "v_mul_f32 v31, v31, v248\n"
    "s_nop 1\n"
    "s_branch BODY0%=\n"
    "TSC%=:\n"
    "s_mov_b32 s87, 0\n"
    "TPC%=:\n"
    "buffer_load_dwordx4 v[12:15], v244, s[56:59], s84 offen sc1\n"
    "buffer_load_dwordx4 v[16:19], v244, s[56:59], s85 offen sc1\n"
    "buffer_load_dwordx4 v[20:23], v60, s[56:59], s84 offen sc1\n"
    "buffer_load_dwordx4 v[24:27], v60, s[56:59], s85 offen sc1\n"
    "s_waitcnt vmcnt(0)\n"
    "v_min3_u32 v28, v13, v15, v17\n"
    "v_max3_u32 v29, v13, v15, v17\n"
    "v_min3_u32 v28, v28, v19, v21\n"
    "v_max3_u32 v29, v29, v19, v21\n"
    "v_min3_u32 v28, v28, v23, v25\n"
    "v_max3_u32 v29, v29, v23, v25\n"
    "v_min3_u32 v28, v28, v27, v27\n"
    "v_max3_u32 v29, v29, v27, v27\n"
    "v_xor_b32 v28, s67, v28\n"
    "v_xor_b32 v29, s67, v29\n"
    "v_or_b32 v28, v28, v29\n"
    "v_cmp_ne_u32 vcc, 0, v28\n"
    "s_nop 1\n"
    "s_cmp_eq_u64 vcc, 0\n"
    "s_cbranch_scc1 TDC%=\n"
    "s_add_u32 s87, s87, 1\n"
    "s_cmp_gt_u32 s87, 262144\n"
    "s_cbranch_scc1 TDC%=\n"
    "s_sleep 1\n"
    "s_branch TPC%=\n"
    "OSA%=:\n"
    "s_mov_b32 s87, 0\n"
    "OPA%=:\n"
    "buffer_load_dword v251, v229, s[56:59], s85 offen offset:0 sc1\n"
    "s_waitcnt vmcnt(0)\n"
    "v_cmp_eq_u32 vcc, 0, v251\n"
    "s_nop 1\n"
    "s_cmp_eq_u64 vcc, 0\n"
    "s_cbranch_scc1 ODA%=\n"
    "s_add_u32 s87, s87, 1\n"
    "s_cmp_gt_u32 s87, 262144\n"
    "s_cbranch_scc1 ODA%=\n"
    "s_sleep 1\n"
    "s_branch OPA%=\n"
    "OSB%=:\n"
    "s_mov_b32 s87, 0\n"
    "OPB%=:\n"
    "buffer_load_dword v251, v229, s[56:59], s85 offen offset:16 sc1\n"
    "s_waitcnt vmcnt(0)\n"
    "v_cmp_eq_u32 vcc, 0, v251\n"
    "s_nop 1\n"
    "s_cmp_eq_u64 vcc, 0\n"
    "s_cbranch_scc1 ODB%=\n"
    "s_add_u32 s87, s87, 1\n"
    "s_cmp_gt_u32 s87, 262144\n"
    "s_cbranch_scc1 ODB%=\n"
    "s_sleep 1\n"
    "s_branch OPB%=\n"
    "TSM%=:\n"
    "s_mov_b32 s87, 0\n"
    "TPM%=:\n"
    "buffer_load_dwordx4 v[32:35], v248, s[56:59], s92 offen offset:0 sc1\n"
    "buffer_load_dwordx4 v[40:43], v248, s[56:59], s92 offen offset:1024 sc1\n"
    "buffer_load_dwordx4 v[48:51], v248, s[56:59], s92 offen offset:2048 sc1\n"
    "buffer_load_dwordx4 v[56:59], v248, s[56:59], s92 offen offset:3072 sc1\n"
    "buffer_load_dwordx4 v[64:67], v252, s[56:59], s92 offen offset:0 sc1\n"
    "s_waitcnt vmcnt(0)\n"
    "v_min3_u32 v72, v33, v35, v41\n"
    "v_max3_u32 v73, v33, v35, v41\n"
    "v_min3_u32 v72, v72, v43, v49\n"
    "v_max3_u32 v73, v73, v43, v49\n"
    "v_min3_u32 v72, v72, v51, v57\n"
    "v_max3_u32 v73, v73, v51, v57\n"
    "v_min3_u32 v72, v72, v59, v65\n"
    "v_max3_u32 v73, v73, v59, v65\n"
    "v_min3_u32 v72, v72, v67, v67\n"
    "v_max3_u32 v73, v73, v67, v67\n"
    "v_xor_b32 v72, s67, v72\n"
    "v_xor_b32 v73, s67, v73\n"
    "v_or_b32 v72, v72, v73\n"
    "v_cmp_ne_u32 vcc, 0, v72\n"
    "s_nop 1\n"
    "s_cmp_eq_u64 vcc, 0\n"
    "s_cbranch_scc1 TDM%=\n"
    "s_add_u32 s87, s87, 1\n"
    "s_cmp_gt_u32 s87, 262144\n"
    "s_cbranch_scc1 TDM%=\n"
    "s_sleep 1\n"
    "s_branch TPM%=\n"
    "REFINE%=:\n"
    "s_waitcnt vmcnt(0) lgkmcnt(0)\n"
    "s_mov_b32 s92, 1\n"
    "v_readfirstlane_b32 s38, v206\n"
    "s_andn2_b32 s36, 0x7ff, s38\n"
    "s_lshr_b32 s37, s36, 5\n"
    "s_lshl_b32 s37, s37, 2\n"
    "s_bfe_u32 s84, s36, 0x20002\n"
    "s_or_b32 s37, s37, s84\n"
    "s_mul_i32 s37, s37, 5120\n"
    "s_cmp_eq_u32 s37, s76\n"
    "s_cselect_b32 s84, 2560, 0\n"
    "s_cmp_eq_u32 s37, s77\n"
    "s_cselect_b32 s84, 2560, s84\n"
    "s_add_u32 s37, s37, s84\n"
    "s_lshl_b32 s80, s78, 6\n"
    "v_add_u32 v0, s80, v239\n"
    "v_lshrrev_b32 v1, 1, v0\n"
    "v_add_u32 v3, 256, v0\n"
    "v_min_u32 v3, 287, v3\n"
    "s_mov_b32 s80, 32768\n"
    "s_mov_b32 s81, 37376\n"
    "v_lshlrev_b32 v5, 4, v0\n"
    "buffer_load_dwordx4 v[132:135], v5, s[56:59], s81 offen sc1\n"
    "buffer_load_dwordx4 v[136:139], v5, s[56:59], s80 offen sc1\n"
    "v_lshrrev_b32 v4, 2, v0\n"
    "v_lshlrev_b32 v4, 8, v4\n"
    "v_and_b32 v5, 3, v0\n"
    "v_lshl_add_u32 v6, v5, 2, v4\n"
    "v_lshlrev_b32 v118, 3, v0\n"
    "v_add_u32 v118, 126976, v118\n"
    "v_lshlrev_b32 v5, 4, v3\n"
    "buffer_load_dwordx4 v[140:143], v5, s[56:59], s81 offen sc1\n"
    "buffer_load_dwordx4 v[144:147], v5, s[56:59], s80 offen sc1\n"
    "v_lshrrev_b32 v4, 2, v3\n"
    "v_lshlrev_b32 v4, 8, v4\n"
    "v_and_b32 v5, 3, v3\n"
    "v_lshl_add_u32 v7, v5, 2, v4\n"
    "v_lshlrev_b32 v119, 3, v3\n"
    "v_add_u32 v119, 126976, v119\n"
    "s_lshr_b32 s80, s74, 4\n"
    "s_mul_i32 s80, s80, 18432\n"
    "s_and_b32 s81, s74, 15\n"
    "s_lshl_b32 s81, s81, 4\n"
    "s_add_u32 s80, s80, s81\n"
    "s_add_u32 s80, s80, 1048576\n"
    "buffer_load_dword v108, v6, s[56:59], s80 offen sc1\n"
    "buffer_load_dword v109, v7, s[56:59], s80 offen sc1\n"
    "s_lshr_b32 s80, s75, 4\n"
    "s_mul_i32 s80, s80, 18432\n"
    "s_and_b32 s81, s75, 15\n"
    "s_lshl_b32 s81, s81, 4\n"
    "s_add_u32 s80, s80, s81\n"
    "s_add_u32 s80, s80, 1048576\n"
    "buffer_load_dword v110, v6, s[56:59], s80 offen sc1\n"
    "buffer_load_dword v111, v7, s[56:59], s80 offen sc1\n"
    "s_lshr_b32 s80, s36, 4\n"
    "s_mul_i32 s80, s80, 18432\n"
    "s_and_b32 s81, s36, 15\n"
    "s_lshl_b32 s81, s81, 4\n"
    "s_add_u32 s80, s80, s81\n"
    "s_add_u32 s80, s80, 1048576\n"
    "buffer_load_dword v112, v6, s[56:59], s80 offen sc1\n"
    "buffer_load_dword v113, v7, s[56:59], s80 offen sc1\n"
    "s_lshl_b32 s80, s88, 9\n"
    "s_lshl_b32 s81, s89, 5\n"
    "s_add_u32 s80, s80, s81\n"
    "v_and_b32 v3, 1, v0\n"
    "v_lshl_add_u32 v3, v3, 4, s80\n"
    "v_add_u32 v2, 0, v1\n"
    "v_min_u32 v2, 575, v2\n"
    "v_lshl_add_u32 v2, v2, 13, v3\n"
    "global_load_dwordx4 v[36:39], v2, %3\n"
    "v_add_u32 v2, 128, v1\n"
    "v_min_u32 v2, 575, v2\n"
    "v_lshl_add_u32 v2, v2, 13, v3\n"
    "global_load_dwordx4 v[40:43], v2, %3\n"
    "v_add_u32 v2, 256, v1\n"
    "v_min_u32 v2, 575, v2\n"
    "v_lshl_add_u32 v2, v2, 13, v3\n"
    "global_load_dwordx4 v[44:47], v2, %3\n"
    "v_add_u32 v2, 384, v1\n"
    "v_min_u32 v2, 575, v2\n"
    "v_lshl_add_u32 v2, v2, 13, v3\n"
    "global_load_dwordx4 v[48:51], v2, %3\n"
    "v_add_u32 v2, 512, v1\n"
    "v_min_u32 v2, 575, v2\n"
    "v_lshl_add_u32 v2, v2, 13, v3\n"
    "global_load_dwordx4 v[52:55], v2, %3\n"
    "v_lshrrev_b32 v4, 4, v0\n"
    "v_and_b32 v5, 15, v239\n"
    "s_and_b32 s80, s88, 3\n"
    "s_lshl_b32 s80, s80, 4\n"
    "s_add_u32 s80, s80, s89\n"
    "s_mul_i32 s80, s80, 18432\n"
    "v_lshlrev_b32 v3, 4, v5\n"
    "v_mul_u32_u24 v2, 1152, v4\n"
    "v_add_u32 v2, v2, v3\n"
    "v_cmp_gt_u32 vcc, 8, v5\n"
    "v_mov_b32 v6, 2147483632\n"
    "s_nop 0\n"
    "v_cndmask_b32 v6, v6, v2, vcc\n"
    "buffer_load_dwordx4 v[56:59], v2, s[52:55], s80 offen offset:0\n"
    "buffer_load_dwordx4 v[76:79], v3, s[64:67], 0 offen offset:0\n"
    "buffer_load_dwordx4 v[60:63], v2, s[52:55], s80 offen offset:256\n"
    "buffer_load_dwordx4 v[80:83], v3, s[64:67], 0 offen offset:256\n"
    "buffer_load_dwordx4 v[64:67], v2, s[52:55], s80 offen offset:512\n"
    "buffer_load_dwordx4 v[84:87], v3, s[64:67], 0 offen offset:512\n"
    "buffer_load_dwordx4 v[68:71], v2, s[52:55], s80 offen offset:768\n"
    "buffer_load_dwordx4 v[88:91], v3, s[64:67], 0 offen offset:768\n"
    "buffer_load_dwordx4 v[72:75], v6, s[52:55], s80 offen offset:1024\n"
    "buffer_load_dwordx4 v[92:95], v3, s[64:67], 0 offen offset:1024\n"
    "v_lshrrev_b32 v2, 2, v4\n"
    "v_lshlrev_b32 v2, 8, v2\n"
    "v_and_b32 v6, 3, v4\n"
    "v_lshl_add_u32 v2, v6, 1, v2\n"
    "s_lshl_b32 s82, s88, 16\n"
    "s_lshr_b32 s80, s89, 3\n"
    "s_lshl_b32 s80, s80, 13\n"
    "s_add_u32 s82, s82, s80\n"
    "s_bfe_u32 s80, s89, 0x20001\n"
    "s_lshl_b32 s80, s80, 10\n"
    "s_add_u32 s82, s82, s80\n"
    "s_and_b32 s80, s89, 1\n"
    "s_lshl_b32 s80, s80, 3\n"
    "s_add_u32 s82, s82, s80\n"
    "s_add_u32 s82, s82, 25165824\n"
    "s_lshr_b32 s80, s74, 7\n"
    "s_lshl_b32 s80, s80, 20\n"
    "s_bfe_u32 s81, s74, 0x20005\n"
    "s_lshl_b32 s81, s81, 14\n"
    "s_add_u32 s80, s80, s81\n"
    "s_bfe_u32 s81, s74, 0x10004\n"
    "s_lshl_b32 s81, s81, 12\n"
    "s_add_u32 s80, s80, s81\n"
    "s_and_b32 s81, s74, 15\n"
    "s_lshl_b32 s81, s81, 4\n"
    "s_add_u32 s80, s80, s81\n"
    "s_add_u32 s80, s80, s82\n"
    "buffer_load_ushort v96, v2, s[56:59], s80 offen sc1\n"
    "s_lshr_b32 s80, s75, 7\n"
    "s_lshl_b32 s80, s80, 20\n"
    "s_bfe_u32 s81, s75, 0x20005\n"
    "s_lshl_b32 s81, s81, 14\n"
    "s_add_u32 s80, s80, s81\n"
    "s_bfe_u32 s81, s75, 0x10004\n"
    "s_lshl_b32 s81, s81, 12\n"
    "s_add_u32 s80, s80, s81\n"
    "s_and_b32 s81, s75, 15\n"
    "s_lshl_b32 s81, s81, 4\n"
    "s_add_u32 s80, s80, s81\n"
    "s_add_u32 s80, s80, s82\n"
    "buffer_load_ushort v97, v2, s[56:59], s80 offen sc1\n"
    "s_lshr_b32 s80, s36, 7\n"
    "s_lshl_b32 s80, s80, 20\n"
    "s_bfe_u32 s81, s36, 0x20005\n"
    "s_lshl_b32 s81, s81, 14\n"
    "s_add_u32 s80, s80, s81\n"
    "s_bfe_u32 s81, s36, 0x10004\n"
    "s_lshl_b32 s81, s81, 12\n"
    "s_add_u32 s80, s80, s81\n"
    "s_and_b32 s81, s36, 15\n"
    "s_lshl_b32 s81, s81, 4\n"
    "s_add_u32 s80, s80, s81\n"
    "s_add_u32 s80, s80, s82\n"
    "buffer_load_ushort v98, v2, s[56:59], s80 offen sc1\n"
    "s_lshl_b32 s80, s88, 19\n"
    "s_lshl_b32 s81, s89, 15\n"
    "s_add_u32 s80, s80, s81\n"
    "s_lshl_b32 s81, s78, 13\n"
    "s_add_u32 s80, s80, s81\n"
    "v_and_b32 v156, 48, v239\n"
    "v_lshl_add_u32 v156, v5, 8, v156\n"
    "v_add_u32 v156, s80, v156\n"
    "v_add_u32 v157, 4096, v156\n"
    "s_waitcnt vmcnt(23)\n"
    "v_fma_mix_f32 v8, v108, v132, 0 op_sel_hi:[1,0,0]\n"
    "v_fma_mix_f32 v9, v108, v134, 0 op_sel:[1,0,0] op_sel_hi:[1,0,0]\n"
    "v_cvt_pk_bf16_f32 v100, v8, v9\n"
    "v_lshlrev_b32 v8, 16, v100\n"
    "v_and_b32 v9, 0xffff0000, v100\n"
    "v_add_f32 v8, v8, v136\n"
    "v_add_f32 v9, v9, v138\n"
    "v_cvt_pk_bf16_f32 v100, v8, v9\n"
    "v_lshlrev_b32 v8, 16, v100\n"
    "v_and_b32 v9, 0xffff0000, v100\n"
    "ds_write_b64 v118, v[8:9] offset:0\n"
    "s_waitcnt vmcnt(22)\n"
    "v_fma_mix_f32 v10, v109, v140, 0 op_sel_hi:[1,0,0]\n"
    "v_fma_mix_f32 v11, v109, v142, 0 op_sel:[1,0,0] op_sel_hi:[1,0,0]\n"
    "v_cvt_pk_bf16_f32 v100, v10, v11\n"
    "v_lshlrev_b32 v10, 16, v100\n"
    "v_and_b32 v11, 0xffff0000, v100\n"
    "v_add_f32 v10, v10, v144\n"
    "v_add_f32 v11, v11, v146\n"
    "v_cvt_pk_bf16_f32 v100, v10, v11\n"
    "v_lshlrev_b32 v10, 16, v100\n"
    "v_and_b32 v11, 0xffff0000, v100\n"
    "ds_write_b64 v119, v[10:11] offset:0\n"
    "s_waitcnt vmcnt(21)\n"
    "v_fma_mix_f32 v12, v110, v132, 0 op_sel_hi:[1,0,0]\n"
    "v_fma_mix_f32 v13, v110, v134, 0 op_sel:[1,0,0] op_sel_hi:[1,0,0]\n"
    "v_cvt_pk_bf16_f32 v100, v12, v13\n"
    "v_lshlrev_b32 v12, 16, v100\n"
    "v_and_b32 v13, 0xffff0000, v100\n"
    "v_add_f32 v12, v12, v136\n"
    "v_add_f32 v13, v13, v138\n"
    "v_cvt_pk_bf16_f32 v100, v12, v13\n"
    "v_lshlrev_b32 v12, 16, v100\n"
    "v_and_b32 v13, 0xffff0000, v100\n"
    "ds_write_b64 v118, v[12:13] offset:2304\n"
    "s_waitcnt vmcnt(20)\n"
    "v_fma_mix_f32 v14, v111, v140, 0 op_sel_hi:[1,0,0]\n"
    "v_fma_mix_f32 v15, v111, v142, 0 op_sel:[1,0,0] op_sel_hi:[1,0,0]\n"
    "v_cvt_pk_bf16_f32 v100, v14, v15\n"
    "v_lshlrev_b32 v14, 16, v100\n"
    "v_and_b32 v15, 0xffff0000, v100\n"
    "v_add_f32 v14, v14, v144\n"
    "v_add_f32 v15, v15, v146\n"
    "v_cvt_pk_bf16_f32 v100, v14, v15\n"
    "v_lshlrev_b32 v14, 16, v100\n"
    "v_and_b32 v15, 0xffff0000, v100\n"
    "ds_write_b64 v119, v[14:15] offset:2304\n"
    "s_waitcnt vmcnt(19)\n"
    "v_fma_mix_f32 v16, v112, v132, 0 op_sel_hi:[1,0,0]\n"
    "v_fma_mix_f32 v17, v112, v134, 0 op_sel:[1,0,0] op_sel_hi:[1,0,0]\n"
    "v_cvt_pk_bf16_f32 v100, v16, v17\n"
    "v_lshlrev_b32 v16, 16, v100\n"
    "v_and_b32 v17, 0xffff0000, v100\n"
    "v_add_f32 v16, v16, v136\n"
    "v_add_f32 v17, v17, v138\n"
    "v_cvt_pk_bf16_f32 v100, v16, v17\n"
    "v_lshlrev_b32 v16, 16, v100\n"
    "v_and_b32 v17, 0xffff0000, v100\n"
    "ds_write_b64 v118, v[16:17] offset:4608\n"
    "s_waitcnt vmcnt(18)\n"
    "v_fma_mix_f32 v18, v113, v140, 0 op_sel_hi:[1,0,0]\n"
    "v_fma_mix_f32 v19, v113, v142, 0 op_sel:[1,0,0] op_sel_hi:[1,0,0]\n"
    "v_cvt_pk_bf16_f32 v100, v18, v19\n"
    "v_lshlrev_b32 v18, 16, v100\n"
    "v_and_b32 v19, 0xffff0000, v100\n"
    "v_add_f32 v18, v18, v144\n"
    "v_add_f32 v19, v19, v146\n"
    "v_cvt_pk_bf16_f32 v100, v18, v19\n"
    "v_lshlrev_b32 v18, 16, v100\n"
    "v_and_b32 v19, 0xffff0000, v100\n"
    "ds_write_b64 v119, v[18:19] offset:4608\n"
    "s_waitcnt lgkmcnt(0)\n"
    "s_barrier\n"
    "v_lshlrev_b32 v2, 2, v1\n"
    "v_add_u32 v2, 126976, v2\n"
    "ds_read_b32 v8, v2 offset:0\n"
    "ds_read_b32 v9, v2 offset:512\n"
    "ds_read_b32 v10, v2 offset:1024\n"
    "ds_read_b32 v11, v2 offset:1536\n"
    "ds_read_b32 v12, v2 offset:2048\n"
    "ds_read_b32 v13, v2 offset:2304\n"
    "ds_read_b32 v14, v2 offset:2816\n"
    "ds_read_b32 v15, v2 offset:3328\n"
    "ds_read_b32 v16, v2 offset:3840\n"
    "ds_read_b32 v17, v2 offset:4352\n"
    "ds_read_b32 v18, v2 offset:4608\n"
    "ds_read_b32 v19, v2 offset:5120\n"
    "ds_read_b32 v20, v2 offset:5632\n"
    "ds_read_b32 v21, v2 offset:6144\n"
    "ds_read_b32 v22, v2 offset:6656\n"
    "s_cmp_lt_u32 s78, 2\n"
    "s_cselect_b32 s80, -1, 0\n"
    "s_waitcnt lgkmcnt(8)\n"
    "v_and_b32 v12, s80, v12\n"
    "s_waitcnt lgkmcnt(5)\n"
    "v_and_b32 v17, s80, v17\n"
    "s_waitcnt lgkmcnt(0)\n"
    "v_and_b32 v22, s80, v22\n"
    "s_waitcnt vmcnt(17)\n"
    "v_lshlrev_b32 v100, 16, v36\n"
    "v_and_b32 v101, 0xffff0000, v36\n"
    "v_pk_mul_f32 v[132:133], v[100:101], v[8:9] op_sel_hi:[1,0]\n"
    "v_pk_mul_f32 v[140:141], v[100:101], v[12:13] op_sel:[0,1] op_sel_hi:[1,1]\n"
    "v_pk_mul_f32 v[148:149], v[100:101], v[18:19] op_sel_hi:[1,0]\n"
    "global_load_dwordx4 a[64:67], v156, %1 offset:0\n"
    "v_lshlrev_b32 v100, 16, v37\n"
    "v_and_b32 v101, 0xffff0000, v37\n"
    "v_pk_mul_f32 v[134:135], v[100:101], v[8:9] op_sel_hi:[1,0]\n"
    "v_pk_mul_f32 v[142:143], v[100:101], v[12:13] op_sel:[0,1] op_sel_hi:[1,1]\n"
    "v_pk_mul_f32 v[150:151], v[100:101], v[18:19] op_sel_hi:[1,0]\n"
    "v_lshlrev_b32 v100, 16, v38\n"
    "v_and_b32 v101, 0xffff0000, v38\n"
    "v_pk_mul_f32 v[136:137], v[100:101], v[8:9] op_sel_hi:[1,0]\n"
    "v_pk_mul_f32 v[144:145], v[100:101], v[12:13] op_sel:[0,1] op_sel_hi:[1,1]\n"
    "v_pk_mul_f32 v[152:153], v[100:101], v[18:19] op_sel_hi:[1,0]\n"
    "global_load_dwordx4 a[68:71], v156, %1 offset:64\n"
    "v_lshlrev_b32 v100, 16, v39\n"
    "v_and_b32 v101, 0xffff0000, v39\n"
    "v_pk_mul_f32 v[138:139], v[100:101], v[8:9] op_sel_hi:[1,0]\n"
    "v_pk_mul_f32 v[146:147], v[100:101], v[12:13] op_sel:[0,1] op_sel_hi:[1,1]\n"
    "v_pk_mul_f32 v[154:155], v[100:101], v[18:19] op_sel_hi:[1,0]\n"
    "s_waitcnt vmcnt(18)\n"
    "v_lshlrev_b32 v100, 16, v40\n"
    "v_and_b32 v101, 0xffff0000, v40\n"
    "v_pk_fma_f32 v[132:133], v[100:101], v[8:9], v[132:133] op_sel:[0,1,0] op_sel_hi:[1,1,1]\n"
    "v_pk_fma_f32 v[140:141], v[100:101], v[14:15], v[140:141] op_sel_hi:[1,0,1]\n"
    "v_pk_fma_f32 v[148:149], v[100:101], v[18:19], v[148:149] op_sel:[0,1,0] op_sel_hi:[1,1,1]\n"
    "global_load_dwordx4 a[72:75], v156, %1 offset:128\n"
    "v_lshlrev_b32 v100, 16, v41\n"
    "v_and_b32 v101, 0xffff0000, v41\n"
    "v_pk_fma_f32 v[134:135], v[100:101], v[8:9], v[134:135] op_sel:[0,1,0] op_sel_hi:[1,1,1]\n"
    "v_pk_fma_f32 v[142:143], v[100:101], v[14:15], v[142:143] op_sel_hi:[1,0,1]\n"
    "v_pk_fma_f32 v[150:151], v[100:101], v[18:19], v[150:151] op_sel:[0,1,0] op_sel_hi:[1,1,1]\n"
    "v_lshlrev_b32 v100, 16, v42\n"
    "v_and_b32 v101, 0xffff0000, v42\n"
    "v_pk_fma_f32 v[136:137], v[100:101], v[8:9], v[136:137] op_sel:[0,1,0] op_sel_hi:[1,1,1]\n"
    "v_pk_fma_f32 v[144:145], v[100:101], v[14:15], v[144:145] op_sel_hi:[1,0,1]\n"
    "v_pk_fma_f32 v[152:153], v[100:101], v[18:19], v[152:153] op_sel:[0,1,0] op_sel_hi:[1,1,1]\n"
    "global_load_dwordx4 a[76:79], v156, %1 offset:192\n"
    "v_lshlrev_b32 v100, 16, v43\n"
    "v_and_b32 v101, 0xffff0000, v43\n"
    "v_pk_fma_f32 v[138:139], v[100:101], v[8:9], v[138:139] op_sel:[0,1,0] op_sel_hi:[1,1,1]\n"
    "v_pk_fma_f32 v[146:147], v[100:101], v[14:15], v[146:147] op_sel_hi:[1,0,1]\n"
    "v_pk_fma_f32 v[154:155], v[100:101], v[18:19], v[154:155] op_sel:[0,1,0] op_sel_hi:[1,1,1]\n"
    "s_waitcnt vmcnt(19)\n"
    "v_lshlrev_b32 v100, 16, v44\n"
    "v_and_b32 v101, 0xffff0000, v44\n"
    "v_pk_fma_f32 v[132:133], v[100:101], v[10:11], v[132:133] op_sel_hi:[1,0,1]\n"
    "v_pk_fma_f32 v[140:141], v[100:101], v[14:15], v[140:141] op_sel:[0,1,0] op_sel_hi:[1,1,1]\n"
    "v_pk_fma_f32 v[148:149], v[100:101], v[20:21], v[148:149] op_sel_hi:[1,0,1]\n"
    "global_load_dwordx4 a[80:83], v157, %1 offset:0\n"
    "v_lshlrev_b32 v100, 16, v45\n"
    "v_and_b32 v101, 0xffff0000, v45\n"
    "v_pk_fma_f32 v[134:135], v[100:101], v[10:11], v[134:135] op_sel_hi:[1,0,1]\n"
    "v_pk_fma_f32 v[142:143], v[100:101], v[14:15], v[142:143] op_sel:[0,1,0] op_sel_hi:[1,1,1]\n"
    "v_pk_fma_f32 v[150:151], v[100:101], v[20:21], v[150:151] op_sel_hi:[1,0,1]\n"
    "v_lshlrev_b32 v100, 16, v46\n"
    "v_and_b32 v101, 0xffff0000, v46\n"
    "v_pk_fma_f32 v[136:137], v[100:101], v[10:11], v[136:137] op_sel_hi:[1,0,1]\n"
    "v_pk_fma_f32 v[144:145], v[100:101], v[14:15], v[144:145] op_sel:[0,1,0] op_sel_hi:[1,1,1]\n"
    "v_pk_fma_f32 v[152:153], v[100:101], v[20:21], v[152:153] op_sel_hi:[1,0,1]\n"
    "global_load_dwordx4 a[84:87], v157, %1 offset:64\n"
    "v_lshlrev_b32 v100, 16, v47\n"
    "v_and_b32 v101, 0xffff0000, v47\n"
    "v_pk_fma_f32 v[138:139], v[100:101], v[10:11], v[138:139] op_sel_hi:[1,0,1]\n"
    "v_pk_fma_f32 v[146:147], v[100:101], v[14:15], v[146:147] op_sel:[0,1,0] op_sel_hi:[1,1,1]\n"
    "v_pk_fma_f32 v[154:155], v[100:101], v[20:21], v[154:155] op_sel_hi:[1,0,1]\n"
    "s_waitcnt vmcnt(20)\n"
    "v_lshlrev_b32 v100, 16, v48\n"
    "v_and_b32 v101, 0xffff0000, v48\n"
    "v_pk_fma_f32 v[132:133], v[100:101], v[10:11], v[132:133] op_sel:[0,1,0] op_sel_hi:[1,1,1]\n"
    "v_pk_fma_f32 v[140:141], v[100:101], v[16:17], v[140:141] op_sel_hi:[1,0,1]\n"
    "v_pk_fma_f32 v[148:149], v[100:101], v[20:21], v[148:149] op_sel:[0,1,0] op_sel_hi:[1,1,1]\n"
    "global_load_dwordx4 a[88:91], v157, %1 offset:128\n"
    "v_lshlrev_b32 v100, 16, v49\n"
    "v_and_b32 v101, 0xffff0000, v49\n"
    "v_pk_fma_f32 v[134:135], v[100:101], v[10:11], v[134:135] op_sel:[0,1,0] op_sel_hi:[1,1,1]\n"
    "v_pk_fma_f32 v[142:143], v[100:101], v[16:17], v[142:143] op_sel_hi:[1,0,1]\n"
    "v_pk_fma_f32 v[150:151], v[100:101], v[20:21], v[150:151] op_sel:[0,1,0] op_sel_hi:[1,1,1]\n"
    "v_lshlrev_b32 v100, 16, v50\n"
    "v_and_b32 v101, 0xffff0000, v50\n"
    "v_pk_fma_f32 v[136:137], v[100:101], v[10:11], v[136:137] op_sel:[0,1,0] op_sel_hi:[1,1,1]\n"
    "v_pk_fma_f32 v[144:145], v[100:101], v[16:17], v[144:145] op_sel_hi:[1,0,1]\n"
    "v_pk_fma_f32 v[152:153], v[100:101], v[20:21], v[152:153] op_sel:[0,1,0] op_sel_hi:[1,1,1]\n"
    "global_load_dwordx4 a[92:95], v157, %1 offset:192\n"
    "v_lshlrev_b32 v100, 16, v51\n"
    "v_and_b32 v101, 0xffff0000, v51\n"
    "v_pk_fma_f32 v[138:139], v[100:101], v[10:11], v[138:139] op_sel:[0,1,0] op_sel_hi:[1,1,1]\n"
    "v_pk_fma_f32 v[146:147], v[100:101], v[16:17], v[146:147] op_sel_hi:[1,0,1]\n"
    "v_pk_fma_f32 v[154:155], v[100:101], v[20:21], v[154:155] op_sel:[0,1,0] op_sel_hi:[1,1,1]\n"
    "s_waitcnt vmcnt(21)\n"
    "v_lshlrev_b32 v100, 16, v52\n"
    "v_and_b32 v101, 0xffff0000, v52\n"
    "v_pk_fma_f32 v[132:133], v[100:101], v[12:13], v[132:133] op_sel_hi:[1,0,1]\n"
    "v_pk_fma_f32 v[140:141], v[100:101], v[16:17], v[140:141] op_sel:[0,1,0] op_sel_hi:[1,1,1]\n"
    "v_pk_fma_f32 v[148:149], v[100:101], v[22:23], v[148:149] op_sel_hi:[1,0,1]\n"
    "v_lshlrev_b32 v100, 16, v53\n"
    "v_and_b32 v101, 0xffff0000, v53\n"
    "v_pk_fma_f32 v[134:135], v[100:101], v[12:13], v[134:135] op_sel_hi:[1,0,1]\n"
    "v_pk_fma_f32 v[142:143], v[100:101], v[16:17], v[142:143] op_sel:[0,1,0] op_sel_hi:[1,1,1]\n"
    "v_pk_fma_f32 v[150:151], v[100:101], v[22:23], v[150:151] op_sel_hi:[1,0,1]\n"
    "v_lshlrev_b32 v100, 16, v54\n"
    "v_and_b32 v101, 0xffff0000, v54\n"
    "v_pk_fma_f32 v[136:137], v[100:101], v[12:13], v[136:137] op_sel_hi:[1,0,1]\n"
    "v_pk_fma_f32 v[144:145], v[100:101], v[16:17], v[144:145] op_sel:[0,1,0] op_sel_hi:[1,1,1]\n"
    "v_pk_fma_f32 v[152:153], v[100:101], v[22:23], v[152:153] op_sel_hi:[1,0,1]\n"
    "v_lshlrev_b32 v100, 16, v55\n"
    "v_and_b32 v101, 0xffff0000, v55\n"
    "v_pk_fma_f32 v[138:139], v[100:101], v[12:13], v[138:139] op_sel_hi:[1,0,1]\n"
    "v_pk_fma_f32 v[146:147], v[100:101], v[16:17], v[146:147] op_sel:[0,1,0] op_sel_hi:[1,1,1]\n"
    "v_pk_fma_f32 v[154:155], v[100:101], v[22:23], v[154:155] op_sel_hi:[1,0,1]\n"
    "v_and_b32 v2, 1, v239\n"
    "v_lshlrev_b32 v2, 3, v2\n"
    "v_lshrrev_b32 v3, 4, v239\n"
    "v_lshl_add_u32 v2, v3, 1, v2\n"
    "s_lshl_b32 s80, s78, 2\n"
    "s_add_u32 s80, s80, 122880\n"
    "v_lshl_add_u32 v2, v2, 4, s80\n"
    "v_permlane32_swap_b32 v132, v136\n"
    "v_permlane32_swap_b32 v133, v137\n"
    "v_permlane32_swap_b32 v134, v138\n"
    "v_permlane32_swap_b32 v135, v139\n"
    "s_nop 1\n"
    "v_add_f32 v132, v132, v136\n"
    "v_add_f32 v133, v133, v137\n"
    "v_add_f32 v134, v134, v138\n"
    "v_add_f32 v135, v135, v139\n"
    "s_nop 0\n"
    "v_permlane16_swap_b32 v132, v134\n"
    "v_permlane16_swap_b32 v133, v135\n"
    "s_nop 1\n"
    "v_add_f32 v132, v132, v134\n"
    "v_add_f32 v133, v133, v135\n"
    "s_nop 0\n"
    "v_add_f32_dpp v132, v132, v132 quad_perm:[2,3,0,1] row_mask:0xf bank_mask:0xf\n"
    "v_add_f32_dpp v133, v133, v133 quad_perm:[2,3,0,1] row_mask:0xf bank_mask:0xf\n"
    "s_nop 0\n"
    "v_add_f32_dpp v132, v132, v132 row_ror:4 row_mask:0xf bank_mask:0xf\n"
    "v_add_f32_dpp v133, v133, v133 row_ror:4 row_mask:0xf bank_mask:0xf\n"
    "s_nop 0\n"
    "v_add_f32_dpp v132, v132, v132 row_ror:8 row_mask:0xf bank_mask:0xf\n"
    "v_add_f32_dpp v133, v133, v133 row_ror:8 row_mask:0xf bank_mask:0xf\n"
    "ds_write2_b32 v2, v132, v133 offset0:0 offset1:4\n"
    "v_permlane32_swap_b32 v140, v144\n"
    "v_permlane32_swap_b32 v141, v145\n"
    "v_permlane32_swap_b32 v142, v146\n"
    "v_permlane32_swap_b32 v143, v147\n"
    "s_nop 1\n"
    "v_add_f32 v140, v140, v144\n"
    "v_add_f32 v141, v141, v145\n"
    "v_add_f32 v142, v142, v146\n"
    "v_add_f32 v143, v143, v147\n"
    "s_nop 0\n"
    "v_permlane16_swap_b32 v140, v142\n"
    "v_permlane16_swap_b32 v141, v143\n"
    "s_nop 1\n"
    "v_add_f32 v140, v140, v142\n"
    "v_add_f32 v141, v141, v143\n"
    "s_nop 0\n"
    "v_add_f32_dpp v140, v140, v140 quad_perm:[2,3,0,1] row_mask:0xf bank_mask:0xf\n"
    "v_add_f32_dpp v141, v141, v141 quad_perm:[2,3,0,1] row_mask:0xf bank_mask:0xf\n"
    "s_nop 0\n"
    "v_add_f32_dpp v140, v140, v140 row_ror:4 row_mask:0xf bank_mask:0xf\n"
    "v_add_f32_dpp v141, v141, v141 row_ror:4 row_mask:0xf bank_mask:0xf\n"
    "s_nop 0\n"
    "v_add_f32_dpp v140, v140, v140 row_ror:8 row_mask:0xf bank_mask:0xf\n"
    "v_add_f32_dpp v141, v141, v141 row_ror:8 row_mask:0xf bank_mask:0xf\n"
    "ds_write2_b32 v2, v140, v141 offset0:64 offset1:68\n"
    "v_permlane32_swap_b32 v148, v152\n"
    "v_permlane32_swap_b32 v149, v153\n"
    "v_permlane32_swap_b32 v150, v154\n"
    "v_permlane32_swap_b32 v151, v155\n"
    "s_nop 1\n"
    "v_add_f32 v148, v148, v152\n"
    "v_add_f32 v149, v149, v153\n"
    "v_add_f32 v150, v150, v154\n"
    "v_add_f32 v151, v151, v155\n"
    "s_nop 0\n"
    "v_permlane16_swap_b32 v148, v150\n"
    "v_permlane16_swap_b32 v149, v151\n"
    "s_nop 1\n"
    "v_add_f32 v148, v148, v150\n"
    "v_add_f32 v149, v149, v151\n"
    "s_nop 0\n"
    "v_add_f32_dpp v148, v148, v148 quad_perm:[2,3,0,1] row_mask:0xf bank_mask:0xf\n"
    "v_add_f32_dpp v149, v149, v149 quad_perm:[2,3,0,1] row_mask:0xf bank_mask:0xf\n"
    "s_nop 0\n"
    "v_add_f32_dpp v148, v148, v148 row_ror:4 row_mask:0xf bank_mask:0xf\n"
    "v_add_f32_dpp v149, v149, v149 row_ror:4 row_mask:0xf bank_mask:0xf\n"
    "s_nop 0\n"
    "v_add_f32_dpp v148, v148, v148 row_ror:8 row_mask:0xf bank_mask:0xf\n"
    "v_add_f32_dpp v149, v149, v149 row_ror:8 row_mask:0xf bank_mask:0xf\n"
    "ds_write2_b32 v2, v148, v149 offset0:128 offset1:132\n"
    "v_lshlrev_b32 v3, 5, v5\n"
    "v_add_u32 v3, 0, v3\n"
    "v_mov_b32 v102, 0\n"
    "v_mov_b32 v103, 0\n"
    "v_mov_b32 v104, 0\n"
    "v_mov_b32 v105, 0\n"
    "v_mov_b32 v106, 0\n"
    "v_mov_b32 v107, 0\n"
    "ds_read_b128 v[8:11], v3 offset:0\n"
    "ds_read_b128 v[16:19], v3 offset:4608\n"
    "ds_read_b128 v[12:15], v3 offset:16\n"
    "ds_read_b128 v[20:23], v3 offset:4624\n"
    "s_waitcnt vmcnt(20)\n"
    "v_lshlrev_b32 v24, 16, v56\n"
    "v_and_b32 v25, 0xffff0000, v56\n"
    "s_waitcnt vmcnt(19)\n"
    "v_lshlrev_b32 v26, 16, v76\n"
    "v_and_b32 v27, 0xffff0000, v76\n"
    "s_waitcnt lgkmcnt(3)\n"
    "v_pk_mul_f32 v[24:25], v[24:25], v[8:9]\n"
    "s_waitcnt lgkmcnt(2)\n"
    "v_pk_mul_f32 v[26:27], v[26:27], v[16:17]\n"
    "v_pk_fma_f32 v[102:103], v[24:25], v[26:27], v[102:103]\n"
    "v_pk_add_f32 v[104:105], v[104:105], v[24:25]\n"
    "v_pk_add_f32 v[106:107], v[106:107], v[26:27]\n"
    "v_lshlrev_b32 v24, 16, v57\n"
    "v_and_b32 v25, 0xffff0000, v57\n"
    "v_lshlrev_b32 v26, 16, v77\n"
    "v_and_b32 v27, 0xffff0000, v77\n"
    "v_pk_mul_f32 v[24:25], v[24:25], v[10:11]\n"
    "v_pk_mul_f32 v[26:27], v[26:27], v[18:19]\n"
    "v_pk_fma_f32 v[102:103], v[24:25], v[26:27], v[102:103]\n"
    "v_pk_add_f32 v[104:105], v[104:105], v[24:25]\n"
    "v_pk_add_f32 v[106:107], v[106:107], v[26:27]\n"
    "v_lshlrev_b32 v24, 16, v58\n"
    "v_and_b32 v25, 0xffff0000, v58\n"
    "v_lshlrev_b32 v26, 16, v78\n"
    "v_and_b32 v27, 0xffff0000, v78\n"
    "s_waitcnt lgkmcnt(0)\n"
    "v_pk_mul_f32 v[24:25], v[24:25], v[12:13]\n"
    "v_pk_mul_f32 v[26:27], v[26:27], v[20:21]\n"
    "v_pk_fma_f32 v[102:103], v[24:25], v[26:27], v[102:103]\n"
    "v_pk_add_f32 v[104:105], v[104:105], v[24:25]\n"
    "v_pk_add_f32 v[106:107], v[106:107], v[26:27]\n"
    "v_lshlrev_b32 v24, 16, v59\n"
    "v_and_b32 v25, 0xffff0000, v59\n"
    "v_lshlrev_b32 v26, 16, v79\n"
    "v_and_b32 v27, 0xffff0000, v79\n"
    "v_pk_mul_f32 v[24:25], v[24:25], v[14:15]\n"
    "v_pk_mul_f32 v[26:27], v[26:27], v[22:23]\n"
    "v_pk_fma_f32 v[102:103], v[24:25], v[26:27], v[102:103]\n"
    "v_pk_add_f32 v[104:105], v[104:105], v[24:25]\n"
    "v_pk_add_f32 v[106:107], v[106:107], v[26:27]\n"
    "ds_read_b128 v[8:11], v3 offset:512\n"
    "ds_read_b128 v[16:19], v3 offset:5120\n"
    "ds_read_b128 v[12:15], v3 offset:528\n"
    "ds_read_b128 v[20:23], v3 offset:5136\n"
    "s_waitcnt vmcnt(18)\n"
    "v_lshlrev_b32 v24, 16, v60\n"
    "v_and_b32 v25, 0xffff0000, v60\n"
    "s_waitcnt vmcnt(17)\n"
    "v_lshlrev_b32 v26, 16, v80\n"
    "v_and_b32 v27, 0xffff0000, v80\n"
    "s_waitcnt lgkmcnt(3)\n"
    "v_pk_mul_f32 v[24:25], v[24:25], v[8:9]\n"
    "s_waitcnt lgkmcnt(2)\n"
    "v_pk_mul_f32 v[26:27], v[26:27], v[16:17]\n"
    "v_pk_fma_f32 v[102:103], v[24:25], v[26:27], v[102:103]\n"
    "v_pk_add_f32 v[104:105], v[104:105], v[24:25]\n"
    "v_pk_add_f32 v[106:107], v[106:107], v[26:27]\n"
    "v_lshlrev_b32 v24, 16, v61\n"
    "v_and_b32 v25, 0xffff0000, v61\n"
    "v_lshlrev_b32 v26, 16, v81\n"
    "v_and_b32 v27, 0xffff0000, v81\n"
    "v_pk_mul_f32 v[24:25], v[24:25], v[10:11]\n"
    "v_pk_mul_f32 v[26:27], v[26:27], v[18:19]\n"
    "v_pk_fma_f32 v[102:103], v[24:25], v[26:27], v[102:103]\n"
    "v_pk_add_f32 v[104:105], v[104:105], v[24:25]\n"
    "v_pk_add_f32 v[106:107], v[106:107], v[26:27]\n"
    "v_lshlrev_b32 v24, 16, v62\n"
    "v_and_b32 v25, 0xffff0000, v62\n"
    "v_lshlrev_b32 v26, 16, v82\n"
    "v_and_b32 v27, 0xffff0000, v82\n"
    "s_waitcnt lgkmcnt(0)\n"
    "v_pk_mul_f32 v[24:25], v[24:25], v[12:13]\n"
    "v_pk_mul_f32 v[26:27], v[26:27], v[20:21]\n"
    "v_pk_fma_f32 v[102:103], v[24:25], v[26:27], v[102:103]\n"
    "v_pk_add_f32 v[104:105], v[104:105], v[24:25]\n"
    "v_pk_add_f32 v[106:107], v[106:107], v[26:27]\n"
    "v_lshlrev_b32 v24, 16, v63\n"
    "v_and_b32 v25, 0xffff0000, v63\n"
    "v_lshlrev_b32 v26, 16, v83\n"
    "v_and_b32 v27, 0xffff0000, v83\n"
    "v_pk_mul_f32 v[24:25], v[24:25], v[14:15]\n"
    "v_pk_mul_f32 v[26:27], v[26:27], v[22:23]\n"
    "v_pk_fma_f32 v[102:103], v[24:25], v[26:27], v[102:103]\n"
    "v_pk_add_f32 v[104:105], v[104:105], v[24:25]\n"
    "v_pk_add_f32 v[106:107], v[106:107], v[26:27]\n"
    "ds_read_b128 v[8:11], v3 offset:1024\n"
    "ds_read_b128 v[16:19], v3 offset:5632\n"
    "ds_read_b128 v[12:15], v3 offset:1040\n"
    "ds_read_b128 v[20:23], v3 offset:5648\n"
    "s_waitcnt vmcnt(16)\n"
    "v_lshlrev_b32 v24, 16, v64\n"
    "v_and_b32 v25, 0xffff0000, v64\n"
    "s_waitcnt vmcnt(15)\n"
    "v_lshlrev_b32 v26, 16, v84\n"
    "v_and_b32 v27, 0xffff0000, v84\n"
    "s_waitcnt lgkmcnt(3)\n"
    "v_pk_mul_f32 v[24:25], v[24:25], v[8:9]\n"
    "s_waitcnt lgkmcnt(2)\n"
    "v_pk_mul_f32 v[26:27], v[26:27], v[16:17]\n"
    "v_pk_fma_f32 v[102:103], v[24:25], v[26:27], v[102:103]\n"
    "v_pk_add_f32 v[104:105], v[104:105], v[24:25]\n"
    "v_pk_add_f32 v[106:107], v[106:107], v[26:27]\n"
    "v_lshlrev_b32 v24, 16, v65\n"
    "v_and_b32 v25, 0xffff0000, v65\n"
    "v_lshlrev_b32 v26, 16, v85\n"
    "v_and_b32 v27, 0xffff0000, v85\n"
    "v_pk_mul_f32 v[24:25], v[24:25], v[10:11]\n"
    "v_pk_mul_f32 v[26:27], v[26:27], v[18:19]\n"
    "v_pk_fma_f32 v[102:103], v[24:25], v[26:27], v[102:103]\n"
    "v_pk_add_f32 v[104:105], v[104:105], v[24:25]\n"
    "v_pk_add_f32 v[106:107], v[106:107], v[26:27]\n"
    "v_lshlrev_b32 v24, 16, v66\n"
    "v_and_b32 v25, 0xffff0000, v66\n"
    "v_lshlrev_b32 v26, 16, v86\n"
    "v_and_b32 v27, 0xffff0000, v86\n"
    "s_waitcnt lgkmcnt(0)\n"
    "v_pk_mul_f32 v[24:25], v[24:25], v[12:13]\n"
    "v_pk_mul_f32 v[26:27], v[26:27], v[20:21]\n"
    "v_pk_fma_f32 v[102:103], v[24:25], v[26:27], v[102:103]\n"
    "v_pk_add_f32 v[104:105], v[104:105], v[24:25]\n"
    "v_pk_add_f32 v[106:107], v[106:107], v[26:27]\n"
    "v_lshlrev_b32 v24, 16, v67\n"
    "v_and_b32 v25, 0xffff0000, v67\n"
    "v_lshlrev_b32 v26, 16, v87\n"
    "v_and_b32 v27, 0xffff0000, v87\n"
    "v_pk_mul_f32 v[24:25], v[24:25], v[14:15]\n"
    "v_pk_mul_f32 v[26:27], v[26:27], v[22:23]\n"
    "v_pk_fma_f32 v[102:103], v[24:25], v[26:27], v[102:103]\n"
    "v_pk_add_f32 v[104:105], v[104:105], v[24:25]\n"
    "v_pk_add_f32 v[106:107], v[106:107], v[26:27]\n"
    "ds_read_b128 v[8:11], v3 offset:1536\n"
    "ds_read_b128 v[16:19], v3 offset:6144\n"
    "ds_read_b128 v[12:15], v3 offset:1552\n"
    "ds_read_b128 v[20:23], v3 offset:6160\n"
    "s_waitcnt vmcnt(14)\n"
    "v_lshlrev_b32 v24, 16, v68\n"
    "v_and_b32 v25, 0xffff0000, v68\n"
    "s_waitcnt vmcnt(13)\n"
    "v_lshlrev_b32 v26, 16, v88\n"
    "v_and_b32 v27, 0xffff0000, v88\n"
    "s_waitcnt lgkmcnt(3)\n"
    "v_pk_mul_f32 v[24:25], v[24:25], v[8:9]\n"
    "s_waitcnt lgkmcnt(2)\n"
    "v_pk_mul_f32 v[26:27], v[26:27], v[16:17]\n"
    "v_pk_fma_f32 v[102:103], v[24:25], v[26:27], v[102:103]\n"
    "v_pk_add_f32 v[104:105], v[104:105], v[24:25]\n"
    "v_pk_add_f32 v[106:107], v[106:107], v[26:27]\n"
    "v_lshlrev_b32 v24, 16, v69\n"
    "v_and_b32 v25, 0xffff0000, v69\n"
    "v_lshlrev_b32 v26, 16, v89\n"
    "v_and_b32 v27, 0xffff0000, v89\n"
    "v_pk_mul_f32 v[24:25], v[24:25], v[10:11]\n"
    "v_pk_mul_f32 v[26:27], v[26:27], v[18:19]\n"
    "v_pk_fma_f32 v[102:103], v[24:25], v[26:27], v[102:103]\n"
    "v_pk_add_f32 v[104:105], v[104:105], v[24:25]\n"
    "v_pk_add_f32 v[106:107], v[106:107], v[26:27]\n"
    "v_lshlrev_b32 v24, 16, v70\n"
    "v_and_b32 v25, 0xffff0000, v70\n"
    "v_lshlrev_b32 v26, 16, v90\n"
    "v_and_b32 v27, 0xffff0000, v90\n"
    "s_waitcnt lgkmcnt(0)\n"
    "v_pk_mul_f32 v[24:25], v[24:25], v[12:13]\n"
    "v_pk_mul_f32 v[26:27], v[26:27], v[20:21]\n"
    "v_pk_fma_f32 v[102:103], v[24:25], v[26:27], v[102:103]\n"
    "v_pk_add_f32 v[104:105], v[104:105], v[24:25]\n"
    "v_pk_add_f32 v[106:107], v[106:107], v[26:27]\n"
    "v_lshlrev_b32 v24, 16, v71\n"
    "v_and_b32 v25, 0xffff0000, v71\n"
    "v_lshlrev_b32 v26, 16, v91\n"
    "v_and_b32 v27, 0xffff0000, v91\n"
    "v_pk_mul_f32 v[24:25], v[24:25], v[14:15]\n"
    "v_pk_mul_f32 v[26:27], v[26:27], v[22:23]\n"
    "v_pk_fma_f32 v[102:103], v[24:25], v[26:27], v[102:103]\n"
    "v_pk_add_f32 v[104:105], v[104:105], v[24:25]\n"
    "v_pk_add_f32 v[106:107], v[106:107], v[26:27]\n"
    "ds_read_b128 v[8:11], v3 offset:2048\n"
    "ds_read_b128 v[16:19], v3 offset:6656\n"
    "ds_read_b128 v[12:15], v3 offset:2064\n"
    "ds_read_b128 v[20:23], v3 offset:6672\n"
    "s_waitcnt vmcnt(12)\n"
    "v_lshlrev_b32 v24, 16, v72\n"
    "v_and_b32 v25, 0xffff0000, v72\n"
    "s_waitcnt vmcnt(11)\n"
    "v_lshlrev_b32 v26, 16, v92\n"
    "v_and_b32 v27, 0xffff0000, v92\n"
    "s_waitcnt lgkmcnt(3)\n"
    "v_pk_mul_f32 v[24:25], v[24:25], v[8:9]\n"
    "s_waitcnt lgkmcnt(2)\n"
    "v_pk_mul_f32 v[26:27], v[26:27], v[16:17]\n"
    "v_pk_fma_f32 v[102:103], v[24:25], v[26:27], v[102:103]\n"
    "v_pk_add_f32 v[104:105], v[104:105], v[24:25]\n"
    "v_pk_add_f32 v[106:107], v[106:107], v[26:27]\n"
    "v_lshlrev_b32 v24, 16, v73\n"
    "v_and_b32 v25, 0xffff0000, v73\n"
    "v_lshlrev_b32 v26, 16, v93\n"
    "v_and_b32 v27, 0xffff0000, v93\n"
    "v_pk_mul_f32 v[24:25], v[24:25], v[10:11]\n"
    "v_pk_mul_f32 v[26:27], v[26:27], v[18:19]\n"
    "v_pk_fma_f32 v[102:103], v[24:25], v[26:27], v[102:103]\n"
    "v_pk_add_f32 v[104:105], v[104:105], v[24:25]\n"
    "v_pk_add_f32 v[106:107], v[106:107], v[26:27]\n"
    "v_lshlrev_b32 v24, 16, v74\n"
    "v_and_b32 v25, 0xffff0000, v74\n"
    "v_lshlrev_b32 v26, 16, v94\n"
    "v_and_b32 v27, 0xffff0000, v94\n"
    "s_waitcnt lgkmcnt(0)\n"
    "v_pk_mul_f32 v[24:25], v[24:25], v[12:13]\n"
    "v_pk_mul_f32 v[26:27], v[26:27], v[20:21]\n"
    "v_pk_fma_f32 v[102:103], v[24:25], v[26:27], v[102:103]\n"
    "v_pk_add_f32 v[104:105], v[104:105], v[24:25]\n"
    "v_pk_add_f32 v[106:107], v[106:107], v[26:27]\n"
    "v_lshlrev_b32 v24, 16, v75\n"
    "v_and_b32 v25, 0xffff0000, v75\n"
    "v_lshlrev_b32 v26, 16, v95\n"
    "v_and_b32 v27, 0xffff0000, v95\n"
    "v_pk_mul_f32 v[24:25], v[24:25], v[14:15]\n"
    "v_pk_mul_f32 v[26:27], v[26:27], v[22:23]\n"
    "v_pk_fma_f32 v[102:103], v[24:25], v[26:27], v[102:103]\n"
    "v_pk_add_f32 v[104:105], v[104:105], v[24:25]\n"
    "v_pk_add_f32 v[106:107], v[106:107], v[26:27]\n"
    "v_add_f32 v102, v102, v103\n"
    "v_add_f32 v104, v104, v105\n"
    "v_add_f32 v106, v106, v107\n"
    "v_add_f32_dpp v102, v102, v102 row_ror:1 row_mask:0xf bank_mask:0xf\n"
    "v_add_f32_dpp v104, v104, v104 row_ror:1 row_mask:0xf bank_mask:0xf\n"
    "v_add_f32_dpp v106, v106, v106 row_ror:1 row_mask:0xf bank_mask:0xf\n"
    "v_add_f32_dpp v102, v102, v102 row_ror:2 row_mask:0xf bank_mask:0xf\n"
    "v_add_f32_dpp v104, v104, v104 row_ror:2 row_mask:0xf bank_mask:0xf\n"
    "v_add_f32_dpp v106, v106, v106 row_ror:2 row_mask:0xf bank_mask:0xf\n"
    "v_add_f32_dpp v102, v102, v102 row_ror:4 row_mask:0xf bank_mask:0xf\n"
    "v_add_f32_dpp v104, v104, v104 row_ror:4 row_mask:0xf bank_mask:0xf\n"
    "v_add_f32_dpp v106, v106, v106 row_ror:4 row_mask:0xf bank_mask:0xf\n"
    "v_add_f32_dpp v102, v102, v102 row_ror:8 row_mask:0xf bank_mask:0xf\n"
    "v_add_f32_dpp v104, v104, v104 row_ror:8 row_mask:0xf bank_mask:0xf\n"
    "v_add_f32_dpp v106, v106, v106 row_ror:8 row_mask:0xf bank_mask:0xf\n"
    "v_mul_f32 v106, 0xbae38e39, v106\n"
    "v_fmac_f32 v102, v104, v106\n"
    "v_mov_b32 v8, v102\n"
    "v_cvt_pk_bf16_f32 v8, v8, v8\n"
    "v_and_b32 v8, 0xffff0000, v8\n"
    "v_sub_f32 v12, v102, v8\n"
    "v_mov_b32 v9, 0\n"
    "v_mov_b32 v10, 0\n"
    "v_mov_b32 v11, 0\n"
    "v_mov_b32 v13, 0\n"
    "v_mov_b32 v14, 0\n"
    "v_mov_b32 v15, 0\n"
    "v_lshlrev_b32 v3, 4, v4\n"
    "v_add_u32 v3, 122880, v3\n"
    "ds_write_b128 v3, v[8:11] offset:768\n"
    "ds_write_b128 v3, v[12:15] offset:1024\n"
    "s_waitcnt vmcnt(10)\n"
    "v_lshlrev_b32 v96, 16, v96\n"
    "v_mul_f32 v96, v96, v102\n"
    "s_waitcnt vmcnt(9)\n"
    "v_lshlrev_b32 v97, 16, v97\n"
    "v_mul_f32 v97, v97, v102\n"
    "s_waitcnt vmcnt(8)\n"
    "v_lshlrev_b32 v98, 16, v98\n"
    "v_mul_f32 v98, v98, v102\n"
    "v_lshlrev_b32 v3, 2, v4\n"
    "v_add_u32 v3, 122880, v3\n"
    "ds_write_b32 v3, v96 offset:1280\n"
    "ds_write_b32 v3, v97 offset:1344\n"
    "ds_write_b32 v3, v98 offset:1408\n"
    "s_waitcnt lgkmcnt(0)\n"
    "s_barrier\n"
    "v_min_u32 v2, 39, v0\n"
    "v_lshlrev_b32 v3, 5, v2\n"
    "v_add_u32 v3, 122880, v3\n"
    "ds_read_b128 v[8:11], v3\n"
    "ds_read_b128 v[12:15], v3 offset:16\n"
    "v_lshrrev_b32 v3, 3, v2\n"
    "v_lshlrev_b32 v3, 9, v3\n"
    "v_and_b32 v2, 7, v2\n"
    "v_lshl_add_u32 v3, v2, 2, v3\n"
    "v_cmp_gt_u32 vcc, 40, v0\n"
    "v_mov_b32 v2, 2147483632\n"
    "s_nop 0\n"
    "v_cndmask_b32 v3, v2, v3, vcc\n"
    "s_mul_i32 s82, s88, 2560\n"
    "s_add_u32 s82, s82, 7340032\n"
    "s_lshl_b32 s80, s89, 5\n"
    "s_add_u32 s80, s80, s82\n"
    "s_waitcnt lgkmcnt(0)\n"
    "v_pk_add_f32 v[8:9], v[8:9], v[10:11]\n"
    "v_pk_add_f32 v[12:13], v[12:13], v[14:15]\n"
    "v_add_f32 v8, v8, v9\n"
    "v_add_f32 v12, v12, v13\n"
    "v_cvt_pk_bf16_f32 v8, v8, v12\n"
    "v_min_u32 v8, -2, v8\n"
    "buffer_store_dword v8, v3, s[56:59], s80 offen sc1\n"
    "global_load_dwordx4 a[96:99], v156, %2 offset:0\n"
    "global_load_dwordx4 a[100:103], v156, %2 offset:64\n"
    "global_load_dwordx4 a[104:107], v156, %2 offset:128\n"
    "global_load_dwordx4 a[108:111], v156, %2 offset:192\n"
    "global_load_dwordx4 a[112:115], v157, %2 offset:0\n"
    "global_load_dwordx4 a[116:119], v157, %2 offset:64\n"
    "global_load_dwordx4 a[120:123], v157, %2 offset:128\n"
    "global_load_dwordx4 a[124:127], v157, %2 offset:192\n"
    "v_min_u32 v2, 5, v5\n"
    "v_cmp_lt_u32 vcc, 2, v2\n"
    "s_nop 1\n"
    "v_cndmask_b32 v3, 0, 1, vcc\n"
    "v_mul_u32_u24 v6, 3, v3\n"
    "v_sub_u32 v6, v2, v6\n"
    "v_and_b32 v7, 48, v239\n"
    "v_lshl_add_u32 v7, v3, 8, v7\n"
    "v_lshl_add_u32 v6, v6, 9, v7\n"
    "s_mov_b32 s87, 0\n"
    "RQP%=:\n"
    "buffer_load_dwordx4 v[8:11], v6, s[56:59], s82 offen offset:0 sc1\n"
    "buffer_load_dwordx4 v[24:27], v7, s[56:59], s82 offen offset:1536 sc1\n"
    "buffer_load_dwordx4 v[40:43], v7, s[56:59], s82 offen offset:2048 sc1\n"
    "buffer_load_dwordx4 v[12:15], v6, s[56:59], s82 offen offset:64 sc1\n"
    "buffer_load_dwordx4 v[28:31], v7, s[56:59], s82 offen offset:1600 sc1\n"
    "buffer_load_dwordx4 v[44:47], v7, s[56:59], s82 offen offset:2112 sc1\n"
    "buffer_load_dwordx4 v[16:19], v6, s[56:59], s82 offen offset:128 sc1\n"
    "buffer_load_dwordx4 v[32:35], v7, s[56:59], s82 offen offset:1664 sc1\n"
    "buffer_load_dwordx4 v[48:51], v7, s[56:59], s82 offen offset:2176 sc1\n"
    "buffer_load_dwordx4 v[20:23], v6, s[56:59], s82 offen offset:192 sc1\n"
    "buffer_load_dwordx4 v[36:39], v7, s[56:59], s82 offen offset:1728 sc1\n"
    "buffer_load_dwordx4 v[52:55], v7, s[56:59], s82 offen offset:2240 sc1\n"
    "s_waitcnt vmcnt(0)\n"
    "v_max_u32 v56, v8, v9\n"
    "v_max3_u32 v56, v56, v10, v11\n"
    "v_max3_u32 v56, v56, v12, v13\n"
    "v_max3_u32 v56, v56, v14, v15\n"
    "v_max3_u32 v56, v56, v16, v17\n"
    "v_max3_u32 v56, v56, v18, v19\n"
    "v_max3_u32 v56, v56, v20, v21\n"
    "v_max3_u32 v56, v56, v22, v23\n"
    "v_max3_u32 v56, v56, v24, v25\n"
    "v_max3_u32 v56, v56, v26, v27\n"
    "v_max3_u32 v56, v56, v28, v29\n"
    "v_max3_u32 v56, v56, v30, v31\n"
    "v_max3_u32 v56, v56, v32, v33\n"
    "v_max3_u32 v56, v56, v34, v35\n"
    "v_max3_u32 v56, v56, v36, v37\n"
    "v_max3_u32 v56, v56, v38, v39\n"
    "v_max3_u32 v56, v56, v40, v41\n"
    "v_max3_u32 v56, v56, v42, v43\n"
    "v_max3_u32 v56, v56, v44, v45\n"
    "v_max3_u32 v56, v56, v46, v47\n"
    "v_max3_u32 v56, v56, v48, v49\n"
    "v_max3_u32 v56, v56, v50, v51\n"
    "v_max3_u32 v56, v56, v52, v53\n"
    "v_max3_u32 v56, v56, v54, v55\n"
    "v_cmp_eq_u32 vcc, -1, v56\n"
    "s_nop 1\n"
    "s_cmp_eq_u64 vcc, 0\n"
    "s_cbranch_scc1 RQD%=\n"
    "s_add_u32 s87, s87, 1\n"
    "s_cmp_gt_u32 s87, 262144\n"
    "s_cbranch_scc1 RQD%=\n"
    "s_sleep 1\n"
    "s_branch RQP%=\n"
    "RQD%=:\n"
    "v_mfma_f32_16x16x32_bf16 v[60:63], a[64:67], v[8:11], 0\n"
    "v_mfma_f32_16x16x32_bf16 v[60:63], a[68:71], v[12:15], v[60:63]\n"
    "v_mfma_f32_16x16x32_bf16 v[60:63], a[72:75], v[16:19], v[60:63]\n"
    "v_mfma_f32_16x16x32_bf16 v[60:63], a[76:79], v[20:23], v[60:63]\n"
    "v_mfma_f32_16x16x32_bf16 v[64:67], a[80:83], v[8:11], 0\n"
    "v_mfma_f32_16x16x32_bf16 v[64:67], a[84:87], v[12:15], v[64:67]\n"
    "v_mfma_f32_16x16x32_bf16 v[64:67], a[88:91], v[16:19], v[64:67]\n"
    "v_mfma_f32_16x16x32_bf16 v[64:67], a[92:95], v[20:23], v[64:67]\n"
    "v_mfma_f32_16x16x32_bf16 v[68:71], a[96:99], v[24:27], 0\n"
    "v_mfma_f32_16x16x32_bf16 v[68:71], a[100:103], v[28:31], v[68:71]\n"
    "v_mfma_f32_16x16x32_bf16 v[68:71], a[104:107], v[32:35], v[68:71]\n"
    "v_mfma_f32_16x16x32_bf16 v[68:71], a[108:111], v[36:39], v[68:71]\n"
    "v_mfma_f32_16x16x32_bf16 v[68:71], a[96:99], v[40:43], v[68:71]\n"
    "v_mfma_f32_16x16x32_bf16 v[68:71], a[100:103], v[44:47], v[68:71]\n"
    "v_mfma_f32_16x16x32_bf16 v[68:71], a[104:107], v[48:51], v[68:71]\n"
    "v_mfma_f32_16x16x32_bf16 v[68:71], a[108:111], v[52:55], v[68:71]\n"
    "v_mfma_f32_16x16x32_bf16 v[72:75], a[112:115], v[24:27], 0\n"
    "v_mfma_f32_16x16x32_bf16 v[72:75], a[116:119], v[28:31], v[72:75]\n"
    "v_mfma_f32_16x16x32_bf16 v[72:75], a[120:123], v[32:35], v[72:75]\n"
    "v_mfma_f32_16x16x32_bf16 v[72:75], a[124:127], v[36:39], v[72:75]\n"
    "v_mfma_f32_16x16x32_bf16 v[72:75], a[112:115], v[40:43], v[72:75]\n"
    "v_mfma_f32_16x16x32_bf16 v[72:75], a[116:119], v[44:47], v[72:75]\n"
    "v_mfma_f32_16x16x32_bf16 v[72:75], a[120:123], v[48:51], v[72:75]\n"
    "v_mfma_f32_16x16x32_bf16 v[72:75], a[124:127], v[52:55], v[72:75]\n"
    "v_cvt_pk_bf16_f32 v56, v60, v61\n"
    "v_lshlrev_b32 v60, 16, v56\n"
    "v_and_b32 v61, 0xffff0000, v56\n"
    "v_mul_f32 v60, 0x3db504f3, v60\n"
    "v_mul_f32 v61, 0x3db504f3, v61\n"
    "v_cvt_pk_bf16_f32 v56, v60, v61\n"
    "v_lshlrev_b32 v60, 16, v56\n"
    "v_and_b32 v61, 0xffff0000, v56\n"
    "v_mul_f32 v60, 0x3fb8aa3b, v60\n"
    "v_mul_f32 v61, 0x3fb8aa3b, v61\n"
    "v_cvt_pk_bf16_f32 v56, v62, v63\n"
    "v_lshlrev_b32 v62, 16, v56\n"
    "v_and_b32 v63, 0xffff0000, v56\n"
    "v_mul_f32 v62, 0x3db504f3, v62\n"
    "v_mul_f32 v63, 0x3db504f3, v63\n"
    "v_cvt_pk_bf16_f32 v56, v62, v63\n"
    "v_lshlrev_b32 v62, 16, v56\n"
    "v_and_b32 v63, 0xffff0000, v56\n"
    "v_mul_f32 v62, 0x3fb8aa3b, v62\n"
    "v_mul_f32 v63, 0x3fb8aa3b, v63\n"
    "v_cvt_pk_bf16_f32 v56, v64, v65\n"
    "v_lshlrev_b32 v64, 16, v56\n"
    "v_and_b32 v65, 0xffff0000, v56\n"
    "v_mul_f32 v64, 0x3db504f3, v64\n"
    "v_mul_f32 v65, 0x3db504f3, v65\n"
    "v_cvt_pk_bf16_f32 v56, v64, v65\n"
    "v_lshlrev_b32 v64, 16, v56\n"
    "v_and_b32 v65, 0xffff0000, v56\n"
    "v_mul_f32 v64, 0x3fb8aa3b, v64\n"
    "v_mul_f32 v65, 0x3fb8aa3b, v65\n"
    "v_cvt_pk_bf16_f32 v56, v66, v67\n"
    "v_lshlrev_b32 v66, 16, v56\n"
    "v_and_b32 v67, 0xffff0000, v56\n"
    "v_mul_f32 v66, 0x3db504f3, v66\n"
    "v_mul_f32 v67, 0x3db504f3, v67\n"
    "v_cvt_pk_bf16_f32 v56, v66, v67\n"
    "v_lshlrev_b32 v66, 16, v56\n"
    "v_and_b32 v67, 0xffff0000, v56\n"
    "v_mul_f32 v66, 0x3fb8aa3b, v66\n"
    "v_mul_f32 v67, 0x3fb8aa3b, v67\n"
    "v_max3_f32 v76, v60, v61, v62\n"
    "v_max3_f32 v76, v76, v63, v64\n"
    "v_max3_f32 v76, v76, v65, v66\n"
    "v_max_f32 v76, v76, v67\n"
    "v_mov_b32 v56, v76\n"
    "s_nop 1\n"
    "v_permlane32_swap_b32 v76, v56\n"
    "s_nop 1\n"
    "v_max_f32 v76, v76, v56\n"
    "v_mov_b32 v56, v76\n"
    "s_nop 1\n"
    "v_permlane16_swap_b32 v76, v56\n"
    "s_nop 1\n"
    "v_max_f32 v76, v76, v56\n"
    "v_sub_f32 v60, v60, v76\n"
    "v_sub_f32 v61, v61, v76\n"
    "v_sub_f32 v62, v62, v76\n"
    "v_sub_f32 v63, v63, v76\n"
    "v_sub_f32 v64, v64, v76\n"
    "v_sub_f32 v65, v65, v76\n"
    "v_sub_f32 v66, v66, v76\n"
    "v_sub_f32 v67, v67, v76\n"
    "v_exp_f32 v60, v60\n"
    "v_exp_f32 v61, v61\n"
    "v_exp_f32 v62, v62\n"
    "v_exp_f32 v63, v63\n"
    "v_exp_f32 v64, v64\n"
    "v_exp_f32 v65, v65\n"
    "v_exp_f32 v66, v66\n"
    "v_exp_f32 v67, v67\n"
    "v_mov_b32 v77, 0\n"
    "v_mov_b32 v78, 0\n"
    "v_mov_b32 v79, 0\n"
    "v_add_f32 v77, v77, v60\n"
    "v_fmac_f32 v78, v60, v68\n"
    "v_add_f32 v77, v77, v61\n"
    "v_fmac_f32 v78, v61, v69\n"
    "v_add_f32 v77, v77, v62\n"
    "v_fmac_f32 v78, v62, v70\n"
    "v_add_f32 v77, v77, v63\n"
    "v_fmac_f32 v78, v63, v71\n"
    "v_add_f32 v77, v77, v64\n"
    "v_fmac_f32 v78, v64, v72\n"
    "v_add_f32 v77, v77, v65\n"
    "v_fmac_f32 v78, v65, v73\n"
    "v_add_f32 v77, v77, v66\n"
    "v_fmac_f32 v78, v66, v74\n"
    "v_add_f32 v77, v77, v67\n"
    "v_fmac_f32 v78, v67, v75\n"
    "v_mov_b32 v56, v77\n"
    "s_nop 1\n"
    "v_permlane32_swap_b32 v77, v56\n"
    "s_nop 1\n"
    "v_add_f32 v77, v77, v56\n"
    "v_mov_b32 v56, v77\n"
    "s_nop 1\n"
    "v_permlane16_swap_b32 v77, v56\n"
    "s_nop 1\n"
    "v_add_f32 v77, v77, v56\n"
    "v_mov_b32 v56, v78\n"
    "s_nop 1\n"
    "v_permlane32_swap_b32 v78, v56\n"
    "s_nop 1\n"
    "v_add_f32 v78, v78, v56\n"
    "v_mov_b32 v56, v78\n"
    "s_nop 1\n"
    "v_permlane16_swap_b32 v78, v56\n"
    "s_nop 1\n"
    "v_add_f32 v78, v78, v56\n"
    "s_lshl_b32 s80, s78, 4\n"
    "s_add_u32 s80, s80, 124928\n"
    "v_lshl_add_u32 v6, v2, 6, s80\n"
    "ds_write_b128 v6, v[76:79]\n"
    "v_lshlrev_b32 v6, 6, v2\n"
    "v_add_u32 v6, 122880, v6\n"
    "s_waitcnt lgkmcnt(0)\n"
    "s_barrier\n"
    "ds_read_b128 v[8:11], v6 offset:2048\n"
    "ds_read_b128 v[12:15], v6 offset:2064\n"
    "ds_read_b128 v[16:19], v6 offset:2080\n"
    "ds_read_b128 v[20:23], v6 offset:2096\n"
    "ds_read_b128 v[24:27], v6 offset:1280\n"
    "ds_read_b128 v[28:31], v6 offset:1296\n"
    "ds_read_b128 v[32:35], v6 offset:1312\n"
    "ds_read_b128 v[36:39], v6 offset:1328\n"
    "s_waitcnt lgkmcnt(5)\n"
    "v_max3_f32 v76, v8, v12, v16\n"
    "s_waitcnt lgkmcnt(4)\n"
    "v_max_f32 v76, v76, v20\n"
    "v_mov_b32 v77, 0\n"
    "v_mov_b32 v78, 0\n"
    "v_sub_f32 v8, v8, v76\n"
    "v_sub_f32 v12, v12, v76\n"
    "v_sub_f32 v16, v16, v76\n"
    "v_sub_f32 v20, v20, v76\n"
    "v_exp_f32 v8, v8\n"
    "v_exp_f32 v12, v12\n"
    "v_exp_f32 v16, v16\n"
    "v_exp_f32 v20, v20\n"
    "v_fmac_f32 v77, v8, v9\n"
    "v_fmac_f32 v78, v8, v10\n"
    "v_fmac_f32 v77, v12, v13\n"
    "v_fmac_f32 v78, v12, v14\n"
    "v_fmac_f32 v77, v16, v17\n"
    "v_fmac_f32 v78, v16, v18\n"
    "v_fmac_f32 v77, v20, v21\n"
    "v_fmac_f32 v78, v20, v22\n"
    "s_waitcnt lgkmcnt(0)\n"
    "v_add_f32 v79, v24, v25\n"
    "v_add_f32 v79, v79, v26\n"
    "v_add_f32 v79, v79, v27\n"
    "v_add_f32 v79, v79, v28\n"
    "v_add_f32 v79, v79, v29\n"
    "v_add_f32 v79, v79, v30\n"
    "v_add_f32 v79, v79, v31\n"
    "v_add_f32 v79, v79, v32\n"
    "v_add_f32 v79, v79, v33\n"
    "v_add_f32 v79, v79, v34\n"
    "v_add_f32 v79, v79, v35\n"
    "v_add_f32 v79, v79, v36\n"
    "v_add_f32 v79, v79, v37\n"
    "v_add_f32 v79, v79, v38\n"
    "v_add_f32 v79, v79, v39\n"
    "v_cmp_eq_u32 vcc, 0, v3\n"
    "s_nop 1\n"
    "v_cndmask_b32 v79, 0, v79, vcc\n"
    "v_min_u32 v76, -2, v76\n"
    "v_min_u32 v77, -2, v77\n"
    "v_min_u32 v78, -2, v78\n"
    "v_min_u32 v79, -2, v79\n"
    "v_lshlrev_b32 v6, 8, v0\n"
    "v_cmp_gt_u32 vcc, 6, v0\n"
    "v_mov_b32 v7, 2147483632\n"
    "s_nop 0\n"
    "v_cndmask_b32 v6, v7, v6, vcc\n"
    "s_lshl_b32 s80, s88, 7\n"
    "s_add_u32 s80, s80, s89\n"
    "s_lshl_b32 s80, s80, 4\n"
    "s_add_u32 s80, s80, 7405568\n"
    "buffer_store_dwordx4 v[76:79], v6, s[56:59], s80 offen sc1\n"
    "s_or_b32 s81, s89, s78\n"
    "s_cmp_lg_u32 s81, 0\n"
    "s_cbranch_scc1 RLN%=\n"
    "v_lshlrev_b32 v6, 4, v5\n"
    "s_mov_b32 s87, 0\n"
    "RRP%=:\n"
    "buffer_load_dwordx4 v[8:11], v6, s[56:59], s80 offen offset:0 sc1\n"
    "buffer_load_dwordx4 v[12:15], v6, s[56:59], s80 offen offset:256 sc1\n"
    "buffer_load_dwordx4 v[16:19], v6, s[56:59], s80 offen offset:512 sc1\n"
    "buffer_load_dwordx4 v[20:23], v6, s[56:59], s80 offen offset:768 sc1\n"
    "buffer_load_dwordx4 v[24:27], v6, s[56:59], s80 offen offset:1024 sc1\n"
    "buffer_load_dwordx4 v[28:31], v6, s[56:59], s80 offen offset:1280 sc1\n"
    "s_waitcnt vmcnt(0)\n"
    "v_max_u32 v56, v8, v9\n"
    "v_max3_u32 v56, v56, v10, v11\n"
    "v_max3_u32 v56, v56, v12, v13\n"
    "v_max3_u32 v56, v56, v14, v15\n"
    "v_max3_u32 v56, v56, v16, v17\n"
    "v_max3_u32 v56, v56, v18, v19\n"
    "v_max3_u32 v56, v56, v20, v21\n"
    "v_max3_u32 v56, v56, v22, v23\n"
    "v_max3_u32 v56, v56, v24, v25\n"
    "v_max3_u32 v56, v56, v26, v27\n"
    "v_max3_u32 v56, v56, v28, v29\n"
    "v_max3_u32 v56, v56, v30, v31\n"
    "v_cmp_eq_u32 vcc, -1, v56\n"
    "s_nop 1\n"
    "s_cmp_eq_u64 vcc, 0\n"
    "s_cbranch_scc1 RRD%=\n"
    "s_add_u32 s87, s87, 1\n"
    "s_cmp_gt_u32 s87, 262144\n"
    "s_cbranch_scc1 RRD%=\n"
    "s_sleep 1\n"
    "s_branch RRP%=\n"
    "RRD%=:\n"
    "v_mov_b32 v40, v8\n"
    "v_mov_b32 v41, v12\n"
    "v_mov_b32 v42, v16\n"
    "v_mov_b32 v43, v20\n"
    "v_mov_b32 v44, v24\n"
    "v_mov_b32 v45, v28\n"
    "v_max_f32_dpp v40, v40, v40 row_ror:1 row_mask:0xf bank_mask:0xf\n"
    "v_max_f32_dpp v41, v41, v41 row_ror:1 row_mask:0xf bank_mask:0xf\n"
    "v_max_f32_dpp v42, v42, v42 row_ror:1 row_mask:0xf bank_mask:0xf\n"
    "v_max_f32_dpp v43, v43, v43 row_ror:1 row_mask:0xf bank_mask:0xf\n"
    "v_max_f32_dpp v44, v44, v44 row_ror:1 row_mask:0xf bank_mask:0xf\n"
    "v_max_f32_dpp v45, v45, v45 row_ror:1 row_mask:0xf bank_mask:0xf\n"
    "v_max_f32_dpp v40, v40, v40 row_ror:2 row_mask:0xf bank_mask:0xf\n"
    "v_max_f32_dpp v41, v41, v41 row_ror:2 row_mask:0xf bank_mask:0xf\n"
    "v_max_f32_dpp v42, v42, v42 row_ror:2 row_mask:0xf bank_mask:0xf\n"
    "v_max_f32_dpp v43, v43, v43 row_ror:2 row_mask:0xf bank_mask:0xf\n"
    "v_max_f32_dpp v44, v44, v44 row_ror:2 row_mask:0xf bank_mask:0xf\n"
    "v_max_f32_dpp v45, v45, v45 row_ror:2 row_mask:0xf bank_mask:0xf\n"
    "v_max_f32_dpp v40, v40, v40 row_ror:4 row_mask:0xf bank_mask:0xf\n"
    "v_max_f32_dpp v41, v41, v41 row_ror:4 row_mask:0xf bank_mask:0xf\n"
    "v_max_f32_dpp v42, v42, v42 row_ror:4 row_mask:0xf bank_mask:0xf\n"
    "v_max_f32_dpp v43, v43, v43 row_ror:4 row_mask:0xf bank_mask:0xf\n"
    "v_max_f32_dpp v44, v44, v44 row_ror:4 row_mask:0xf bank_mask:0xf\n"
    "v_max_f32_dpp v45, v45, v45 row_ror:4 row_mask:0xf bank_mask:0xf\n"
    "v_max_f32_dpp v40, v40, v40 row_ror:8 row_mask:0xf bank_mask:0xf\n"
    "v_max_f32_dpp v41, v41, v41 row_ror:8 row_mask:0xf bank_mask:0xf\n"
    "v_max_f32_dpp v42, v42, v42 row_ror:8 row_mask:0xf bank_mask:0xf\n"
    "v_max_f32_dpp v43, v43, v43 row_ror:8 row_mask:0xf bank_mask:0xf\n"
    "v_max_f32_dpp v44, v44, v44 row_ror:8 row_mask:0xf bank_mask:0xf\n"
    "v_max_f32_dpp v45, v45, v45 row_ror:8 row_mask:0xf bank_mask:0xf\n"
    "v_sub_f32 v40, v8, v40\n"
    "v_sub_f32 v41, v12, v41\n"
    "v_sub_f32 v42, v16, v42\n"
    "v_sub_f32 v43, v20, v43\n"
    "v_sub_f32 v44, v24, v44\n"
    "v_sub_f32 v45, v28, v45\n"
    "v_exp_f32 v40, v40\n"
    "v_exp_f32 v41, v41\n"
    "v_exp_f32 v42, v42\n"
    "v_exp_f32 v43, v43\n"
    "v_exp_f32 v44, v44\n"
    "v_exp_f32 v45, v45\n"
    "v_mul_f32 v9, v9, v40\n"
    "v_mul_f32 v10, v10, v40\n"
    "v_mul_f32 v13, v13, v41\n"
    "v_mul_f32 v14, v14, v41\n"
    "v_mul_f32 v17, v17, v42\n"
    "v_mul_f32 v18, v18, v42\n"
    "v_mul_f32 v21, v21, v43\n"
    "v_mul_f32 v22, v22, v43\n"
    "v_mul_f32 v25, v25, v44\n"
    "v_mul_f32 v26, v26, v44\n"
    "v_mul_f32 v29, v29, v45\n"
    "v_mul_f32 v30, v30, v45\n"
    "v_add_f32_dpp v9, v9, v9 row_ror:1 row_mask:0xf bank_mask:0xf\n"
    "v_add_f32_dpp v10, v10, v10 row_ror:1 row_mask:0xf bank_mask:0xf\n"
    "v_add_f32_dpp v11, v11, v11 row_ror:1 row_mask:0xf bank_mask:0xf\n"
    "v_add_f32_dpp v13, v13, v13 row_ror:1 row_mask:0xf bank_mask:0xf\n"
    "v_add_f32_dpp v14, v14, v14 row_ror:1 row_mask:0xf bank_mask:0xf\n"
    "v_add_f32_dpp v15, v15, v15 row_ror:1 row_mask:0xf bank_mask:0xf\n"
    "v_add_f32_dpp v17, v17, v17 row_ror:1 row_mask:0xf bank_mask:0xf\n"
    "v_add_f32_dpp v18, v18, v18 row_ror:1 row_mask:0xf bank_mask:0xf\n"
    "v_add_f32_dpp v19, v19, v19 row_ror:1 row_mask:0xf bank_mask:0xf\n"
    "v_add_f32_dpp v21, v21, v21 row_ror:1 row_mask:0xf bank_mask:0xf\n"
    "v_add_f32_dpp v22, v22, v22 row_ror:1 row_mask:0xf bank_mask:0xf\n"
    "v_add_f32_dpp v25, v25, v25 row_ror:1 row_mask:0xf bank_mask:0xf\n"
    "v_add_f32_dpp v26, v26, v26 row_ror:1 row_mask:0xf bank_mask:0xf\n"
    "v_add_f32_dpp v29, v29, v29 row_ror:1 row_mask:0xf bank_mask:0xf\n"
    "v_add_f32_dpp v30, v30, v30 row_ror:1 row_mask:0xf bank_mask:0xf\n"
    "v_add_f32_dpp v9, v9, v9 row_ror:2 row_mask:0xf bank_mask:0xf\n"
    "v_add_f32_dpp v10, v10, v10 row_ror:2 row_mask:0xf bank_mask:0xf\n"
    "v_add_f32_dpp v11, v11, v11 row_ror:2 row_mask:0xf bank_mask:0xf\n"
    "v_add_f32_dpp v13, v13, v13 row_ror:2 row_mask:0xf bank_mask:0xf\n"
    "v_add_f32_dpp v14, v14, v14 row_ror:2 row_mask:0xf bank_mask:0xf\n"
    "v_add_f32_dpp v15, v15, v15 row_ror:2 row_mask:0xf bank_mask:0xf\n"
    "v_add_f32_dpp v17, v17, v17 row_ror:2 row_mask:0xf bank_mask:0xf\n"
    "v_add_f32_dpp v18, v18, v18 row_ror:2 row_mask:0xf bank_mask:0xf\n"
    "v_add_f32_dpp v19, v19, v19 row_ror:2 row_mask:0xf bank_mask:0xf\n"
    "v_add_f32_dpp v21, v21, v21 row_ror:2 row_mask:0xf bank_mask:0xf\n"
    "v_add_f32_dpp v22, v22, v22 row_ror:2 row_mask:0xf bank_mask:0xf\n"
    "v_add_f32_dpp v25, v25, v25 row_ror:2 row_mask:0xf bank_mask:0xf\n"
    "v_add_f32_dpp v26, v26, v26 row_ror:2 row_mask:0xf bank_mask:0xf\n"
    "v_add_f32_dpp v29, v29, v29 row_ror:2 row_mask:0xf bank_mask:0xf\n"
    "v_add_f32_dpp v30, v30, v30 row_ror:2 row_mask:0xf bank_mask:0xf\n"
    "v_add_f32_dpp v9, v9, v9 row_ror:4 row_mask:0xf bank_mask:0xf\n"
    "v_add_f32_dpp v10, v10, v10 row_ror:4 row_mask:0xf bank_mask:0xf\n"
    "v_add_f32_dpp v11, v11, v11 row_ror:4 row_mask:0xf bank_mask:0xf\n"
    "v_add_f32_dpp v13, v13, v13 row_ror:4 row_mask:0xf bank_mask:0xf\n"
    "v_add_f32_dpp v14, v14, v14 row_ror:4 row_mask:0xf bank_mask:0xf\n"
    "v_add_f32_dpp v15, v15, v15 row_ror:4 row_mask:0xf bank_mask:0xf\n"
    "v_add_f32_dpp v17, v17, v17 row_ror:4 row_mask:0xf bank_mask:0xf\n"
    "v_add_f32_dpp v18, v18, v18 row_ror:4 row_mask:0xf bank_mask:0xf\n"
    "v_add_f32_dpp v19, v19, v19 row_ror:4 row_mask:0xf bank_mask:0xf\n"
    "v_add_f32_dpp v21, v21, v21 row_ror:4 row_mask:0xf bank_mask:0xf\n"
    "v_add_f32_dpp v22, v22, v22 row_ror:4 row_mask:0xf bank_mask:0xf\n"
    "v_add_f32_dpp v25, v25, v25 row_ror:4 row_mask:0xf bank_mask:0xf\n"
    "v_add_f32_dpp v26, v26, v26 row_ror:4 row_mask:0xf bank_mask:0xf\n"
    "v_add_f32_dpp v29, v29, v29 row_ror:4 row_mask:0xf bank_mask:0xf\n"
    "v_add_f32_dpp v30, v30, v30 row_ror:4 row_mask:0xf bank_mask:0xf\n"
    "v_add_f32_dpp v9, v9, v9 row_ror:8 row_mask:0xf bank_mask:0xf\n"
    "v_add_f32_dpp v10, v10, v10 row_ror:8 row_mask:0xf bank_mask:0xf\n"
    "v_add_f32_dpp v11, v11, v11 row_ror:8 row_mask:0xf bank_mask:0xf\n"
    "v_add_f32_dpp v13, v13, v13 row_ror:8 row_mask:0xf bank_mask:0xf\n"
    "v_add_f32_dpp v14, v14, v14 row_ror:8 row_mask:0xf bank_mask:0xf\n"
    "v_add_f32_dpp v15, v15, v15 row_ror:8 row_mask:0xf bank_mask:0xf\n"
    "v_add_f32_dpp v17, v17, v17 row_ror:8 row_mask:0xf bank_mask:0xf\n"
    "v_add_f32_dpp v18, v18, v18 row_ror:8 row_mask:0xf bank_mask:0xf\n"
    "v_add_f32_dpp v19, v19, v19 row_ror:8 row_mask:0xf bank_mask:0xf\n"
    "v_add_f32_dpp v21, v21, v21 row_ror:8 row_mask:0xf bank_mask:0xf\n"
    "v_add_f32_dpp v22, v22, v22 row_ror:8 row_mask:0xf bank_mask:0xf\n"
    "v_add_f32_dpp v25, v25, v25 row_ror:8 row_mask:0xf bank_mask:0xf\n"
    "v_add_f32_dpp v26, v26, v26 row_ror:8 row_mask:0xf bank_mask:0xf\n"
    "v_add_f32_dpp v29, v29, v29 row_ror:8 row_mask:0xf bank_mask:0xf\n"
    "v_add_f32_dpp v30, v30, v30 row_ror:8 row_mask:0xf bank_mask:0xf\n"
    "v_rcp_f32 v9, v9\n"
    "v_rcp_f32 v13, v13\n"
    "v_rcp_f32 v17, v17\n"
    "v_rcp_f32 v21, v21\n"
    "v_rcp_f32 v25, v25\n"
    "v_rcp_f32 v29, v29\n"
    "v_fma_f32 v48, v9, v10, -v11\n"
    "v_fmac_f32 v48, v21, v22\n"
    "v_fma_f32 v49, v13, v14, -v15\n"
    "v_fmac_f32 v49, v25, v26\n"
    "v_fma_f32 v50, v17, v18, -v19\n"
    "v_fmac_f32 v50, v29, v30\n"
    "v_mov_b32 v51, 0\n"
    "v_min_u32 v48, -2, v48\n"
    "v_min_u32 v49, -2, v49\n"
    "v_min_u32 v50, -2, v50\n"
    "buffer_store_dwordx4 v[48:51], v234, s[56:59], s80 offen offset:1536 sc1\n"
    "RLN%=:\n"
    "v_lshlrev_b32 v6, 11, v5\n"
    "s_mov_b32 s80, 7407104\n"
    "s_mov_b32 s87, 0\n"
    "v_mov_b32 v7, 4196608\n"
    "v_ashrrev_i32 v56, 31, v204\n"
    "v_not_b32 v56, v56\n"
    "v_or_b32 v56, 0x80000000, v56\n"
    "v_xor_b32 v40, v204, v56\n"
    "v_ashrrev_i32 v56, 31, v205\n"
    "v_not_b32 v56, v56\n"
    "v_or_b32 v56, 0x80000000, v56\n"
    "v_xor_b32 v41, v205, v56\n"
    "v_ashrrev_i32 v56, 31, v206\n"
    "v_not_b32 v56, v56\n"
    "v_or_b32 v56, 0x80000000, v56\n"
    "v_xor_b32 v42, v206, v56\n"
    "RFP%=:\n"
    "buffer_load_dwordx4 v[8:11], v6, s[56:59], s80 offen sc1\n"
    "buffer_load_dword v32, v7, s[56:59], s76 offen sc1\n"
    "buffer_load_dword v33, v7, s[56:59], s77 offen sc1\n"
    "buffer_load_dword v34, v7, s[56:59], s37 offen sc1\n"
    "s_waitcnt vmcnt(0)\n"
    "v_max3_u32 v56, v8, v9, v10\n"
    "v_max3_u32 v56, v56, v32, v33\n"
    "v_max_u32 v56, v56, v34\n"
    "v_cmp_eq_u32 vcc, -1, v56\n"
    "s_nop 1\n"
    "s_cmp_eq_u64 vcc, 0\n"
    "s_cbranch_scc1 RFD%=\n"
    "s_add_u32 s87, s87, 1\n"
    "s_cmp_gt_u32 s87, 262144\n"
    "s_cbranch_scc1 RFD%=\n"
    "s_sleep 3\n"
    "s_branch RFP%=\n"
    "RFD%=:\n"
    "v_add_f32_dpp v8, v8, v8 row_ror:1 row_mask:0xf bank_mask:0xf\n"
    "v_add_f32_dpp v9, v9, v9 row_ror:1 row_mask:0xf bank_mask:0xf\n"
    "v_add_f32_dpp v10, v10, v10 row_ror:1 row_mask:0xf bank_mask:0xf\n"
    "v_add_f32_dpp v8, v8, v8 row_ror:2 row_mask:0xf bank_mask:0xf\n"
    "v_add_f32_dpp v9, v9, v9 row_ror:2 row_mask:0xf bank_mask:0xf\n"
    "v_add_f32_dpp v10, v10, v10 row_ror:2 row_mask:0xf bank_mask:0xf\n"
    "v_add_f32_dpp v8, v8, v8 row_ror:4 row_mask:0xf bank_mask:0xf\n"
    "v_add_f32_dpp v9, v9, v9 row_ror:4 row_mask:0xf bank_mask:0xf\n"
    "v_add_f32_dpp v10, v10, v10 row_ror:4 row_mask:0xf bank_mask:0xf\n"
    "v_add_f32_dpp v8, v8, v8 row_ror:8 row_mask:0xf bank_mask:0xf\n"
    "v_add_f32_dpp v9, v9, v9 row_ror:8 row_mask:0xf bank_mask:0xf\n"
    "v_add_f32_dpp v10, v10, v10 row_ror:8 row_mask:0xf bank_mask:0xf\n"
    "v_readfirstlane_b32 s80, v8\n"
    "v_readfirstlane_b32 s81, v9\n"
    "v_readfirstlane_b32 s82, v10\n"
    "s_nop 1\n"
    "v_fmac_f32 v40, s80, v32\n"
    "v_fmac_f32 v41, s81, v33\n"
    "v_fmac_f32 v42, s82, v34\n"
    "v_cmp_lt_f32 vcc, v40, v41\n"
    "v_min_f32 v56, v40, v41\n"
    "v_cmp_gt_f32 s[80:81], v42, v56\n"
    "s_nop 1\n"
    "s_and_b64 s[82:83], s[80:81], vcc\n"
    "s_andn2_b64 s[80:81], s[80:81], vcc\n"
    "s_cmp_lg_u64 s[82:83], 0\n"
    "s_cselect_b32 s72, s38, s72\n"
    "s_cselect_b32 s74, s36, s74\n"
    "s_cselect_b32 s76, s37, s76\n"
    "s_cmp_lg_u64 s[80:81], 0\n"
    "s_cselect_b32 s73, s38, s73\n"
    "s_cselect_b32 s75, s36, s75\n"
    "s_cselect_b32 s77, s37, s77\n"
    "s_branch DISP%=\n"
    "FALLBACK%=:\n"
    "s_waitcnt vmcnt(0) lgkmcnt(0)\n"
    "s_barrier\n"
    "v_lshrrev_b32 v247, 4, v239\n"
    "v_and_b32 v248, 15, v239\n"
    "v_add_u32 v249, 0, v247\n"
    "v_xor_b32 v249, v249, v248\n"
    "v_lshlrev_b32 v249, 4, v249\n"
    "v_lshl_add_u32 v208, v248, 8, v249\n"
    "v_add_u32 v208, 131072, v208\n"
    "v_lshl_add_u32 v249, v247, 8, v249\n"
    "v_add_u32 v214, s79, v249\n"
    "v_add_u32 v249, 4, v247\n"
    "v_xor_b32 v249, v249, v248\n"
    "v_lshlrev_b32 v249, 4, v249\n"
    "v_lshl_add_u32 v209, v248, 8, v249\n"
    "v_add_u32 v209, 131072, v209\n"
    "v_lshl_add_u32 v249, v247, 8, v249\n"
    "v_add_u32 v215, s79, v249\n"
    "v_add_u32 v249, 8, v247\n"
    "v_xor_b32 v249, v249, v248\n"
    "v_lshlrev_b32 v249, 4, v249\n"
    "v_lshl_add_u32 v210, v248, 8, v249\n"
    "v_add_u32 v210, 131072, v210\n"
    "v_lshl_add_u32 v249, v247, 8, v249\n"
    "v_add_u32 v216, s79, v249\n"
    "v_add_u32 v249, 12, v247\n"
    "v_xor_b32 v249, v249, v248\n"
    "v_lshlrev_b32 v249, 4, v249\n"
    "v_lshl_add_u32 v211, v248, 8, v249\n"
    "v_add_u32 v211, 131072, v211\n"
    "v_lshl_add_u32 v249, v247, 8, v249\n"
    "v_add_u32 v217, s79, v249\n"
    "v_and_b32 v246, 3, v239\n"
    "v_lshlrev_b32 v249, 4, v246\n"
    "v_lshl_add_u32 v249, v247, 6, v249\n"
    "v_bfe_u32 v248, v239, 2, 2\n"
    "v_lshl_add_u32 v218, v248, 8, v249\n"
    "v_add_u32 v218, s79, v218\n"
    "v_xor_b32 v219, 32, v218\n"
    "v_mul_u32_u24 v227, 1152, v248\n"
    "v_add_u32 v227, v227, v249\n"
    "s_and_b32 s85, s88, 3\n"
    "s_lshl_b32 s85, s85, 8\n"
    "v_add_u32 v227, s85, v227\n"
    "v_mov_b32 v240, v227\n"
    "v_xor_b32 v241, 32, v240\n"
    "v_add_u32 v241, 3584, v241\n"
    "v_add_u32 v242, 7168, v240\n"
    "v_add_u32 v243, 7168, v241\n"
    "v_lshlrev_b32 v246, 3, v246\n"
    "v_lshl_add_u32 v246, v248, 6, v246\n"
    "v_lshl_add_u32 v246, v247, 10, v246\n"
    "v_and_b32 v249, 16, v239\n"
    "v_lshl_add_u32 v235, v249, 1, v246\n"
    "v_xor_b32 v236, 32, v235\n"
    "v_add_u32 v212, 98304, v235\n"
    "v_add_u32 v237, 90112, v235\n"
    "v_add_u32 v213, 98304, v236\n"
    "v_add_u32 v238, 90112, v236\n"
    "v_and_b32 v249, 1, v239\n"
    "v_and_b32 v246, 2, v239\n"
    "v_cmp_eq_u32 vcc, 0, v246\n"
    "v_mov_b32 v248, 0x3f803f80\n"
    "v_mov_b32 v247, 0x44444444\n"
    "v_mov_b32 v246, 0xeeeeeeee\n"
    "v_cndmask_b32 v220, 0, v248, vcc\n"
    "v_cndmask_b32 v221, 0, v248, vcc\n"
    "v_cndmask_b32 v222, 0, v248, vcc\n"
    "v_cndmask_b32 v223, 0, v248, vcc\n"
    "v_cmp_eq_u32 vcc, 0, v249\n"
    "s_add_u32 s74, s79, 131072\n"
    "s_add_u32 s76, s79, 114688\n"
    "s_add_u32 s75, s79, 147456\n"
    "s_add_u32 s77, s79, 98304\n"
    "v_cndmask_b32 v224, v246, v247, vcc\n"
    "v_lshrrev_b32 v246, 1, v239\n"
    "v_mul_u32_u24 v233, 1152, v246\n"
    "v_lshl_add_u32 v233, v249, 4, v233\n"
    "s_lshr_b32 s85, s85, 3\n"
    "s_add_u32 s85, s85, 1024\n"
    "v_add_u32 v233, s85, v233\n"
    "s_and_b32 s85, s78, 1\n"
    "s_lshl_b32 s86, s85, 5\n"
    "v_xor_b32 v227, s86, v227\n"
    "s_lshl_b32 s73, s78, 10\n"
    "s_movk_i32 s37, 4096\n"
    "s_movk_i32 s36, 18432\n"
    "s_and_b32 s84, s88, 3\n"
    "s_mul_i32 s38, s84, 294912\n"
    "s_mul_i32 s92, s78, 4608\n"
    "s_add_u32 s92, s92, s38\n"
    "s_mov_b64 s[68:69], s[52:53]\n"
    "s_mov_b64 s[70:71], s[54:55]\n"
    "s_and_b32 s39, %14, 7\n"
    "s_lshl_b32 s39, s39, 1\n"
    "s_bfe_u32 s84, %14, 0x10003\n"
    "s_add_u32 s39, s39, s84\n"
    "s_and_b32 s84, %14, 0xf0\n"
    "s_add_u32 s39, s39, s84\n"
    "s_cmp_eq_u32 s78, 1\n"
    "s_cselect_b64 s[60:61], %6, %7\n"
    "s_cmp_eq_u32 s78, 3\n"
    "s_cselect_b64 s[60:61], %8, s[60:61]\n"
    "s_lshr_b32 s84, %14, 3\n"
    "s_cmp_eq_u32 s78, 0\n"
    "s_cselect_b64 s[60:61], s[52:53], s[60:61]\n"
    "s_cselect_b32 s84, s84, s39\n"
    "s_mul_i32 s85, s84, 36864\n"
    "s_lshr_b32 s86, s39, 1\n"
    "s_lshl_b32 s86, s86, 7\n"
    "s_and_b32 s87, s39, 1\n"
    "s_mul_i32 s87, s87, 9216\n"
    "s_add_u32 s86, s86, s87\n"
    "s_sub_u32 s87, 1769472, s86\n"
    "s_add_u32 s64, s78, 1\n"
    "s_bitcmp1_b32 s64, 1\n"
    "s_cselect_b32 s85, s86, s85\n"
    "s_cselect_b32 s62, s87, 36864\n"
    "s_cselect_b32 s64, 144, 1\n"
    "s_cmp_lt_u32 s84, 144\n"
    "s_cselect_b32 s62, s62, 0\n"
    "s_add_u32 s60, s60, s85\n"
    "s_addc_u32 s61, s61, 0\n"
    "s_and_b32 s61, s61, 0xffff\n"
    "s_mov_b32 s63, 0x20000\n"
    "s_lshl_b32 s64, s64, 7\n"
    "v_mul_u32_u24 v234, s64, v246\n"
    "s_lshl_b32 s64, s64, 5\n"
    "s_mov_b32 s65, -1\n"
    "s_mov_b64 s[94:95], 0\n"
    "s_mov_b32 s66, -1\n"
    "s_mov_b32 s70, 0\n"
    "s_mov_b32 s37, 0\n"
    "s_add_u32 s73, s73, 86016\n"
    "v_mov_b32 v192, 0xff800000\n"
    "v_mov_b32 v196, 0xff800000\n"
    "v_mov_b32 v200, 0xff800000\n"
    "v_mov_b32 v204, 0xff800000\n"
    "s_mov_b32 s72, 0\n"
    "MAXL%=:\n"
    "s_mov_b32 m0, s74\n"
    "s_nop 0\n"
    "buffer_load_dwordx4 v214, s[40:43], s72 offen offset:0  lds\n"
    "buffer_load_dwordx4 v215, s[40:43], s72 offen offset:1024  lds\n"
    "buffer_load_dwordx4 v216, s[40:43], s72 offen offset:2048  lds\n"
    "buffer_load_dwordx4 v217, s[40:43], s72 offen offset:3072  lds\n"
    "s_waitcnt vmcnt(0)\n"
    "s_barrier\n"
    "ds_read_b128 a[64:67], v208 offset:0\n"
    "ds_read_b128 a[68:71], v209 offset:0\n"
    "ds_read_b128 a[72:75], v210 offset:0\n"
    "ds_read_b128 a[76:79], v211 offset:0\n"
    "ds_read_b128 a[80:83], v208 offset:4096\n"
    "ds_read_b128 a[84:87], v209 offset:4096\n"
    "ds_read_b128 a[88:91], v210 offset:4096\n"
    "ds_read_b128 a[92:95], v211 offset:4096\n"
    "ds_read_b128 a[96:99], v208 offset:8192\n"
    "ds_read_b128 a[100:103], v209 offset:8192\n"
    "ds_read_b128 a[104:107], v210 offset:8192\n"
    "ds_read_b128 a[108:111], v211 offset:8192\n"
    "ds_read_b128 a[112:115], v208 offset:12288\n"
    "ds_read_b128 a[116:119], v209 offset:12288\n"
    "ds_read_b128 a[120:123], v210 offset:12288\n"
    "ds_read_b128 a[124:127], v211 offset:12288\n"
    "s_waitcnt lgkmcnt(0)\n"
    "s_barrier\n"
    "v_mfma_f32_16x16x32_bf16 v[128:131], a[64:67], a[0:3], 0\n"
    "v_mfma_f32_16x16x32_bf16 v[128:131], a[68:71], a[4:7], v[128:131]\n"
    "v_mfma_f32_16x16x32_bf16 v[128:131], a[72:75], a[8:11], v[128:131]\n"
    "v_mfma_f32_16x16x32_bf16 v[128:131], a[76:79], a[12:15], v[128:131]\n"
    "v_mfma_f32_16x16x32_bf16 v[132:135], a[80:83], a[0:3], 0\n"
    "v_mfma_f32_16x16x32_bf16 v[132:135], a[84:87], a[4:7], v[132:135]\n"
    "v_mfma_f32_16x16x32_bf16 v[132:135], a[88:91], a[8:11], v[132:135]\n"
    "v_mfma_f32_16x16x32_bf16 v[132:135], a[92:95], a[12:15], v[132:135]\n"
    "v_mfma_f32_16x16x32_bf16 v[136:139], a[96:99], a[0:3], 0\n"
    "v_mfma_f32_16x16x32_bf16 v[136:139], a[100:103], a[4:7], v[136:139]\n"
    "v_mfma_f32_16x16x32_bf16 v[136:139], a[104:107], a[8:11], v[136:139]\n"
    "v_mfma_f32_16x16x32_bf16 v[136:139], a[108:111], a[12:15], v[136:139]\n"
    "v_mfma_f32_16x16x32_bf16 v[140:143], a[112:115], a[0:3], 0\n"
    "v_mfma_f32_16x16x32_bf16 v[140:143], a[116:119], a[4:7], v[140:143]\n"
    "v_mfma_f32_16x16x32_bf16 v[140:143], a[120:123], a[8:11], v[140:143]\n"
    "v_mfma_f32_16x16x32_bf16 v[140:143], a[124:127], a[12:15], v[140:143]\n"
    "s_nop 7\n"
    "s_nop 1\n"
    "v_max3_f32 v192, v192, v128, v129\n"
    "v_max3_f32 v192, v192, v130, v131\n"
    "v_max3_f32 v192, v192, v132, v133\n"
    "v_max3_f32 v192, v192, v134, v135\n"
    "v_max3_f32 v192, v192, v136, v137\n"
    "v_max3_f32 v192, v192, v138, v139\n"
    "v_max3_f32 v192, v192, v140, v141\n"
    "v_max3_f32 v192, v192, v142, v143\n"
    "v_mfma_f32_16x16x32_bf16 v[144:147], a[64:67], a[16:19], 0\n"
    "v_mfma_f32_16x16x32_bf16 v[144:147], a[68:71], a[20:23], v[144:147]\n"
    "v_mfma_f32_16x16x32_bf16 v[144:147], a[72:75], a[24:27], v[144:147]\n"
    "v_mfma_f32_16x16x32_bf16 v[144:147], a[76:79], a[28:31], v[144:147]\n"
    "v_mfma_f32_16x16x32_bf16 v[148:151], a[80:83], a[16:19], 0\n"
    "v_mfma_f32_16x16x32_bf16 v[148:151], a[84:87], a[20:23], v[148:151]\n"
    "v_mfma_f32_16x16x32_bf16 v[148:151], a[88:91], a[24:27], v[148:151]\n"
    "v_mfma_f32_16x16x32_bf16 v[148:151], a[92:95], a[28:31], v[148:151]\n"
    "v_mfma_f32_16x16x32_bf16 v[152:155], a[96:99], a[16:19], 0\n"
    "v_mfma_f32_16x16x32_bf16 v[152:155], a[100:103], a[20:23], v[152:155]\n"
    "v_mfma_f32_16x16x32_bf16 v[152:155], a[104:107], a[24:27], v[152:155]\n"
    "v_mfma_f32_16x16x32_bf16 v[152:155], a[108:111], a[28:31], v[152:155]\n"
    "v_mfma_f32_16x16x32_bf16 v[156:159], a[112:115], a[16:19], 0\n"
    "v_mfma_f32_16x16x32_bf16 v[156:159], a[116:119], a[20:23], v[156:159]\n"
    "v_mfma_f32_16x16x32_bf16 v[156:159], a[120:123], a[24:27], v[156:159]\n"
    "v_mfma_f32_16x16x32_bf16 v[156:159], a[124:127], a[28:31], v[156:159]\n"
    "s_nop 7\n"
    "s_nop 1\n"
    "v_max3_f32 v196, v196, v144, v145\n"
    "v_max3_f32 v196, v196, v146, v147\n"
    "v_max3_f32 v196, v196, v148, v149\n"
    "v_max3_f32 v196, v196, v150, v151\n"
    "v_max3_f32 v196, v196, v152, v153\n"
    "v_max3_f32 v196, v196, v154, v155\n"
    "v_max3_f32 v196, v196, v156, v157\n"
    "v_max3_f32 v196, v196, v158, v159\n"
    "v_mfma_f32_16x16x32_bf16 v[128:131], a[64:67], a[32:35], 0\n"
    "v_mfma_f32_16x16x32_bf16 v[128:131], a[68:71], a[36:39], v[128:131]\n"
    "v_mfma_f32_16x16x32_bf16 v[128:131], a[72:75], a[40:43], v[128:131]\n"
    "v_mfma_f32_16x16x32_bf16 v[128:131], a[76:79], a[44:47], v[128:131]\n"
    "v_mfma_f32_16x16x32_bf16 v[132:135], a[80:83], a[32:35], 0\n"
    "v_mfma_f32_16x16x32_bf16 v[132:135], a[84:87], a[36:39], v[132:135]\n"
    "v_mfma_f32_16x16x32_bf16 v[132:135], a[88:91], a[40:43], v[132:135]\n"
    "v_mfma_f32_16x16x32_bf16 v[132:135], a[92:95], a[44:47], v[132:135]\n"
    "v_mfma_f32_16x16x32_bf16 v[136:139], a[96:99], a[32:35], 0\n"
    "v_mfma_f32_16x16x32_bf16 v[136:139], a[100:103], a[36:39], v[136:139]\n"
    "v_mfma_f32_16x16x32_bf16 v[136:139], a[104:107], a[40:43], v[136:139]\n"
    "v_mfma_f32_16x16x32_bf16 v[136:139], a[108:111], a[44:47], v[136:139]\n"
    "v_mfma_f32_16x16x32_bf16 v[140:143], a[112:115], a[32:35], 0\n"
    "v_mfma_f32_16x16x32_bf16 v[140:143], a[116:119], a[36:39], v[140:143]\n"
    "v_mfma_f32_16x16x32_bf16 v[140:143], a[120:123], a[40:43], v[140:143]\n"
    "v_mfma_f32_16x16x32_bf16 v[140:143], a[124:127], a[44:47], v[140:143]\n"
    "s_nop 7\n"
    "s_nop 1\n"
    "v_max3_f32 v200, v200, v128, v129\n"
    "v_max3_f32 v200, v200, v130, v131\n"
    "v_max3_f32 v200, v200, v132, v133\n"
    "v_max3_f32 v200, v200, v134, v135\n"
    "v_max3_f32 v200, v200, v136, v137\n"
    "v_max3_f32 v200, v200, v138, v139\n"
    "v_max3_f32 v200, v200, v140, v141\n"
    "v_max3_f32 v200, v200, v142, v143\n"
    "v_mfma_f32_16x16x32_bf16 v[144:147], a[64:67], a[48:51], 0\n"
    "v_mfma_f32_16x16x32_bf16 v[144:147], a[68:71], a[52:55], v[144:147]\n"
    "v_mfma_f32_16x16x32_bf16 v[144:147], a[72:75], a[56:59], v[144:147]\n"
    "v_mfma_f32_16x16x32_bf16 v[144:147], a[76:79], a[60:63], v[144:147]\n"
    "v_mfma_f32_16x16x32_bf16 v[148:151], a[80:83], a[48:51], 0\n"
    "v_mfma_f32_16x16x32_bf16 v[148:151], a[84:87], a[52:55], v[148:151]\n"
    "v_mfma_f32_16x16x32_bf16 v[148:151], a[88:91], a[56:59], v[148:151]\n"
    "v_mfma_f32_16x16x32_bf16 v[148:151], a[92:95], a[60:63], v[148:151]\n"
    "v_mfma_f32_16x16x32_bf16 v[152:155], a[96:99], a[48:51], 0\n"
    "v_mfma_f32_16x16x32_bf16 v[152:155], a[100:103], a[52:55], v[152:155]\n"
    "v_mfma_f32_16x16x32_bf16 v[152:155], a[104:107], a[56:59], v[152:155]\n"
    "v_mfma_f32_16x16x32_bf16 v[152:155], a[108:111], a[60:63], v[152:155]\n"
    "v_mfma_f32_16x16x32_bf16 v[156:159], a[112:115], a[48:51], 0\n"
    "v_mfma_f32_16x16x32_bf16 v[156:159], a[116:119], a[52:55], v[156:159]\n"
    "v_mfma_f32_16x16x32_bf16 v[156:159], a[120:123], a[56:59], v[156:159]\n"
    "v_mfma_f32_16x16x32_bf16 v[156:159], a[124:127], a[60:63], v[156:159]\n"
    "s_nop 7\n"
    "s_nop 1\n"
    "v_max3_f32 v204, v204, v144, v145\n"
    "v_max3_f32 v204, v204, v146, v147\n"
    "v_max3_f32 v204, v204, v148, v149\n"
    "v_max3_f32 v204, v204, v150, v151\n"
    "v_max3_f32 v204, v204, v152, v153\n"
    "v_max3_f32 v204, v204, v154, v155\n"
    "v_max3_f32 v204, v204, v156, v157\n"
    "v_max3_f32 v204, v204, v158, v159\n"
    "s_add_u32 s72, s72, 0x4000\n"
    "s_cmp_lt_u32 s72, 524288\n"
    "s_cbranch_scc1 MAXL%=\n"
    "s_mov_b32 s72, 0\n"
    "s_mov_b32 m0, s74\n"
    "s_nop 0\n"
    "buffer_load_dwordx4 v214, s[40:43], s72 offen offset:0  lds\n"
    "buffer_load_dwordx4 v215, s[40:43], s72 offen offset:1024  lds\n"
    "buffer_load_dwordx4 v216, s[40:43], s72 offen offset:2048  lds\n"
    "buffer_load_dwordx4 v217, s[40:43], s72 offen offset:3072  lds\n"
    "s_mov_b32 m0, s76\n"
    "s_nop 0\n"
    "buffer_load_dwordx4 v218, s[44:47], s72 offen offset:0  lds\n"
    "buffer_load_dwordx4 v219, s[44:47], s72 offen offset:1024  lds\n"
    "buffer_load_dwordx4 v218, s[44:47], s72 offen offset:2048  lds\n"
    "buffer_load_dwordx4 v219, s[44:47], s72 offen offset:3072  lds\n"
    "s_mov_b32 s72, 16384\n"
    "s_mov_b32 m0, s75\n"
    "s_nop 0\n"
    "buffer_load_dwordx4 v214, s[40:43], s72 offen offset:0  lds\n"
    "buffer_load_dwordx4 v215, s[40:43], s72 offen offset:1024  lds\n"
    "buffer_load_dwordx4 v216, s[40:43], s72 offen offset:2048  lds\n"
    "buffer_load_dwordx4 v217, s[40:43], s72 offen offset:3072  lds\n"
    "s_mov_b32 m0, s77\n"
    "s_nop 0\n"
    "buffer_load_dwordx4 v218, s[44:47], s72 offen offset:0  lds\n"
    "buffer_load_dwordx4 v219, s[44:47], s72 offen offset:1024  lds\n"
    "buffer_load_dwordx4 v218, s[44:47], s72 offen offset:2048  lds\n"
    "buffer_load_dwordx4 v219, s[44:47], s72 offen offset:3072  lds\n"
    "s_mov_b32 s72, 0x8000\n"
    "v_mov_b32 v246, v192\n"
    "s_nop 1\n"
    "v_permlane32_swap_b32 v192, v246\n"
    "s_nop 1\n"
    "v_max_f32 v192, v192, v246\n"
    "v_mov_b32 v246, v192\n"
    "s_nop 1\n"
    "v_permlane16_swap_b32 v192, v246\n"
    "s_nop 1\n"
    "v_max_f32 v192, v192, v246\n"
    "v_sub_f32 v176, 0, v192\n"
    "v_sub_f32 v177, 0, v192\n"
    "v_sub_f32 v178, 0, v192\n"
    "v_sub_f32 v179, 0, v192\n"
    "v_mov_b32 v246, v196\n"
    "s_nop 1\n"
    "v_permlane32_swap_b32 v196, v246\n"
    "s_nop 1\n"
    "v_max_f32 v196, v196, v246\n"
    "v_mov_b32 v246, v196\n"
    "s_nop 1\n"
    "v_permlane16_swap_b32 v196, v246\n"
    "s_nop 1\n"
    "v_max_f32 v196, v196, v246\n"
    "v_sub_f32 v180, 0, v196\n"
    "v_sub_f32 v181, 0, v196\n"
    "v_sub_f32 v182, 0, v196\n"
    "v_sub_f32 v183, 0, v196\n"
    "v_mov_b32 v246, v200\n"
    "s_nop 1\n"
    "v_permlane32_swap_b32 v200, v246\n"
    "s_nop 1\n"
    "v_max_f32 v200, v200, v246\n"
    "v_mov_b32 v246, v200\n"
    "s_nop 1\n"
    "v_permlane16_swap_b32 v200, v246\n"
    "s_nop 1\n"
    "v_max_f32 v200, v200, v246\n"
    "v_sub_f32 v184, 0, v200\n"
    "v_sub_f32 v185, 0, v200\n"
    "v_sub_f32 v186, 0, v200\n"
    "v_sub_f32 v187, 0, v200\n"
    "v_mov_b32 v246, v204\n"
    "s_nop 1\n"
    "v_permlane32_swap_b32 v204, v246\n"
    "s_nop 1\n"
    "v_max_f32 v204, v204, v246\n"
    "v_mov_b32 v246, v204\n"
    "s_nop 1\n"
    "v_permlane16_swap_b32 v204, v246\n"
    "s_nop 1\n"
    "v_max_f32 v204, v204, v246\n"
    "v_sub_f32 v188, 0, v204\n"
    "v_sub_f32 v189, 0, v204\n"
    "v_sub_f32 v190, 0, v204\n"
    "v_sub_f32 v191, 0, v204\n"
    "v_mov_b32 v0, 0\n"
    "v_mov_b32 v1, 0\n"
    "v_mov_b32 v2, 0\n"
    "v_mov_b32 v3, 0\n"
    "v_mov_b32 v4, 0\n"
    "v_mov_b32 v5, 0\n"
    "v_mov_b32 v6, 0\n"
    "v_mov_b32 v7, 0\n"
    "v_mov_b32 v8, 0\n"
    "v_mov_b32 v9, 0\n"
    "v_mov_b32 v10, 0\n"
    "v_mov_b32 v11, 0\n"
    "v_mov_b32 v12, 0\n"
    "v_mov_b32 v13, 0\n"
    "v_mov_b32 v14, 0\n"
    "v_mov_b32 v15, 0\n"
    "v_mov_b32 v16, 0\n"
    "v_mov_b32 v17, 0\n"
    "v_mov_b32 v18, 0\n"
    "v_mov_b32 v19, 0\n"
    "v_mov_b32 v20, 0\n"
    "v_mov_b32 v21, 0\n"
    "v_mov_b32 v22, 0\n"
    "v_mov_b32 v23, 0\n"
    "v_mov_b32 v24, 0\n"
    "v_mov_b32 v25, 0\n"
    "v_mov_b32 v26, 0\n"
    "v_mov_b32 v27, 0\n"
    "v_mov_b32 v28, 0\n"
    "v_mov_b32 v29, 0\n"
    "v_mov_b32 v30, 0\n"
    "v_mov_b32 v31, 0\n"
    "v_mov_b32 v32, 0\n"
    "v_mov_b32 v33, 0\n"
    "v_mov_b32 v34, 0\n"
    "v_mov_b32 v35, 0\n"
    "v_mov_b32 v36, 0\n"
    "v_mov_b32 v37, 0\n"
    "v_mov_b32 v38, 0\n"
    "v_mov_b32 v39, 0\n"
    "v_mov_b32 v40, 0\n"
    "v_mov_b32 v41, 0\n"
    "v_mov_b32 v42, 0\n"
    "v_mov_b32 v43, 0\n"
    "v_mov_b32 v44, 0\n"
    "v_mov_b32 v45, 0\n"
    "v_mov_b32 v46, 0\n"
    "v_mov_b32 v47, 0\n"
    "v_mov_b32 v48, 0\n"
    "v_mov_b32 v49, 0\n"
    "v_mov_b32 v50, 0\n"
    "v_mov_b32 v51, 0\n"
    "v_mov_b32 v52, 0\n"
    "v_mov_b32 v53, 0\n"
    "v_mov_b32 v54, 0\n"
    "v_mov_b32 v55, 0\n"
    "v_mov_b32 v56, 0\n"
    "v_mov_b32 v57, 0\n"
    "v_mov_b32 v58, 0\n"
    "v_mov_b32 v59, 0\n"
    "v_mov_b32 v60, 0\n"
    "v_mov_b32 v61, 0\n"
    "v_mov_b32 v62, 0\n"
    "v_mov_b32 v63, 0\n"
    "v_mov_b32 v64, 0\n"
    "v_mov_b32 v65, 0\n"
    "v_mov_b32 v66, 0\n"
    "v_mov_b32 v67, 0\n"
    "v_mov_b32 v68, 0\n"
    "v_mov_b32 v69, 0\n"
    "v_mov_b32 v70, 0\n"
    "v_mov_b32 v71, 0\n"
    "v_mov_b32 v72, 0\n"
    "v_mov_b32 v73, 0\n"
    "v_mov_b32 v74, 0\n"
    "v_mov_b32 v75, 0\n"
    "v_mov_b32 v76, 0\n"
    "v_mov_b32 v77, 0\n"
    "v_mov_b32 v78, 0\n"
    "v_mov_b32 v79, 0\n"
    "v_mov_b32 v80, 0\n"
    "v_mov_b32 v81, 0\n"
    "v_mov_b32 v82, 0\n"
    "v_mov_b32 v83, 0\n"
    "v_mov_b32 v84, 0\n"
    "v_mov_b32 v85, 0\n"
    "v_mov_b32 v86, 0\n"
    "v_mov_b32 v87, 0\n"
    "v_mov_b32 v88, 0\n"
    "v_mov_b32 v89, 0\n"
    "v_mov_b32 v90, 0\n"
    "v_mov_b32 v91, 0\n"
    "v_mov_b32 v92, 0\n"
    "v_mov_b32 v93, 0\n"
    "v_mov_b32 v94, 0\n"
    "v_mov_b32 v95, 0\n"
    "v_mov_b32 v96, 0\n"
    "v_mov_b32 v97, 0\n"
    "v_mov_b32 v98, 0\n"
    "v_mov_b32 v99, 0\n"
    "v_mov_b32 v100, 0\n"
    "v_mov_b32 v101, 0\n"
    "v_mov_b32 v102, 0\n"
    "v_mov_b32 v103, 0\n"
    "v_mov_b32 v104, 0\n"
    "v_mov_b32 v105, 0\n"
    "v_mov_b32 v106, 0\n"
    "v_mov_b32 v107, 0\n"
    "v_mov_b32 v108, 0\n"
    "v_mov_b32 v109, 0\n"
    "v_mov_b32 v110, 0\n"
    "v_mov_b32 v111, 0\n"
    "v_mov_b32 v112, 0\n"
    "v_mov_b32 v113, 0\n"
    "v_mov_b32 v114, 0\n"
    "v_mov_b32 v115, 0\n"
    "v_mov_b32 v116, 0\n"
    "v_mov_b32 v117, 0\n"
    "v_mov_b32 v118, 0\n"
    "v_mov_b32 v119, 0\n"
    "v_mov_b32 v120, 0\n"
    "v_mov_b32 v121, 0\n"
    "v_mov_b32 v122, 0\n"
    "v_mov_b32 v123, 0\n"
    "v_mov_b32 v124, 0\n"
    "v_mov_b32 v125, 0\n"
    "v_mov_b32 v126, 0\n"
    "v_mov_b32 v127, 0\n"
    "v_mov_b32 v144, 0\n"
    "v_mov_b32 v145, 0\n"
    "v_mov_b32 v146, 0\n"
    "v_mov_b32 v147, 0\n"
    "v_mov_b32 v148, 0\n"
    "v_mov_b32 v149, 0\n"
    "v_mov_b32 v150, 0\n"
    "v_mov_b32 v151, 0\n"
    "v_mov_b32 v152, 0\n"
    "v_mov_b32 v153, 0\n"
    "v_mov_b32 v154, 0\n"
    "v_mov_b32 v155, 0\n"
    "v_mov_b32 v156, 0xff800000\n"
    "v_mov_b32 v157, 0xff800000\n"
    "v_mov_b32 v158, 0xff800000\n"
    "v_mov_b32 v159, 0xff800000\n"
    "v_mov_b32 v192, 0\n"
    "v_mov_b32 v160, 0\n"
    "v_mov_b32 v193, 0\n"
    "v_mov_b32 v161, 0\n"
    "v_mov_b32 v194, 0\n"
    "v_mov_b32 v162, 0\n"
    "v_mov_b32 v195, 0\n"
    "v_mov_b32 v163, 0\n"
    "v_mov_b32 v196, 0\n"
    "v_mov_b32 v164, 0\n"
    "v_mov_b32 v197, 0\n"
    "v_mov_b32 v165, 0\n"
    "v_mov_b32 v198, 0\n"
    "v_mov_b32 v166, 0\n"
    "v_mov_b32 v199, 0\n"
    "v_mov_b32 v167, 0\n"
    "v_mov_b32 v200, 0\n"
    "v_mov_b32 v168, 0\n"
    "v_mov_b32 v201, 0\n"
    "v_mov_b32 v169, 0\n"
    "v_mov_b32 v202, 0\n"
    "v_mov_b32 v170, 0\n"
    "v_mov_b32 v203, 0\n"
    "v_mov_b32 v171, 0\n"
    "v_mov_b32 v204, 0\n"
    "v_mov_b32 v172, 0\n"
    "v_mov_b32 v205, 0\n"
    "v_mov_b32 v173, 0\n"
    "v_mov_b32 v206, 0\n"
    "v_mov_b32 v174, 0\n"
    "v_mov_b32 v207, 0\n"
    "v_mov_b32 v175, 0\n"
    "v_accvgpr_write_b32 a192, 0\n"
    "v_accvgpr_write_b32 a193, 0\n"
    "v_accvgpr_write_b32 a194, 0\n"
    "v_accvgpr_write_b32 a195, 0\n"
    "v_accvgpr_write_b32 a196, 0\n"
    "v_accvgpr_write_b32 a197, 0\n"
    "v_accvgpr_write_b32 a198, 0\n"
    "v_accvgpr_write_b32 a199, 0\n"
    "v_accvgpr_write_b32 a200, 0\n"
    "v_accvgpr_write_b32 a201, 0\n"
    "v_accvgpr_write_b32 a202, 0\n"
    "v_accvgpr_write_b32 a203, 0\n"
    "v_accvgpr_write_b32 a204, 0\n"
    "v_accvgpr_write_b32 a205, 0\n"
    "v_accvgpr_write_b32 a206, 0\n"
    "v_accvgpr_write_b32 a207, 0\n"
    "v_accvgpr_write_b32 a208, 0\n"
    "v_accvgpr_write_b32 a209, 0\n"
    "v_accvgpr_write_b32 a210, 0\n"
    "v_accvgpr_write_b32 a211, 0\n"
    "v_accvgpr_write_b32 a212, 0\n"
    "v_accvgpr_write_b32 a213, 0\n"
    "v_accvgpr_write_b32 a214, 0\n"
    "v_accvgpr_write_b32 a215, 0\n"
    "v_accvgpr_write_b32 a216, 0\n"
    "v_accvgpr_write_b32 a217, 0\n"
    "v_accvgpr_write_b32 a218, 0\n"
    "v_accvgpr_write_b32 a219, 0\n"
    "v_accvgpr_write_b32 a220, 0\n"
    "v_accvgpr_write_b32 a221, 0\n"
    "v_accvgpr_write_b32 a222, 0\n"
    "v_accvgpr_write_b32 a223, 0\n"
    "v_accvgpr_write_b32 a224, 0\n"
    "v_accvgpr_write_b32 a225, 0\n"
    "v_accvgpr_write_b32 a226, 0\n"
    "v_accvgpr_write_b32 a227, 0\n"
    "v_accvgpr_write_b32 a228, 0\n"
    "v_accvgpr_write_b32 a229, 0\n"
    "v_accvgpr_write_b32 a230, 0\n"
    "v_accvgpr_write_b32 a231, 0\n"
    "v_accvgpr_write_b32 a232, 0\n"
    "v_accvgpr_write_b32 a233, 0\n"
    "v_accvgpr_write_b32 a234, 0\n"
    "v_accvgpr_write_b32 a235, 0\n"
    "v_accvgpr_write_b32 a236, 0\n"
    "v_accvgpr_write_b32 a237, 0\n"
    "v_accvgpr_write_b32 a238, 0\n"
    "v_accvgpr_write_b32 a239, 0\n"
    "v_accvgpr_write_b32 a240, 0\n"
    "v_accvgpr_write_b32 a241, 0\n"
    "v_accvgpr_write_b32 a242, 0\n"
    "v_accvgpr_write_b32 a243, 0\n"
    "v_accvgpr_write_b32 a244, 0\n"
    "v_accvgpr_write_b32 a245, 0\n"
    "v_accvgpr_write_b32 a246, 0\n"
    "v_accvgpr_write_b32 a247, 0\n"
    "v_accvgpr_write_b32 a248, 0\n"
    "v_accvgpr_write_b32 a249, 0\n"
    "v_accvgpr_write_b32 a250, 0\n"
    "v_accvgpr_write_b32 a251, 0\n"
    "v_accvgpr_write_b32 a252, 0\n"
    "v_accvgpr_write_b32 a253, 0\n"
    "v_accvgpr_write_b32 a254, 0\n"
    "v_accvgpr_write_b32 a255, 0\n"
    "s_waitcnt vmcnt(0)\n"
    "s_barrier\n"
    "ds_read_b128 a[64:67], v208 offset:0\n"
    "ds_read_b128 a[68:71], v209 offset:0\n"
    "ds_read_b128 a[72:75], v210 offset:0\n"
    "ds_read_b128 a[76:79], v211 offset:0\n"
    "ds_read_b128 a[80:83], v208 offset:4096\n"
    "ds_read_b128 a[84:87], v209 offset:4096\n"
    "ds_read_b128 a[88:91], v210 offset:4096\n"
    "ds_read_b128 a[92:95], v211 offset:4096\n"
    "ds_read_b128 a[96:99], v208 offset:8192\n"
    "ds_read_b128 a[100:103], v209 offset:8192\n"
    "ds_read_b128 a[104:107], v210 offset:8192\n"
    "ds_read_b128 a[108:111], v211 offset:8192\n"
    "ds_read_b128 a[112:115], v208 offset:12288\n"
    "ds_read_b128 a[116:119], v209 offset:12288\n"
    "ds_read_b128 a[120:123], v210 offset:12288\n"
    "ds_read_b128 a[124:127], v211 offset:12288\n"
    "ds_read_b64_tr_b16 a[128:129], v212 offset:16384\n"
    "ds_read_b64_tr_b16 a[130:131], v212 offset:20480\n"
    "ds_read_b64_tr_b16 a[132:133], v212 offset:24576\n"
    "ds_read_b64_tr_b16 a[134:135], v212 offset:28672\n"
    "ds_read_b64_tr_b16 a[136:137], v213 offset:16384\n"
    "ds_read_b64_tr_b16 a[138:139], v213 offset:20480\n"
    "ds_read_b64_tr_b16 a[140:141], v213 offset:24576\n"
    "ds_read_b64_tr_b16 a[142:143], v213 offset:28672\n"
    "ds_read_b64_tr_b16 a[144:145], v212 offset:16640\n"
    "ds_read_b64_tr_b16 a[146:147], v212 offset:20736\n"
    "ds_read_b64_tr_b16 a[148:149], v212 offset:24832\n"
    "ds_read_b64_tr_b16 a[150:151], v212 offset:28928\n"
    "ds_read_b64_tr_b16 a[152:153], v213 offset:16640\n"
    "ds_read_b64_tr_b16 a[154:155], v213 offset:20736\n"
    "ds_read_b64_tr_b16 a[156:157], v213 offset:24832\n"
    "ds_read_b64_tr_b16 a[158:159], v213 offset:28928\n"
    "ds_read_b64_tr_b16 a[160:161], v212 offset:16896\n"
    "ds_read_b64_tr_b16 a[162:163], v212 offset:20992\n"
    "ds_read_b64_tr_b16 a[164:165], v212 offset:25088\n"
    "ds_read_b64_tr_b16 a[166:167], v212 offset:29184\n"
    "ds_read_b64_tr_b16 a[168:169], v213 offset:16896\n"
    "ds_read_b64_tr_b16 a[170:171], v213 offset:20992\n"
    "ds_read_b64_tr_b16 a[172:173], v213 offset:25088\n"
    "ds_read_b64_tr_b16 a[174:175], v213 offset:29184\n"
    "ds_read_b64_tr_b16 a[176:177], v212 offset:17152\n"
    "ds_read_b64_tr_b16 a[178:179], v212 offset:21248\n"
    "ds_read_b64_tr_b16 a[180:181], v212 offset:25344\n"
    "ds_read_b64_tr_b16 a[182:183], v212 offset:29440\n"
    "ds_read_b64_tr_b16 a[184:185], v213 offset:17152\n"
    "ds_read_b64_tr_b16 a[186:187], v213 offset:21248\n"
    "ds_read_b64_tr_b16 a[188:189], v213 offset:25344\n"
    "ds_read_b64_tr_b16 a[190:191], v213 offset:29440\n"
    "s_waitcnt lgkmcnt(0)\n"
    "s_barrier\n"
    "s_branch BODY0%=\n"
    "KEND%=:\n"
  :: "s"(x), "s"(k), "s"(v), "s"(wq), "s"(wo), "s"(wr), "s"(wg), "s"(wu), "s"(wd), "s"(out), "s"(ws), "s"(c), "s"(wm), "s"(wid), "s"(blockIdx.x), "s"((unsigned)(size_t)lds)
  : "v0", "v1", "v2", "v3", "v4", "v5", "v6", "v7", "v8", "v9", "v10", "v11", "v12", "v13", "v14", "v15", "v16", "v17", "v18", "v19", "v20", "v21", "v22", "v23", "v24", "v25", "v26", "v27", "v28", "v29", "v30", "v31", "v32", "v33", "v34", "v35", "v36", "v37", "v38", "v39", "v40", "v41", "v42", "v43", "v44", "v45", "v46", "v47", "v48", "v49", "v50", "v51", "v52", "v53", "v54", "v55", "v56", "v57", "v58", "v59", "v60", "v61", "v62", "v63", "v64", "v65", "v66", "v67", "v68", "v69", "v70", "v71", "v72", "v73", "v74", "v75", "v76", "v77", "v78", "v79", "v80", "v81", "v82", "v83", "v84", "v85", "v86", "v87", "v88", "v89", "v90", "v91", "v92", "v93", "v94", "v95", "v96", "v97", "v98", "v99", "v100", "v101", "v102", "v103", "v104", "v105", "v106", "v107", "v108", "v109", "v110", "v111", "v112", "v113", "v114", "v115", "v116", "v117", "v118", "v119", "v120", "v121", "v122", "v123", "v124", "v125", "v126", "v127", "v128", "v129", "v130", "v131", "v132", "v133", "v134", "v135", "v136", "v137", "v138", "v139", "v140", "v141", "v142", "v143", "v144", "v145", "v146", "v147", "v148", "v149", "v150", "v151", "v152", "v153", "v154", "v155", "v156", "v157", "v158", "v159", "v160", "v161", "v162", "v163", "v164", "v165", "v166", "v167", "v168", "v169", "v170", "v171", "v172", "v173", "v174", "v175", "v176", "v177", "v178", "v179", "v180", "v181", "v182", "v183", "v184", "v185", "v186", "v187", "v188", "v189", "v190", "v191", "v192", "v193", "v194", "v195", "v196", "v197", "v198", "v199", "v200", "v201", "v202", "v203", "v204", "v205", "v206", "v207", "v208", "v209", "v210", "v211", "v212", "v213", "v214", "v215", "v216", "v217", "v218", "v219", "v220", "v221", "v222", "v223", "v224", "v225", "v226", "v227", "v228", "v229", "v230", "v231", "v232", "v233", "v234", "v235", "v236", "v237", "v238", "v239", "v240", "v241", "v242", "v243", "v244", "v245", "v246", "v247", "v248", "v249", "v250", "v251", "v252", "v253", "v254", "v255", "a0", "a1", "a2", "a3", "a4", "a5", "a6", "a7", "a8", "a9", "a10", "a11", "a12", "a13", "a14", "a15", "a16", "a17", "a18", "a19", "a20", "a21", "a22", "a23", "a24", "a25", "a26", "a27", "a28", "a29", "a30", "a31", "a32", "a33", "a34", "a35", "a36", "a37", "a38", "a39", "a40", "a41", "a42", "a43", "a44", "a45", "a46", "a47", "a48", "a49", "a50", "a51", "a52", "a53", "a54", "a55", "a56", "a57", "a58", "a59", "a60", "a61", "a62", "a63", "a64", "a65", "a66", "a67", "a68", "a69", "a70", "a71", "a72", "a73", "a74", "a75", "a76", "a77", "a78", "a79", "a80", "a81", "a82", "a83", "a84", "a85", "a86", "a87", "a88", "a89", "a90", "a91", "a92", "a93", "a94", "a95", "a96", "a97", "a98", "a99", "a100", "a101", "a102", "a103", "a104", "a105", "a106", "a107", "a108", "a109", "a110", "a111", "a112", "a113", "a114", "a115", "a116", "a117", "a118", "a119", "a120", "a121", "a122", "a123", "a124", "a125", "a126", "a127", "a128", "a129", "a130", "a131", "a132", "a133", "a134", "a135", "a136", "a137", "a138", "a139", "a140", "a141", "a142", "a143", "a144", "a145", "a146", "a147", "a148", "a149", "a150", "a151", "a152", "a153", "a154", "a155", "a156", "a157", "a158", "a159", "a160", "a161", "a162", "a163", "a164", "a165", "a166", "a167", "a168", "a169", "a170", "a171", "a172", "a173", "a174", "a175", "a176", "a177", "a178", "a179", "a180", "a181", "a182", "a183", "a184", "a185", "a186", "a187", "a188", "a189", "a190", "a191", "a192", "a193", "a194", "a195", "a196", "a197", "a198", "a199", "a200", "a201", "a202", "a203", "a204", "a205", "a206", "a207", "a208", "a209", "a210", "a211", "a212", "a213", "a214", "a215", "a216", "a217", "a218", "a219", "a220", "a221", "a222", "a223", "a224", "a225", "a226", "a227", "a228", "a229", "a230", "a231", "a232", "a233", "a234", "a235", "a236", "a237", "a238", "a239", "a240", "a241", "a242", "a243", "a244", "a245", "a246", "a247", "a248", "a249", "a250", "a251", "a252", "a253", "a254", "a255", "s36", "s37", "s38", "s39", "s40", "s41", "s42", "s43", "s44", "s45", "s46", "s47", "s48", "s49", "s50", "s51", "s52", "s53", "s54", "s55", "s56", "s57", "s58", "s59", "s60", "s61", "s62", "s63", "s64", "s65", "s66", "s67", "s68", "s69", "s70", "s71", "s72", "s73", "s74", "s75", "s76", "s77", "s78", "s79", "s80", "s81", "s82", "s83", "s84", "s85", "s86", "s87", "s88", "s89", "s90", "s91", "s92", "s93", "s94", "s95", "s96", "s97", "s98", "s99", "vcc", "scc", "memory");
}


extern "C" void kernel_launch(void* const* d_in, const int* in_sizes, int n_in, void* d_out, int out_size, void* d_ws, size_t ws_size, hipStream_t stream) {
  const u16* x = (const u16*)d_in[0]; const u16* kc = (const u16*)d_in[1]; const u16* vc = (const u16*)d_in[2];
  const u16* wq = (const u16*)d_in[3]; const u16* wo = (const u16*)d_in[4];
  const u16* wr = (const u16*)d_in[6]; const u16* wg = (const u16*)d_in[7]; const u16* wu = (const u16*)d_in[8]; const u16* wd = (const u16*)d_in[9];
  const u16* c = (const u16*)d_in[10]; const u16* wm = (const u16*)d_in[11];
  k_main<<<256, 256, 0, stream>>>(x, kc, vc, wq, wo, wr, wg, wu, wd, (u16*)d_out, (char*)d_ws, c, wm);
}
```
